```python
import torch

def _softmax(x, axis=-1):
    return torch.softmax(x, dim=axis)
def _gelu(x):
    return torch.nn.functional.gelu(x, approximate="tanh")
def _silu(x):
    return torch.nn.functional.silu(x)
def _sigmoid(x):
    return torch.sigmoid(x)
def _layernorm(x, axis=-1, eps=1e-5):
    m = x.mean(dim=axis, keepdim=True)
    v = x.var(dim=axis, keepdim=True, unbiased=False)
    return (x - m) / torch.sqrt(v + eps)
def _scatter_add(tgt, idx, src, axis=0):
    return tgt.clone().index_add_(axis, idx, src)
def _segment_sum(src, seg, n):
    out = torch.zeros((n,) + src.shape[1:], dtype=src.dtype)
    return out.index_add_(0, seg, src)
def _pad(x, pw):
    flat = tuple(v for pair in reversed(pw) for v in pair)
    return torch.nn.functional.pad(x, flat)

kernel_name = 'concat_heads__gqa_block_cached__gqa_block_cached__near_ridge'

def setup_inputs():

    _w = (lambda a: (lambda t: t.to(torch.bfloat16) if t.is_floating_point() else t)(torch.as_tensor(a)))
    def _A_setup():
        B, Sq, D = 8, 112, 448
        Hq, Hkv, Sk, Dh = 16, 8, 2048, 256
        assert Hq % Hkv == 0, (Hq, Hkv)
        return {
            "x": _w((lambda *s: torch.randn(*s, dtype=torch.bfloat16) * 0.8)(B, Sq, D)),
            "k_cache": _w((lambda *s: torch.randn(*s, dtype=torch.bfloat16) * 0.8)(B, Hkv, Sk, Dh)),
            "v_cache": _w((lambda *s: torch.randn(*s, dtype=torch.bfloat16) * 0.05)(B, Hkv, Sk, Dh)),
            "wq": _w((lambda *s: torch.randn(*s, dtype=torch.bfloat16) * 0.8)(D, Hq * Dh) * (D ** -0.5)),
            "wo": _w((lambda *s: torch.randn(*s, dtype=torch.bfloat16) * 0.05)(Hq * Dh, D) * ((Hq * Dh) ** -0.5)),
        }
    _A_in = _A_setup()

    def _B_setup():
        B, Sq, D = 8, 112, 448
        Hq, Hkv, Sk, Dh = 16, 8, 2048, 256
        assert Hq % Hkv == 0, (Hq, Hkv)
        return {
            "x": _w((lambda *s: torch.randn(*s, dtype=torch.bfloat16) * 0.8)(B, Sq, D)),
            "k_cache": _w((lambda *s: torch.randn(*s, dtype=torch.bfloat16) * 0.8)(B, Hkv, Sk, Dh)),
            "v_cache": _w((lambda *s: torch.randn(*s, dtype=torch.bfloat16) * 0.05)(B, Hkv, Sk, Dh)),
            "wq": _w((lambda *s: torch.randn(*s, dtype=torch.bfloat16) * 0.8)(D, Hq * Dh) * (D ** -0.5)),
            "wo": _w((lambda *s: torch.randn(*s, dtype=torch.bfloat16) * 0.05)(Hq * Dh, D) * ((Hq * Dh) ** -0.5)),
        }
    _B_in = _B_setup()

    return {
        "A_x": _A_in["x"],
        "A_k_cache": _A_in["k_cache"],
        "A_v_cache": _A_in["v_cache"],
        "A_wq": _A_in["wq"],
        "A_wo": _A_in["wo"],
        "B_x": _B_in["x"],
        "B_k_cache": _B_in["k_cache"],
        "B_v_cache": _B_in["v_cache"],
        "B_wq": _B_in["wq"],
        "B_wo": _B_in["wo"],
    }

def reference(A_x, A_k_cache, A_v_cache, A_wq, A_wo, B_x, B_k_cache, B_v_cache, B_wq, B_wo):
    def _A_ref(x, k_cache, v_cache, wq, wo):
        B, Sq, D = x.shape
        Hq, Dh = 16, 256
        Hkv = k_cache.shape[1]
        rep = Hq // Hkv
        q = (lambda x, *d: x.permute(*d))((x @ wq).reshape(B, Sq, Hq, Dh), 0, 2, 1, 3)
        k = (lambda x, n, axis: torch.repeat_interleave(x, n, dim=axis))(k_cache, rep, axis=1)
        v = (lambda x, n, axis: torch.repeat_interleave(x, n, dim=axis))(v_cache, rep, axis=1)
        scores = torch.einsum("bhqd,bhkd->bhqk", q, k) * (Dh ** -0.5)
        p = _softmax(scores, axis=-1)
        o = torch.einsum("bhqk,bhkd->bhqd", p, v)
        return (lambda x, *d: x.permute(*d))(o, 0, 2, 1, 3).reshape(B, Sq, Hq * Dh) @ wo

    def _B_ref(x, k_cache, v_cache, wq, wo):
        B, Sq, D = x.shape
        Hq, Dh = 16, 256
        Hkv = k_cache.shape[1]
        rep = Hq // Hkv
        q = (lambda x, *d: x.permute(*d))((x @ wq).reshape(B, Sq, Hq, Dh), 0, 2, 1, 3)
        k = (lambda x, n, axis: torch.repeat_interleave(x, n, dim=axis))(k_cache, rep, axis=1)
        v = (lambda x, n, axis: torch.repeat_interleave(x, n, dim=axis))(v_cache, rep, axis=1)
        scores = torch.einsum("bhqd,bhkd->bhqk", q, k) * (Dh ** -0.5)
        p = _softmax(scores, axis=-1)
        o = torch.einsum("bhqk,bhkd->bhqd", p, v)
        return (lambda x, *d: x.permute(*d))(o, 0, 2, 1, 3).reshape(B, Sq, Hq * Dh) @ wo

    a = _A_ref(A_x, A_k_cache, A_v_cache, A_wq, A_wo)
    b = _B_ref(B_x, B_k_cache, B_v_cache, B_wq, B_wo)
    return torch.concatenate([a, b], axis=-1)
```

```cpp
#include <hip/hip_runtime.h>
#include <hip/hip_bf16.h>
#include <stdint.h>

constexpr unsigned PART_UNITS = 100;
constexpr size_t FLAGS_OFF = (size_t)32 << 20;

__global__ __launch_bounds__(512, 2) void attn_fused(const char* __restrict__ xA, const char* __restrict__ kA, const char* __restrict__ vA,
                                                     const char* __restrict__ wqA, const char* __restrict__ woA,
                                                     const char* __restrict__ xB, const char* __restrict__ kB, const char* __restrict__ vB,
                                                     const char* __restrict__ wqB, const char* __restrict__ woB, char* __restrict__ ws, char* __restrict__ out) {
  __shared__ __attribute__((aligned(16))) char lds[163840];
  const unsigned bid = blockIdx.x;
  const unsigned kvh = bid & 7u, j = bid >> 3;
  const unsigned br = j >> 4, b = (j >> 1) & 7u, hq = j & 1u, h = 2u * kvh + hq;
  const char* x = (br ? xB : xA) + (size_t)b * (112 * 896);
  const char* wq = (br ? wqB : wqA) + (size_t)h * 512;
  const char* k = (br ? kB : kA) + (size_t)(b * 8u + kvh) * 1048576;
  const char* v = (br ? vB : vA) + (size_t)(b * 8u + kvh) * 1048576;
  const char* wo = (br ? woB : woA) + (size_t)h * (256 * 896);
  const unsigned g = br * 8u + b;
  char* gpart = ws + (size_t)g * PART_UNITS * 16 * 1024;
  char* flags = ws + FLAGS_OFF + (size_t)g * 16384;
  char* outp = out + (size_t)b * (112 * 1792) + br * 896u;
  const unsigned wave = __builtin_amdgcn_readfirstlane(threadIdx.x >> 6);
  const unsigned rsum = b + 3u * hq;
  const unsigned rot = __builtin_amdgcn_readfirstlane(rsum >= 7u ? rsum - 7u : rsum);
  const unsigned t0 = (0u * (g * 8u + kvh)) & 31u;
  const unsigned pk = __builtin_amdgcn_readfirstlane(wave | (hq << 4) | (h << 8) | (rot << 16) | (t0 << 24));
  asm volatile("" :: "s"((unsigned)(size_t)lds));
  asm volatile(
"s_mov_b32 s91, 0\n"
"L_start:\n"
"s_bfe_u32 s86, %8, 0x40000\n"
"s_and_b32 s87, s86, 3\n"
"s_mov_b64 s[40:41], %1\n"
"s_and_b32 s41, s41, 0xffff\n"
"s_mov_b32 s42, 3662336\n"
"s_mov_b32 s43, 0x20000\n"
"s_mov_b64 s[36:37], %0\n"
"s_and_b32 s37, s37, 0xffff\n"
"s_mov_b32 s38, 100352\n"
"s_mov_b32 s39, 0x20000\n"
"s_lshr_b32 s84, s86, 1\n"
"s_and_b32 s84, s84, 3\n"
"s_lshl_b32 s84, s84, 3\n"
"s_and_b32 s85, s86, 1\n"
"s_lshl_b32 s85, s85, 1\n"
"s_add_u32 s84, s84, s85\n"
"s_add_u32 s85, s84, 0\n"
"s_lshl_b32 s76, s85, 13\n"
"s_add_u32 s85, s86, 0\n"
"s_mul_i32 s72, s85, 1088\n"
"s_add_u32 s85, s84, 4\n"
"s_lshl_b32 s77, s85, 13\n"
"s_add_u32 s85, s86, 8\n"
"s_mul_i32 s73, s85, 1088\n"
"s_add_u32 s85, s84, 32\n"
"s_lshl_b32 s78, s85, 13\n"
"s_add_u32 s85, s86, 16\n"
"s_mul_i32 s74, s85, 1088\n"
"s_add_u32 s85, s84, 36\n"
"s_lshl_b32 s79, s85, 13\n"
"s_add_u32 s85, s86, 24\n"
"s_mul_i32 s75, s85, 1088\n"
"s_min_u32 s84, s86, 6\n"
"s_mul_i32 s80, s84, 14336\n"
"s_add_u32 s81, s80, 64\n"
"s_lshl_b32 s82, s84, 11\n"
"s_add_u32 s82, s82, 104448\n"
"v_mbcnt_lo_u32_b32 v0, -1, 0\n"
"v_mbcnt_hi_u32_b32 v0, -1, v0\n"
"v_lshrrev_b32 v1, 5, v0\n"
"v_and_b32 v2, 31, v0\n"
"v_lshlrev_b32 v3, 1, v1\n"
"v_xor_b32 v2, v2, v3\n"
"v_lshlrev_b32 v2, 4, v2\n"
"v_lshl_add_u32 v106, v1, 13, v2\n"
"v_and_b32 v4, 15, v0\n"
"v_lshrrev_b32 v5, 4, v0\n"
"v_lshrrev_b32 v6, 2, v0\n"
"v_mul_u32_u24 v7, 896, v6\n"
"v_lshrrev_b32 v6, 5, v0\n"
"v_lshlrev_b32 v6, 1, v6\n"
"v_and_b32 v8, 3, v0\n"
"v_xor_b32 v6, v6, v8\n"
"v_lshl_add_u32 v107, v6, 4, v7\n"
"s_mov_b32 s90, 0\n"
"s_mov_b32 s89, s90\n"
"s_lshl_b32 s84, s90, 19\n"
"s_lshl_b32 s85, s90, 7\n"
"v_add_u32 v100, s84, v106\n"
"v_add_u32 v101, s85, v107\n"
"s_add_u32 m0, s72, 0\n"
"s_nop 0\n"
"buffer_load_dwordx4 v100, s[40:43], s76 offen lds\n"
"s_add_u32 m0, s73, 0\n"
"s_nop 0\n"
"buffer_load_dwordx4 v100, s[40:43], s77 offen lds\n"
"s_add_u32 m0, s74, 0\n"
"s_nop 0\n"
"buffer_load_dwordx4 v100, s[40:43], s78 offen lds\n"
"s_add_u32 m0, s75, 0\n"
"s_nop 0\n"
"buffer_load_dwordx4 v100, s[40:43], s79 offen lds\n"
"s_add_u32 s90, s90, 1\n"
"s_cmp_eq_u32 s90, 7\n"
"s_cselect_b32 s90, 0, s90\n"
"s_lshl_b32 s84, s90, 19\n"
"v_add_u32 v100, s84, v106\n"
"s_add_u32 m0, s82, 0\n"
"s_nop 0\n"
"buffer_load_dwordx4 v101, s[36:39], s80 offen lds\n"
"s_add_u32 m0, s82, 1024\n"
"s_nop 0\n"
"buffer_load_dwordx4 v101, s[36:39], s81 offen lds\n"
"s_add_u32 s89, s89, 1\n"
"s_cmp_eq_u32 s89, 7\n"
"s_cselect_b32 s89, 0, s89\n"
"s_lshl_b32 s85, s89, 7\n"
"v_add_u32 v101, s85, v107\n"
"s_add_u32 m0, s82, 14336\n"
"s_nop 0\n"
"buffer_load_dwordx4 v101, s[36:39], s80 offen lds\n"
"s_add_u32 m0, s82, 15360\n"
"s_nop 0\n"
"buffer_load_dwordx4 v101, s[36:39], s81 offen lds\n"
"s_add_u32 s89, s89, 1\n"
"s_cmp_eq_u32 s89, 7\n"
"s_cselect_b32 s89, 0, s89\n"
"s_lshl_b32 s85, s89, 7\n"
"v_add_u32 v101, s85, v107\n"
"s_mov_b64 s[56:57], %6\n"
"s_and_b32 s57, s57, 0xffff\n"
"s_mov_b32 s58, 8448\n"
"s_mov_b32 s59, 0x20000\n"
"v_mov_b32 v108, 8192\n"
"buffer_load_dword v108, v108, s[56:59], 0 offen sc1\n"
"s_add_u32 m0, s72, 34816\n"
"s_nop 0\n"
"buffer_load_dwordx4 v100, s[40:43], s76 offen lds\n"
"s_add_u32 m0, s73, 34816\n"
"s_nop 0\n"
"buffer_load_dwordx4 v100, s[40:43], s77 offen lds\n"
"s_add_u32 m0, s74, 34816\n"
"s_nop 0\n"
"buffer_load_dwordx4 v100, s[40:43], s78 offen lds\n"
"s_add_u32 m0, s75, 34816\n"
"s_nop 0\n"
"buffer_load_dwordx4 v100, s[40:43], s79 offen lds\n"
"s_add_u32 s90, s90, 1\n"
"s_cmp_eq_u32 s90, 7\n"
"s_cselect_b32 s90, 0, s90\n"
"s_lshl_b32 s84, s90, 19\n"
"v_add_u32 v100, s84, v106\n"
"s_add_u32 m0, s82, 28672\n"
"s_nop 0\n"
"buffer_load_dwordx4 v101, s[36:39], s80 offen lds\n"
"s_add_u32 m0, s82, 29696\n"
"s_nop 0\n"
"buffer_load_dwordx4 v101, s[36:39], s81 offen lds\n"
"s_add_u32 s89, s89, 1\n"
"s_cmp_eq_u32 s89, 7\n"
"s_cselect_b32 s89, 0, s89\n"
"s_lshl_b32 s85, s89, 7\n"
"v_add_u32 v101, s85, v107\n"
"s_mov_b64 s[48:49], %4\n"
"s_and_b32 s49, s49, 0xffff\n"
"s_mov_b32 s50, 229376\n"
"s_mov_b32 s51, 0x20000\n"
"s_bfe_u32 s84, %8, 0x40004\n"
"s_mul_i32 s84, s84, 0\n"
"s_xor_b32 s85, s84, 0\n"
"s_lshl_b32 s85, s85, 2\n"
"s_add_u32 s85, s85, s87\n"
"s_mul_i32 s56, s85, 1088\n"
"s_lshl_b32 s64, s85, 10\n"
"s_xor_b32 s85, s84, 1\n"
"s_lshl_b32 s85, s85, 2\n"
"s_add_u32 s85, s85, s87\n"
"s_mul_i32 s57, s85, 1088\n"
"s_lshl_b32 s65, s85, 10\n"
"s_xor_b32 s85, s84, 2\n"
"s_lshl_b32 s85, s85, 2\n"
"s_add_u32 s85, s85, s87\n"
"s_mul_i32 s58, s85, 1088\n"
"s_lshl_b32 s66, s85, 10\n"
"s_xor_b32 s85, s84, 3\n"
"s_lshl_b32 s85, s85, 2\n"
"s_add_u32 s85, s85, s87\n"
"s_mul_i32 s59, s85, 1088\n"
"s_lshl_b32 s67, s85, 10\n"
"s_xor_b32 s85, s84, 4\n"
"s_lshl_b32 s85, s85, 2\n"
"s_add_u32 s85, s85, s87\n"
"s_mul_i32 s60, s85, 1088\n"
"s_lshl_b32 s68, s85, 10\n"
"s_xor_b32 s85, s84, 5\n"
"s_lshl_b32 s85, s85, 2\n"
"s_add_u32 s85, s85, s87\n"
"s_mul_i32 s61, s85, 1088\n"
"s_lshl_b32 s69, s85, 10\n"
"s_xor_b32 s85, s84, 6\n"
"s_lshl_b32 s85, s85, 2\n"
"s_add_u32 s85, s85, s87\n"
"s_mul_i32 s62, s85, 1088\n"
"s_lshl_b32 s70, s85, 10\n"
"s_xor_b32 s85, s84, 7\n"
"s_lshl_b32 s85, s85, 2\n"
"s_add_u32 s85, s85, s87\n"
"s_mul_i32 s63, s85, 1088\n"
"s_lshl_b32 s71, s85, 10\n"
"v_lshl_add_u32 v249, v1, 9, v2\n"
"v_lshrrev_b32 v7, 1, v4\n"
"s_lshl_b32 s84, s87, 3\n"
"v_add_u32 v7, s84, v7\n"
"v_mul_u32_u24 v7, 1088, v7\n"
"v_and_b32 v8, 1, v4\n"
"v_lshl_add_u32 v7, v8, 9, v7\n"
"v_lshlrev_b32 v8, 1, v8\n"
"v_xor_b32 v8, v5, v8\n"
"v_lshl_add_u32 v244, v8, 4, v7\n"
"v_lshrrev_b32 v7, 2, v0\n"
"v_and_b32 v7, 3, v7\n"
"v_lshrrev_b32 v8, 1, v7\n"
"v_lshl_add_u32 v8, v5, 1, v8\n"
"v_mul_u32_u24 v8, 1088, v8\n"
"v_and_b32 v7, 1, v7\n"
"v_lshl_add_u32 v8, v7, 9, v8\n"
"v_and_b32 v9, 3, v0\n"
"v_lshl_add_u32 v8, v9, 3, v8\n"
"s_lshl_b32 s84, s87, 7\n"
"v_add_u32 v8, s84, v8\n"
"v_lshl_add_u32 v102, v7, 5, v8\n"
"v_xor_b32 v7, 1, v7\n"
"v_lshl_add_u32 v103, v7, 5, v8\n"
"v_add_u32 v245, 69632, v102\n"
"v_add_u32 v246, 69632, v103\n"
"v_lshlrev_b32 v7, 4, v0\n"
"v_add_u32 v248, 139264, v7\n"
"v_add_u32 v109, 104448, v7\n"
"v_lshrrev_b32 v8, 3, v4\n"
"v_lshlrev_b32 v8, 1, v8\n"
"v_xor_b32 v8, v5, v8\n"
"v_lshlrev_b32 v8, 4, v8\n"
"v_lshl_add_u32 v8, v4, 6, v8\n"
"v_add_u32 v104, 104448, v8\n"
"s_lshr_b32 s84, s87, 1\n"
"s_lshl_b32 s84, s84, 10\n"
"s_and_b32 s85, s87, 1\n"
"s_lshl_b32 s85, s85, 3\n"
"s_add_u32 s84, s84, s85\n"
"v_add_u32 v247, s84, v248\n"
"v_lshrrev_b32 v7, 1, v5\n"
"v_lshlrev_b32 v7, 8, v7\n"
"v_lshl_add_u32 v7, v4, 4, v7\n"
"v_and_b32 v8, 1, v5\n"
"v_lshl_add_u32 v7, v8, 3, v7\n"
"s_lshl_b32 s84, s87, 11\n"
"s_add_u32 s84, s84, 104448\n"
"v_add_u32 v105, s84, v7\n"
"v_mov_b64 v[128:129], 0\n"
"v_mov_b64 v[130:131], 0\n"
"v_mov_b64 v[132:133], 0\n"
"v_mov_b64 v[134:135], 0\n"
"v_mov_b64 v[136:137], 0\n"
"v_mov_b64 v[138:139], 0\n"
"v_mov_b64 v[140:141], 0\n"
"v_mov_b64 v[142:143], 0\n"
"v_mov_b64 v[144:145], 0\n"
"v_mov_b64 v[146:147], 0\n"
"v_mov_b64 v[148:149], 0\n"
"v_mov_b64 v[150:151], 0\n"
"v_mov_b64 v[152:153], 0\n"
"v_mov_b64 v[154:155], 0\n"
"v_mov_b64 v[156:157], 0\n"
"v_mov_b64 v[158:159], 0\n"
"v_mov_b64 v[160:161], 0\n"
"v_mov_b64 v[162:163], 0\n"
"v_mov_b64 v[164:165], 0\n"
"v_mov_b64 v[166:167], 0\n"
"v_mov_b64 v[168:169], 0\n"
"v_mov_b64 v[170:171], 0\n"
"v_mov_b64 v[172:173], 0\n"
"v_mov_b64 v[174:175], 0\n"
"v_mov_b64 v[176:177], 0\n"
"v_mov_b64 v[178:179], 0\n"
"v_mov_b64 v[180:181], 0\n"
"v_mov_b64 v[182:183], 0\n"
"v_mov_b64 v[184:185], 0\n"
"v_mov_b64 v[186:187], 0\n"
"v_mov_b64 v[188:189], 0\n"
"v_mov_b64 v[190:191], 0\n"
"v_mov_b32 v7, 163584\n"
"v_mov_b32 v8, 0\n"
"ds_write_b32 v7, v8\n"
"s_waitcnt vmcnt(6) lgkmcnt(0)\n"
"s_barrier\n"
"s_cmp_lt_u32 s86, 4\n"
"s_cbranch_scc0 L_roleB\n"
"L_roleA:\n"
"s_mov_b64 s[44:45], %3\n"
"s_and_b32 s45, s45, 0xffff\n"
"s_mov_b32 s46, 1048576\n"
"s_mov_b32 s47, 0x20000\n"
"ds_read_b128 v[208:211], v104 offset:0\n"
"ds_read_b128 v[212:215], v104 offset:1024\n"
"ds_read_b128 v[216:219], v104 offset:2048\n"
"ds_read_b128 v[220:223], v104 offset:3072\n"
"ds_read_b128 v[224:227], v104 offset:4096\n"
"ds_read_b128 v[228:231], v104 offset:5120\n"
"ds_read_b128 v[232:235], v104 offset:6144\n"
"ds_read_b128 v[236:239], v104 offset:7168\n"
"ds_read_b64_tr_b16 v[192:193], v102 offset:0\n"
"ds_read_b64_tr_b16 v[194:195], v102 offset:8704\n"
"ds_read_b64_tr_b16 v[196:197], v102 offset:17408\n"
"ds_read_b64_tr_b16 v[198:199], v102 offset:26112\n"
"ds_read_b64_tr_b16 v[200:201], v103 offset:0\n"
"ds_read_b64_tr_b16 v[202:203], v103 offset:8704\n"
"ds_read_b64_tr_b16 v[204:205], v103 offset:17408\n"
"ds_read_b64_tr_b16 v[206:207], v103 offset:26112\n"
"s_waitcnt lgkmcnt(6)\n"
"v_mfma_f32_16x16x32_bf16 v[128:131], v[192:195], v[208:211], v[128:131]\n"
"s_waitcnt lgkmcnt(4)\n"
"v_mfma_f32_16x16x32_bf16 v[128:131], v[196:199], v[212:215], v[128:131]\n"
"v_mfma_f32_16x16x32_bf16 v[144:147], v[192:195], v[216:219], v[144:147]\n"
"v_mfma_f32_16x16x32_bf16 v[144:147], v[196:199], v[220:223], v[144:147]\n"
"v_mfma_f32_16x16x32_bf16 v[160:163], v[192:195], v[224:227], v[160:163]\n"
"v_mfma_f32_16x16x32_bf16 v[160:163], v[196:199], v[228:231], v[160:163]\n"
"v_mfma_f32_16x16x32_bf16 v[176:179], v[192:195], v[232:235], v[176:179]\n"
"s_add_u32 m0, s72, 69632\n"
"s_nop 0\n"
"buffer_load_dwordx4 v100, s[40:43], s76 offen lds\n"
"v_mfma_f32_16x16x32_bf16 v[176:179], v[196:199], v[236:239], v[176:179]\n"
"ds_read_b64_tr_b16 v[192:193], v102 offset:64\n"
"ds_read_b64_tr_b16 v[194:195], v102 offset:8768\n"
"ds_read_b64_tr_b16 v[196:197], v102 offset:17472\n"
"ds_read_b64_tr_b16 v[198:199], v102 offset:26176\n"
"s_waitcnt lgkmcnt(6)\n"
"v_mfma_f32_16x16x32_bf16 v[132:135], v[200:203], v[208:211], v[132:135]\n"
"s_waitcnt lgkmcnt(4)\n"
"v_mfma_f32_16x16x32_bf16 v[132:135], v[204:207], v[212:215], v[132:135]\n"
"s_add_u32 m0, s73, 69632\n"
"s_nop 0\n"
"buffer_load_dwordx4 v100, s[40:43], s77 offen lds\n"
"v_mfma_f32_16x16x32_bf16 v[148:151], v[200:203], v[216:219], v[148:151]\n"
"v_mfma_f32_16x16x32_bf16 v[148:151], v[204:207], v[220:223], v[148:151]\n"
"v_mfma_f32_16x16x32_bf16 v[164:167], v[200:203], v[224:227], v[164:167]\n"
"s_add_u32 m0, s74, 69632\n"
"s_nop 0\n"
"buffer_load_dwordx4 v100, s[40:43], s78 offen lds\n"
"v_mfma_f32_16x16x32_bf16 v[164:167], v[204:207], v[228:231], v[164:167]\n"
"v_mfma_f32_16x16x32_bf16 v[180:183], v[200:203], v[232:235], v[180:183]\n"
"v_mfma_f32_16x16x32_bf16 v[180:183], v[204:207], v[236:239], v[180:183]\n"
"ds_read_b64_tr_b16 v[200:201], v103 offset:64\n"
"ds_read_b64_tr_b16 v[202:203], v103 offset:8768\n"
"ds_read_b64_tr_b16 v[204:205], v103 offset:17472\n"
"ds_read_b64_tr_b16 v[206:207], v103 offset:26176\n"
"s_add_u32 m0, s75, 69632\n"
"s_nop 0\n"
"buffer_load_dwordx4 v100, s[40:43], s79 offen lds\n"
"s_add_u32 s90, s90, 1\n"
"s_cmp_eq_u32 s90, 7\n"
"s_cselect_b32 s90, 0, s90\n"
"s_lshl_b32 s84, s90, 19\n"
"v_add_u32 v100, s84, v106\n"
"s_waitcnt lgkmcnt(6)\n"
"v_mfma_f32_16x16x32_bf16 v[136:139], v[192:195], v[208:211], v[136:139]\n"
"ds_read_b128 v[0:3], v104 offset:14336\n"
"s_waitcnt lgkmcnt(5)\n"
"v_mfma_f32_16x16x32_bf16 v[136:139], v[196:199], v[212:215], v[136:139]\n"
"ds_read_b128 v[4:7], v104 offset:15360\n"
"v_mfma_f32_16x16x32_bf16 v[152:155], v[192:195], v[216:219], v[152:155]\n"
"ds_read_b128 v[8:11], v104 offset:16384\n"
"s_add_u32 m0, s82, 43008\n"
"s_nop 0\n"
"buffer_load_dwordx4 v101, s[36:39], s80 offen lds\n"
"v_mfma_f32_16x16x32_bf16 v[152:155], v[196:199], v[220:223], v[152:155]\n"
"ds_read_b128 v[12:15], v104 offset:17408\n"
"v_mfma_f32_16x16x32_bf16 v[168:171], v[192:195], v[224:227], v[168:171]\n"
"ds_read_b128 v[16:19], v104 offset:18432\n"
"v_mfma_f32_16x16x32_bf16 v[168:171], v[196:199], v[228:231], v[168:171]\n"
"ds_read_b128 v[20:23], v104 offset:19456\n"
"s_add_u32 m0, s82, 44032\n"
"s_nop 0\n"
"buffer_load_dwordx4 v101, s[36:39], s81 offen lds\n"
"s_add_u32 s89, s89, 1\n"
"s_cmp_eq_u32 s89, 7\n"
"s_cselect_b32 s89, 0, s89\n"
"s_lshl_b32 s85, s89, 7\n"
"v_add_u32 v101, s85, v107\n"
"v_mfma_f32_16x16x32_bf16 v[184:187], v[192:195], v[232:235], v[184:187]\n"
"ds_read_b128 v[24:27], v104 offset:20480\n"
"v_mfma_f32_16x16x32_bf16 v[184:187], v[196:199], v[236:239], v[184:187]\n"
"ds_read_b128 v[28:31], v104 offset:21504\n"
"s_waitcnt lgkmcnt(10)\n"
"v_mfma_f32_16x16x32_bf16 v[140:143], v[200:203], v[208:211], v[140:143]\n"
"s_waitcnt lgkmcnt(8)\n"
"v_mfma_f32_16x16x32_bf16 v[140:143], v[204:207], v[212:215], v[140:143]\n"
"v_mfma_f32_16x16x32_bf16 v[156:159], v[200:203], v[216:219], v[156:159]\n"
"v_mfma_f32_16x16x32_bf16 v[156:159], v[204:207], v[220:223], v[156:159]\n"
"s_waitcnt lgkmcnt(0)\n"
"v_readfirstlane_b32 s88, v108\n"
"s_nop 1\n"
"s_add_u32 s88, s88, 1\n"
"s_waitcnt vmcnt(6)\n"
"s_barrier\n"
"v_mfma_f32_16x16x32_bf16 v[172:175], v[200:203], v[224:227], v[172:175]\n"
"ds_read_b64_tr_b16 v[192:193], v102 offset:34816\n"
"v_mfma_f32_16x16x32_bf16 v[172:175], v[204:207], v[228:231], v[172:175]\n"
"ds_read_b64_tr_b16 v[194:195], v102 offset:43520\n"
"v_mfma_f32_16x16x32_bf16 v[188:191], v[200:203], v[232:235], v[188:191]\n"
"ds_read_b64_tr_b16 v[196:197], v102 offset:52224\n"
"v_mfma_f32_16x16x32_bf16 v[188:191], v[204:207], v[236:239], v[188:191]\n"
"ds_read_b64_tr_b16 v[198:199], v102 offset:60928\n"
"ds_read_b64_tr_b16 v[200:201], v103 offset:34816\n"
"ds_read_b64_tr_b16 v[202:203], v103 offset:43520\n"
"ds_read_b64_tr_b16 v[204:205], v103 offset:52224\n"
"ds_read_b64_tr_b16 v[206:207], v103 offset:60928\n"
"s_waitcnt lgkmcnt(6)\n"
"v_mfma_f32_16x16x32_bf16 v[128:131], v[192:195], v[0:3], v[128:131]\n"
"s_waitcnt lgkmcnt(4)\n"
"v_mfma_f32_16x16x32_bf16 v[128:131], v[196:199], v[4:7], v[128:131]\n"
"v_mfma_f32_16x16x32_bf16 v[144:147], v[192:195], v[8:11], v[144:147]\n"
"v_mfma_f32_16x16x32_bf16 v[144:147], v[196:199], v[12:15], v[144:147]\n"
"v_mfma_f32_16x16x32_bf16 v[160:163], v[192:195], v[16:19], v[160:163]\n"
"v_mfma_f32_16x16x32_bf16 v[160:163], v[196:199], v[20:23], v[160:163]\n"
"v_mfma_f32_16x16x32_bf16 v[176:179], v[192:195], v[24:27], v[176:179]\n"
"s_add_u32 m0, s72, 0\n"
"s_nop 0\n"
"buffer_load_dwordx4 v100, s[40:43], s76 offen lds\n"
"v_mfma_f32_16x16x32_bf16 v[176:179], v[196:199], v[28:31], v[176:179]\n"
"ds_read_b64_tr_b16 v[192:193], v102 offset:34880\n"
"ds_read_b64_tr_b16 v[194:195], v102 offset:43584\n"
"ds_read_b64_tr_b16 v[196:197], v102 offset:52288\n"
"ds_read_b64_tr_b16 v[198:199], v102 offset:60992\n"
"s_waitcnt lgkmcnt(6)\n"
"v_mfma_f32_16x16x32_bf16 v[132:135], v[200:203], v[0:3], v[132:135]\n"
"s_waitcnt lgkmcnt(4)\n"
"v_mfma_f32_16x16x32_bf16 v[132:135], v[204:207], v[4:7], v[132:135]\n"
"s_add_u32 m0, s73, 0\n"
"s_nop 0\n"
"buffer_load_dwordx4 v100, s[40:43], s77 offen lds\n"
"v_mfma_f32_16x16x32_bf16 v[148:151], v[200:203], v[8:11], v[148:151]\n"
"v_mfma_f32_16x16x32_bf16 v[148:151], v[204:207], v[12:15], v[148:151]\n"
"v_mfma_f32_16x16x32_bf16 v[164:167], v[200:203], v[16:19], v[164:167]\n"
"s_add_u32 m0, s74, 0\n"
"s_nop 0\n"
"buffer_load_dwordx4 v100, s[40:43], s78 offen lds\n"
"v_mfma_f32_16x16x32_bf16 v[164:167], v[204:207], v[20:23], v[164:167]\n"
"v_mfma_f32_16x16x32_bf16 v[180:183], v[200:203], v[24:27], v[180:183]\n"
"v_mfma_f32_16x16x32_bf16 v[180:183], v[204:207], v[28:31], v[180:183]\n"
"ds_read_b64_tr_b16 v[200:201], v103 offset:34880\n"
"ds_read_b64_tr_b16 v[202:203], v103 offset:43584\n"
"ds_read_b64_tr_b16 v[204:205], v103 offset:52288\n"
"ds_read_b64_tr_b16 v[206:207], v103 offset:60992\n"
"s_add_u32 m0, s75, 0\n"
"s_nop 0\n"
"buffer_load_dwordx4 v100, s[40:43], s79 offen lds\n"
"s_add_u32 s90, s90, 1\n"
"s_cmp_eq_u32 s90, 7\n"
"s_cselect_b32 s90, 0, s90\n"
"s_lshl_b32 s84, s90, 19\n"
"v_add_u32 v100, s84, v106\n"
"s_waitcnt lgkmcnt(6)\n"
"v_mfma_f32_16x16x32_bf16 v[136:139], v[192:195], v[0:3], v[136:139]\n"
"ds_read_b128 v[208:211], v104 offset:28672\n"
"s_waitcnt lgkmcnt(5)\n"
"v_mfma_f32_16x16x32_bf16 v[136:139], v[196:199], v[4:7], v[136:139]\n"
"ds_read_b128 v[212:215], v104 offset:29696\n"
"v_mfma_f32_16x16x32_bf16 v[152:155], v[192:195], v[8:11], v[152:155]\n"
"ds_read_b128 v[216:219], v104 offset:30720\n"
"s_add_u32 m0, s82, 0\n"
"s_nop 0\n"
"buffer_load_dwordx4 v101, s[36:39], s80 offen lds\n"
"v_mfma_f32_16x16x32_bf16 v[152:155], v[196:199], v[12:15], v[152:155]\n"
"ds_read_b128 v[220:223], v104 offset:31744\n"
"v_mfma_f32_16x16x32_bf16 v[168:171], v[192:195], v[16:19], v[168:171]\n"
"ds_read_b128 v[224:227], v104 offset:32768\n"
"v_mfma_f32_16x16x32_bf16 v[168:171], v[196:199], v[20:23], v[168:171]\n"
"ds_read_b128 v[228:231], v104 offset:33792\n"
"s_add_u32 m0, s82, 1024\n"
"s_nop 0\n"
"buffer_load_dwordx4 v101, s[36:39], s81 offen lds\n"
"s_add_u32 s89, s89, 1\n"
"s_cmp_eq_u32 s89, 7\n"
"s_cselect_b32 s89, 0, s89\n"
"s_lshl_b32 s85, s89, 7\n"
"v_add_u32 v101, s85, v107\n"
"v_mfma_f32_16x16x32_bf16 v[184:187], v[192:195], v[24:27], v[184:187]\n"
"ds_read_b128 v[232:235], v104 offset:34816\n"
"v_mfma_f32_16x16x32_bf16 v[184:187], v[196:199], v[28:31], v[184:187]\n"
"ds_read_b128 v[236:239], v104 offset:35840\n"
"s_waitcnt lgkmcnt(10)\n"
"v_mfma_f32_16x16x32_bf16 v[140:143], v[200:203], v[0:3], v[140:143]\n"
"s_waitcnt lgkmcnt(8)\n"
"v_mfma_f32_16x16x32_bf16 v[140:143], v[204:207], v[4:7], v[140:143]\n"
"v_mfma_f32_16x16x32_bf16 v[156:159], v[200:203], v[8:11], v[156:159]\n"
"v_mfma_f32_16x16x32_bf16 v[156:159], v[204:207], v[12:15], v[156:159]\n"
"s_waitcnt lgkmcnt(0)\n"
"s_waitcnt vmcnt(6)\n"
"s_barrier\n"
"v_mfma_f32_16x16x32_bf16 v[172:175], v[200:203], v[16:19], v[172:175]\n"
"ds_read_b64_tr_b16 v[192:193], v245 offset:0\n"
"v_mfma_f32_16x16x32_bf16 v[172:175], v[204:207], v[20:23], v[172:175]\n"
"ds_read_b64_tr_b16 v[194:195], v245 offset:8704\n"
"v_mfma_f32_16x16x32_bf16 v[188:191], v[200:203], v[24:27], v[188:191]\n"
"ds_read_b64_tr_b16 v[196:197], v245 offset:17408\n"
"v_mfma_f32_16x16x32_bf16 v[188:191], v[204:207], v[28:31], v[188:191]\n"
"ds_read_b64_tr_b16 v[198:199], v245 offset:26112\n"
"ds_read_b64_tr_b16 v[200:201], v246 offset:0\n"
"ds_read_b64_tr_b16 v[202:203], v246 offset:8704\n"
"ds_read_b64_tr_b16 v[204:205], v246 offset:17408\n"
"ds_read_b64_tr_b16 v[206:207], v246 offset:26112\n"
"s_waitcnt lgkmcnt(6)\n"
"v_mfma_f32_16x16x32_bf16 v[128:131], v[192:195], v[208:211], v[128:131]\n"
"s_waitcnt lgkmcnt(4)\n"
"v_mfma_f32_16x16x32_bf16 v[128:131], v[196:199], v[212:215], v[128:131]\n"
"v_mfma_f32_16x16x32_bf16 v[144:147], v[192:195], v[216:219], v[144:147]\n"
"v_mfma_f32_16x16x32_bf16 v[144:147], v[196:199], v[220:223], v[144:147]\n"
"v_mfma_f32_16x16x32_bf16 v[160:163], v[192:195], v[224:227], v[160:163]\n"
"v_mfma_f32_16x16x32_bf16 v[160:163], v[196:199], v[228:231], v[160:163]\n"
"v_mfma_f32_16x16x32_bf16 v[176:179], v[192:195], v[232:235], v[176:179]\n"
"s_add_u32 m0, s72, 34816\n"
"s_nop 0\n"
"buffer_load_dwordx4 v100, s[40:43], s76 offen lds\n"
"v_mfma_f32_16x16x32_bf16 v[176:179], v[196:199], v[236:239], v[176:179]\n"
"ds_read_b64_tr_b16 v[192:193], v245 offset:64\n"
"ds_read_b64_tr_b16 v[194:195], v245 offset:8768\n"
"ds_read_b64_tr_b16 v[196:197], v245 offset:17472\n"
"ds_read_b64_tr_b16 v[198:199], v245 offset:26176\n"
"s_waitcnt lgkmcnt(6)\n"
"v_mfma_f32_16x16x32_bf16 v[132:135], v[200:203], v[208:211], v[132:135]\n"
"s_waitcnt lgkmcnt(4)\n"
"v_mfma_f32_16x16x32_bf16 v[132:135], v[204:207], v[212:215], v[132:135]\n"
"s_add_u32 m0, s73, 34816\n"
"s_nop 0\n"
"buffer_load_dwordx4 v100, s[40:43], s77 offen lds\n"
"v_mfma_f32_16x16x32_bf16 v[148:151], v[200:203], v[216:219], v[148:151]\n"
"v_mfma_f32_16x16x32_bf16 v[148:151], v[204:207], v[220:223], v[148:151]\n"
"v_mfma_f32_16x16x32_bf16 v[164:167], v[200:203], v[224:227], v[164:167]\n"
"s_add_u32 m0, s74, 34816\n"
"s_nop 0\n"
"buffer_load_dwordx4 v100, s[40:43], s78 offen lds\n"
"v_mfma_f32_16x16x32_bf16 v[164:167], v[204:207], v[228:231], v[164:167]\n"
"v_mfma_f32_16x16x32_bf16 v[180:183], v[200:203], v[232:235], v[180:183]\n"
"v_mfma_f32_16x16x32_bf16 v[180:183], v[204:207], v[236:239], v[180:183]\n"
"ds_read_b64_tr_b16 v[200:201], v246 offset:64\n"
"ds_read_b64_tr_b16 v[202:203], v246 offset:8768\n"
"ds_read_b64_tr_b16 v[204:205], v246 offset:17472\n"
"ds_read_b64_tr_b16 v[206:207], v246 offset:26176\n"
"s_add_u32 m0, s75, 34816\n"
"s_nop 0\n"
"buffer_load_dwordx4 v100, s[40:43], s79 offen lds\n"
"s_add_u32 s90, s90, 1\n"
"s_cmp_eq_u32 s90, 7\n"
"s_cselect_b32 s90, 0, s90\n"
"s_lshl_b32 s84, s90, 19\n"
"v_add_u32 v100, s84, v106\n"
"s_waitcnt lgkmcnt(6)\n"
"v_mfma_f32_16x16x32_bf16 v[136:139], v[192:195], v[208:211], v[136:139]\n"
"ds_read_b128 v[0:3], v104 offset:43008\n"
"s_waitcnt lgkmcnt(5)\n"
"v_mfma_f32_16x16x32_bf16 v[136:139], v[196:199], v[212:215], v[136:139]\n"
"ds_read_b128 v[4:7], v104 offset:44032\n"
"v_mfma_f32_16x16x32_bf16 v[152:155], v[192:195], v[216:219], v[152:155]\n"
"ds_read_b128 v[8:11], v104 offset:45056\n"
"s_add_u32 m0, s82, 14336\n"
"s_nop 0\n"
"buffer_load_dwordx4 v101, s[36:39], s80 offen lds\n"
"v_mfma_f32_16x16x32_bf16 v[152:155], v[196:199], v[220:223], v[152:155]\n"
"ds_read_b128 v[12:15], v104 offset:46080\n"
"v_mfma_f32_16x16x32_bf16 v[168:171], v[192:195], v[224:227], v[168:171]\n"
"ds_read_b128 v[16:19], v104 offset:47104\n"
"v_mfma_f32_16x16x32_bf16 v[168:171], v[196:199], v[228:231], v[168:171]\n"
"ds_read_b128 v[20:23], v104 offset:48128\n"
"s_add_u32 m0, s82, 15360\n"
"s_nop 0\n"
"buffer_load_dwordx4 v101, s[36:39], s81 offen lds\n"
"s_add_u32 s89, s89, 1\n"
"s_cmp_eq_u32 s89, 7\n"
"s_cselect_b32 s89, 0, s89\n"
"s_lshl_b32 s85, s89, 7\n"
"v_add_u32 v101, s85, v107\n"
"v_mfma_f32_16x16x32_bf16 v[184:187], v[192:195], v[232:235], v[184:187]\n"
"ds_read_b128 v[24:27], v104 offset:49152\n"
"v_mfma_f32_16x16x32_bf16 v[184:187], v[196:199], v[236:239], v[184:187]\n"
"ds_read_b128 v[28:31], v104 offset:50176\n"
"s_waitcnt lgkmcnt(10)\n"
"v_mfma_f32_16x16x32_bf16 v[140:143], v[200:203], v[208:211], v[140:143]\n"
"s_waitcnt lgkmcnt(8)\n"
"v_mfma_f32_16x16x32_bf16 v[140:143], v[204:207], v[212:215], v[140:143]\n"
"v_mfma_f32_16x16x32_bf16 v[156:159], v[200:203], v[216:219], v[156:159]\n"
"v_mfma_f32_16x16x32_bf16 v[156:159], v[204:207], v[220:223], v[156:159]\n"
"s_waitcnt lgkmcnt(0)\n"
"s_waitcnt vmcnt(6)\n"
"s_barrier\n"
"v_mfma_f32_16x16x32_bf16 v[172:175], v[200:203], v[224:227], v[172:175]\n"
"ds_read_b64_tr_b16 v[192:193], v102 offset:0\n"
"v_mfma_f32_16x16x32_bf16 v[172:175], v[204:207], v[228:231], v[172:175]\n"
"ds_read_b64_tr_b16 v[194:195], v102 offset:8704\n"
"v_mfma_f32_16x16x32_bf16 v[188:191], v[200:203], v[232:235], v[188:191]\n"
"ds_read_b64_tr_b16 v[196:197], v102 offset:17408\n"
"v_mfma_f32_16x16x32_bf16 v[188:191], v[204:207], v[236:239], v[188:191]\n"
"ds_read_b64_tr_b16 v[198:199], v102 offset:26112\n"
"ds_read_b64_tr_b16 v[200:201], v103 offset:0\n"
"ds_read_b64_tr_b16 v[202:203], v103 offset:8704\n"
"ds_read_b64_tr_b16 v[204:205], v103 offset:17408\n"
"ds_read_b64_tr_b16 v[206:207], v103 offset:26112\n"
"s_waitcnt lgkmcnt(6)\n"
"v_mfma_f32_16x16x32_bf16 v[128:131], v[192:195], v[0:3], v[128:131]\n"
"s_waitcnt lgkmcnt(4)\n"
"v_mfma_f32_16x16x32_bf16 v[128:131], v[196:199], v[4:7], v[128:131]\n"
"v_mfma_f32_16x16x32_bf16 v[144:147], v[192:195], v[8:11], v[144:147]\n"
"v_mfma_f32_16x16x32_bf16 v[144:147], v[196:199], v[12:15], v[144:147]\n"
"v_mfma_f32_16x16x32_bf16 v[160:163], v[192:195], v[16:19], v[160:163]\n"
"v_mfma_f32_16x16x32_bf16 v[160:163], v[196:199], v[20:23], v[160:163]\n"
"v_mfma_f32_16x16x32_bf16 v[176:179], v[192:195], v[24:27], v[176:179]\n"
"s_add_u32 m0, s72, 69632\n"
"s_nop 0\n"
"buffer_load_dwordx4 v100, s[40:43], s76 offen lds\n"
"v_mfma_f32_16x16x32_bf16 v[176:179], v[196:199], v[28:31], v[176:179]\n"
"ds_read_b64_tr_b16 v[192:193], v102 offset:64\n"
"ds_read_b64_tr_b16 v[194:195], v102 offset:8768\n"
"ds_read_b64_tr_b16 v[196:197], v102 offset:17472\n"
"ds_read_b64_tr_b16 v[198:199], v102 offset:26176\n"
"s_waitcnt lgkmcnt(6)\n"
"v_mfma_f32_16x16x32_bf16 v[132:135], v[200:203], v[0:3], v[132:135]\n"
"s_waitcnt lgkmcnt(4)\n"
"v_mfma_f32_16x16x32_bf16 v[132:135], v[204:207], v[4:7], v[132:135]\n"
"s_add_u32 m0, s73, 69632\n"
"s_nop 0\n"
"buffer_load_dwordx4 v100, s[40:43], s77 offen lds\n"
"v_mfma_f32_16x16x32_bf16 v[148:151], v[200:203], v[8:11], v[148:151]\n"
"v_mfma_f32_16x16x32_bf16 v[148:151], v[204:207], v[12:15], v[148:151]\n"
"v_mfma_f32_16x16x32_bf16 v[164:167], v[200:203], v[16:19], v[164:167]\n"
"s_add_u32 m0, s74, 69632\n"
"s_nop 0\n"
"buffer_load_dwordx4 v100, s[40:43], s78 offen lds\n"
"v_mfma_f32_16x16x32_bf16 v[164:167], v[204:207], v[20:23], v[164:167]\n"
"v_mfma_f32_16x16x32_bf16 v[180:183], v[200:203], v[24:27], v[180:183]\n"
"v_mfma_f32_16x16x32_bf16 v[180:183], v[204:207], v[28:31], v[180:183]\n"
"ds_read_b64_tr_b16 v[200:201], v103 offset:64\n"
"ds_read_b64_tr_b16 v[202:203], v103 offset:8768\n"
"ds_read_b64_tr_b16 v[204:205], v103 offset:17472\n"
"ds_read_b64_tr_b16 v[206:207], v103 offset:26176\n"
"s_add_u32 m0, s75, 69632\n"
"s_nop 0\n"
"buffer_load_dwordx4 v100, s[40:43], s79 offen lds\n"
"s_add_u32 s90, s90, 1\n"
"s_cmp_eq_u32 s90, 7\n"
"s_cselect_b32 s90, 0, s90\n"
"s_lshl_b32 s84, s90, 19\n"
"v_add_u32 v100, s84, v106\n"
"s_waitcnt lgkmcnt(6)\n"
"v_mfma_f32_16x16x32_bf16 v[136:139], v[192:195], v[0:3], v[136:139]\n"
"ds_read_b128 v[208:211], v104 offset:0\n"
"s_waitcnt lgkmcnt(5)\n"
"v_mfma_f32_16x16x32_bf16 v[136:139], v[196:199], v[4:7], v[136:139]\n"
"ds_read_b128 v[212:215], v104 offset:1024\n"
"v_mfma_f32_16x16x32_bf16 v[152:155], v[192:195], v[8:11], v[152:155]\n"
"ds_read_b128 v[216:219], v104 offset:2048\n"
"s_add_u32 m0, s82, 28672\n"
"s_nop 0\n"
"buffer_load_dwordx4 v101, s[36:39], s80 offen lds\n"
"v_mfma_f32_16x16x32_bf16 v[152:155], v[196:199], v[12:15], v[152:155]\n"
"ds_read_b128 v[220:223], v104 offset:3072\n"
"v_mfma_f32_16x16x32_bf16 v[168:171], v[192:195], v[16:19], v[168:171]\n"
"ds_read_b128 v[224:227], v104 offset:4096\n"
"v_mfma_f32_16x16x32_bf16 v[168:171], v[196:199], v[20:23], v[168:171]\n"
"ds_read_b128 v[228:231], v104 offset:5120\n"
"s_add_u32 m0, s82, 29696\n"
"s_nop 0\n"
"buffer_load_dwordx4 v101, s[36:39], s81 offen lds\n"
"s_add_u32 s89, s89, 1\n"
"s_cmp_eq_u32 s89, 7\n"
"s_cselect_b32 s89, 0, s89\n"
"s_lshl_b32 s85, s89, 7\n"
"v_add_u32 v101, s85, v107\n"
"v_mfma_f32_16x16x32_bf16 v[184:187], v[192:195], v[24:27], v[184:187]\n"
"ds_read_b128 v[232:235], v104 offset:6144\n"
"v_mfma_f32_16x16x32_bf16 v[184:187], v[196:199], v[28:31], v[184:187]\n"
"ds_read_b128 v[236:239], v104 offset:7168\n"
"s_waitcnt lgkmcnt(10)\n"
"v_mfma_f32_16x16x32_bf16 v[140:143], v[200:203], v[0:3], v[140:143]\n"
"s_waitcnt lgkmcnt(8)\n"
"v_mfma_f32_16x16x32_bf16 v[140:143], v[204:207], v[4:7], v[140:143]\n"
"v_mfma_f32_16x16x32_bf16 v[156:159], v[200:203], v[8:11], v[156:159]\n"
"v_mfma_f32_16x16x32_bf16 v[156:159], v[204:207], v[12:15], v[156:159]\n"
"s_waitcnt lgkmcnt(0)\n"
"s_waitcnt vmcnt(6)\n"
"s_barrier\n"
"v_mfma_f32_16x16x32_bf16 v[172:175], v[200:203], v[16:19], v[172:175]\n"
"ds_read_b64_tr_b16 v[192:193], v102 offset:34816\n"
"v_mfma_f32_16x16x32_bf16 v[172:175], v[204:207], v[20:23], v[172:175]\n"
"ds_read_b64_tr_b16 v[194:195], v102 offset:43520\n"
"v_mfma_f32_16x16x32_bf16 v[188:191], v[200:203], v[24:27], v[188:191]\n"
"ds_read_b64_tr_b16 v[196:197], v102 offset:52224\n"
"v_mfma_f32_16x16x32_bf16 v[188:191], v[204:207], v[28:31], v[188:191]\n"
"ds_read_b64_tr_b16 v[198:199], v102 offset:60928\n"
"ds_read_b64_tr_b16 v[200:201], v103 offset:34816\n"
"ds_read_b64_tr_b16 v[202:203], v103 offset:43520\n"
"ds_read_b64_tr_b16 v[204:205], v103 offset:52224\n"
"ds_read_b64_tr_b16 v[206:207], v103 offset:60928\n"
"s_waitcnt lgkmcnt(6)\n"
"v_mfma_f32_16x16x32_bf16 v[128:131], v[192:195], v[208:211], v[128:131]\n"
"s_waitcnt lgkmcnt(4)\n"
"v_mfma_f32_16x16x32_bf16 v[128:131], v[196:199], v[212:215], v[128:131]\n"
"v_mfma_f32_16x16x32_bf16 v[144:147], v[192:195], v[216:219], v[144:147]\n"
"v_mfma_f32_16x16x32_bf16 v[144:147], v[196:199], v[220:223], v[144:147]\n"
"v_mfma_f32_16x16x32_bf16 v[160:163], v[192:195], v[224:227], v[160:163]\n"
"v_mfma_f32_16x16x32_bf16 v[160:163], v[196:199], v[228:231], v[160:163]\n"
"v_mfma_f32_16x16x32_bf16 v[176:179], v[192:195], v[232:235], v[176:179]\n"
"s_add_u32 m0, s72, 0\n"
"s_nop 0\n"
"buffer_load_dwordx4 v100, s[40:43], s76 offen lds\n"
"v_mfma_f32_16x16x32_bf16 v[176:179], v[196:199], v[236:239], v[176:179]\n"
"ds_read_b64_tr_b16 v[192:193], v102 offset:34880\n"
"ds_read_b64_tr_b16 v[194:195], v102 offset:43584\n"
"ds_read_b64_tr_b16 v[196:197], v102 offset:52288\n"
"ds_read_b64_tr_b16 v[198:199], v102 offset:60992\n"
"s_waitcnt lgkmcnt(6)\n"
"v_mfma_f32_16x16x32_bf16 v[132:135], v[200:203], v[208:211], v[132:135]\n"
"s_waitcnt lgkmcnt(4)\n"
"v_mfma_f32_16x16x32_bf16 v[132:135], v[204:207], v[212:215], v[132:135]\n"
"s_add_u32 m0, s73, 0\n"
"s_nop 0\n"
"buffer_load_dwordx4 v100, s[40:43], s77 offen lds\n"
"v_mfma_f32_16x16x32_bf16 v[148:151], v[200:203], v[216:219], v[148:151]\n"
"v_mfma_f32_16x16x32_bf16 v[148:151], v[204:207], v[220:223], v[148:151]\n"
"v_mfma_f32_16x16x32_bf16 v[164:167], v[200:203], v[224:227], v[164:167]\n"
"s_add_u32 m0, s74, 0\n"
"s_nop 0\n"
"buffer_load_dwordx4 v100, s[40:43], s78 offen lds\n"
"v_mfma_f32_16x16x32_bf16 v[164:167], v[204:207], v[228:231], v[164:167]\n"
"v_mfma_f32_16x16x32_bf16 v[180:183], v[200:203], v[232:235], v[180:183]\n"
"v_mfma_f32_16x16x32_bf16 v[180:183], v[204:207], v[236:239], v[180:183]\n"
"ds_read_b64_tr_b16 v[200:201], v103 offset:34880\n"
"ds_read_b64_tr_b16 v[202:203], v103 offset:43584\n"
"ds_read_b64_tr_b16 v[204:205], v103 offset:52288\n"
"ds_read_b64_tr_b16 v[206:207], v103 offset:60992\n"
"s_add_u32 m0, s75, 0\n"
"s_nop 0\n"
"buffer_load_dwordx4 v100, s[40:43], s79 offen lds\n"
"s_add_u32 s90, s90, 1\n"
"s_cmp_eq_u32 s90, 7\n"
"s_cselect_b32 s90, 0, s90\n"
"s_lshl_b32 s84, s90, 19\n"
"v_add_u32 v100, s84, v106\n"
"s_waitcnt lgkmcnt(6)\n"
"v_mfma_f32_16x16x32_bf16 v[136:139], v[192:195], v[208:211], v[136:139]\n"
"ds_read_b128 v[0:3], v104 offset:14336\n"
"s_waitcnt lgkmcnt(5)\n"
"v_mfma_f32_16x16x32_bf16 v[136:139], v[196:199], v[212:215], v[136:139]\n"
"ds_read_b128 v[4:7], v104 offset:15360\n"
"v_mfma_f32_16x16x32_bf16 v[152:155], v[192:195], v[216:219], v[152:155]\n"
"ds_read_b128 v[8:11], v104 offset:16384\n"
"v_mfma_f32_16x16x32_bf16 v[152:155], v[196:199], v[220:223], v[152:155]\n"
"ds_read_b128 v[12:15], v104 offset:17408\n"
"v_mfma_f32_16x16x32_bf16 v[168:171], v[192:195], v[224:227], v[168:171]\n"
"ds_read_b128 v[16:19], v104 offset:18432\n"
"v_mfma_f32_16x16x32_bf16 v[168:171], v[196:199], v[228:231], v[168:171]\n"
"ds_read_b128 v[20:23], v104 offset:19456\n"
"v_mfma_f32_16x16x32_bf16 v[184:187], v[192:195], v[232:235], v[184:187]\n"
"ds_read_b128 v[24:27], v104 offset:20480\n"
"v_mfma_f32_16x16x32_bf16 v[184:187], v[196:199], v[236:239], v[184:187]\n"
"ds_read_b128 v[28:31], v104 offset:21504\n"
"s_waitcnt lgkmcnt(10)\n"
"v_mfma_f32_16x16x32_bf16 v[140:143], v[200:203], v[208:211], v[140:143]\n"
"s_waitcnt lgkmcnt(8)\n"
"v_mfma_f32_16x16x32_bf16 v[140:143], v[204:207], v[212:215], v[140:143]\n"
"v_mfma_f32_16x16x32_bf16 v[156:159], v[200:203], v[216:219], v[156:159]\n"
"v_mfma_f32_16x16x32_bf16 v[156:159], v[204:207], v[220:223], v[156:159]\n"
"s_waitcnt lgkmcnt(0)\n"
"s_waitcnt vmcnt(4)\n"
"s_barrier\n"
"v_mfma_f32_16x16x32_bf16 v[172:175], v[200:203], v[224:227], v[172:175]\n"
"ds_read_b64_tr_b16 v[192:193], v245 offset:0\n"
"v_mfma_f32_16x16x32_bf16 v[172:175], v[204:207], v[228:231], v[172:175]\n"
"ds_read_b64_tr_b16 v[194:195], v245 offset:8704\n"
"v_mfma_f32_16x16x32_bf16 v[188:191], v[200:203], v[232:235], v[188:191]\n"
"ds_read_b64_tr_b16 v[196:197], v245 offset:17408\n"
"v_mfma_f32_16x16x32_bf16 v[188:191], v[204:207], v[236:239], v[188:191]\n"
"ds_read_b64_tr_b16 v[198:199], v245 offset:26112\n"
"ds_read_b64_tr_b16 v[200:201], v246 offset:0\n"
"ds_read_b64_tr_b16 v[202:203], v246 offset:8704\n"
"ds_read_b64_tr_b16 v[204:205], v246 offset:17408\n"
"ds_read_b64_tr_b16 v[206:207], v246 offset:26112\n"
"s_waitcnt lgkmcnt(6)\n"
"v_mfma_f32_16x16x32_bf16 v[128:131], v[192:195], v[0:3], v[128:131]\n"
"s_waitcnt lgkmcnt(4)\n"
"v_mfma_f32_16x16x32_bf16 v[128:131], v[196:199], v[4:7], v[128:131]\n"
"v_mfma_f32_16x16x32_bf16 v[144:147], v[192:195], v[8:11], v[144:147]\n"
"v_mfma_f32_16x16x32_bf16 v[144:147], v[196:199], v[12:15], v[144:147]\n"
"v_mfma_f32_16x16x32_bf16 v[160:163], v[192:195], v[16:19], v[160:163]\n"
"v_mfma_f32_16x16x32_bf16 v[160:163], v[196:199], v[20:23], v[160:163]\n"
"v_mfma_f32_16x16x32_bf16 v[176:179], v[192:195], v[24:27], v[176:179]\n"
"v_mfma_f32_16x16x32_bf16 v[176:179], v[196:199], v[28:31], v[176:179]\n"
"ds_read_b64_tr_b16 v[192:193], v245 offset:64\n"
"ds_read_b64_tr_b16 v[194:195], v245 offset:8768\n"
"ds_read_b64_tr_b16 v[196:197], v245 offset:17472\n"
"ds_read_b64_tr_b16 v[198:199], v245 offset:26176\n"
"s_waitcnt lgkmcnt(6)\n"
"v_mfma_f32_16x16x32_bf16 v[132:135], v[200:203], v[0:3], v[132:135]\n"
"s_waitcnt lgkmcnt(4)\n"
"v_mfma_f32_16x16x32_bf16 v[132:135], v[204:207], v[4:7], v[132:135]\n"
"v_mfma_f32_16x16x32_bf16 v[148:151], v[200:203], v[8:11], v[148:151]\n"
"v_mfma_f32_16x16x32_bf16 v[148:151], v[204:207], v[12:15], v[148:151]\n"
"v_mfma_f32_16x16x32_bf16 v[164:167], v[200:203], v[16:19], v[164:167]\n"
"v_mfma_f32_16x16x32_bf16 v[164:167], v[204:207], v[20:23], v[164:167]\n"
"v_mfma_f32_16x16x32_bf16 v[180:183], v[200:203], v[24:27], v[180:183]\n"
"v_mfma_f32_16x16x32_bf16 v[180:183], v[204:207], v[28:31], v[180:183]\n"
"ds_read_b64_tr_b16 v[200:201], v246 offset:64\n"
"ds_read_b64_tr_b16 v[202:203], v246 offset:8768\n"
"ds_read_b64_tr_b16 v[204:205], v246 offset:17472\n"
"ds_read_b64_tr_b16 v[206:207], v246 offset:26176\n"
"s_waitcnt lgkmcnt(6)\n"
"v_mfma_f32_16x16x32_bf16 v[136:139], v[192:195], v[0:3], v[136:139]\n"
"ds_read_b128 v[208:211], v104 offset:28672\n"
"s_waitcnt lgkmcnt(5)\n"
"v_mfma_f32_16x16x32_bf16 v[136:139], v[196:199], v[4:7], v[136:139]\n"
"ds_read_b128 v[212:215], v104 offset:29696\n"
"v_mfma_f32_16x16x32_bf16 v[152:155], v[192:195], v[8:11], v[152:155]\n"
"ds_read_b128 v[216:219], v104 offset:30720\n"
"v_mfma_f32_16x16x32_bf16 v[152:155], v[196:199], v[12:15], v[152:155]\n"
"ds_read_b128 v[220:223], v104 offset:31744\n"
"v_mfma_f32_16x16x32_bf16 v[168:171], v[192:195], v[16:19], v[168:171]\n"
"ds_read_b128 v[224:227], v104 offset:32768\n"
"v_mfma_f32_16x16x32_bf16 v[168:171], v[196:199], v[20:23], v[168:171]\n"
"ds_read_b128 v[228:231], v104 offset:33792\n"
"v_mfma_f32_16x16x32_bf16 v[184:187], v[192:195], v[24:27], v[184:187]\n"
"ds_read_b128 v[232:235], v104 offset:34816\n"
"v_mfma_f32_16x16x32_bf16 v[184:187], v[196:199], v[28:31], v[184:187]\n"
"ds_read_b128 v[236:239], v104 offset:35840\n"
"s_waitcnt lgkmcnt(10)\n"
"v_mfma_f32_16x16x32_bf16 v[140:143], v[200:203], v[0:3], v[140:143]\n"
"s_waitcnt lgkmcnt(8)\n"
"v_mfma_f32_16x16x32_bf16 v[140:143], v[204:207], v[4:7], v[140:143]\n"
"v_mfma_f32_16x16x32_bf16 v[156:159], v[200:203], v[8:11], v[156:159]\n"
"v_mfma_f32_16x16x32_bf16 v[156:159], v[204:207], v[12:15], v[156:159]\n"
"s_waitcnt lgkmcnt(0)\n"
"s_waitcnt vmcnt(0)\n"
"s_barrier\n"
"s_mov_b32 s84, 0x3db8aa3b\n"
"s_mov_b32 s85, 0x3db8aa3b\n"
"v_mfma_f32_16x16x32_bf16 v[172:175], v[200:203], v[16:19], v[172:175]\n"
"ds_read_b64_tr_b16 v[192:193], v102 offset:0\n"
"v_mfma_f32_16x16x32_bf16 v[172:175], v[204:207], v[20:23], v[172:175]\n"
"ds_read_b64_tr_b16 v[194:195], v102 offset:8704\n"
"v_mfma_f32_16x16x32_bf16 v[188:191], v[200:203], v[24:27], v[188:191]\n"
"ds_read_b64_tr_b16 v[196:197], v102 offset:17408\n"
"v_mfma_f32_16x16x32_bf16 v[188:191], v[204:207], v[28:31], v[188:191]\n"
"ds_read_b64_tr_b16 v[198:199], v102 offset:26112\n"
"ds_read_b64_tr_b16 v[200:201], v103 offset:0\n"
"ds_read_b64_tr_b16 v[202:203], v103 offset:8704\n"
"ds_read_b64_tr_b16 v[204:205], v103 offset:17408\n"
"ds_read_b64_tr_b16 v[206:207], v103 offset:26112\n"
"s_waitcnt lgkmcnt(6)\n"
"v_mfma_f32_16x16x32_bf16 v[128:131], v[192:195], v[208:211], v[128:131]\n"
"s_add_u32 m0, s56, 69632\n"
"s_nop 0\n"
"buffer_load_dwordx4 v249, s[44:47], s64 offen nt lds\n"
"s_waitcnt lgkmcnt(4)\n"
"v_mfma_f32_16x16x32_bf16 v[128:131], v[196:199], v[212:215], v[128:131]\n"
"v_mfma_f32_16x16x32_bf16 v[144:147], v[192:195], v[216:219], v[144:147]\n"
"s_add_u32 m0, s57, 69632\n"
"s_nop 0\n"
"buffer_load_dwordx4 v249, s[44:47], s65 offen nt lds\n"
"v_mfma_f32_16x16x32_bf16 v[144:147], v[196:199], v[220:223], v[144:147]\n"
"v_mfma_f32_16x16x32_bf16 v[160:163], v[192:195], v[224:227], v[160:163]\n"
"s_add_u32 m0, s58, 69632\n"
"s_nop 0\n"
"buffer_load_dwordx4 v249, s[44:47], s66 offen nt lds\n"
"v_mfma_f32_16x16x32_bf16 v[160:163], v[196:199], v[228:231], v[160:163]\n"
"v_mfma_f32_16x16x32_bf16 v[176:179], v[192:195], v[232:235], v[176:179]\n"
"s_add_u32 m0, s59, 69632\n"
"s_nop 0\n"
"buffer_load_dwordx4 v249, s[44:47], s67 offen nt lds\n"
"v_mfma_f32_16x16x32_bf16 v[176:179], v[196:199], v[236:239], v[176:179]\n"
"ds_read_b64_tr_b16 v[192:193], v102 offset:64\n"
"ds_read_b64_tr_b16 v[194:195], v102 offset:8768\n"
"ds_read_b64_tr_b16 v[196:197], v102 offset:17472\n"
"ds_read_b64_tr_b16 v[198:199], v102 offset:26176\n"
"s_waitcnt lgkmcnt(6)\n"
"v_mfma_f32_16x16x32_bf16 v[132:135], v[200:203], v[208:211], v[132:135]\n"
"s_add_u32 m0, s60, 69632\n"
"s_nop 0\n"
"buffer_load_dwordx4 v249, s[44:47], s68 offen nt lds\n"
"s_waitcnt lgkmcnt(4)\n"
"v_mfma_f32_16x16x32_bf16 v[132:135], v[204:207], v[212:215], v[132:135]\n"
"v_pk_mul_f32 v[128:129], v[128:129], s[84:85]\n"
"v_pk_mul_f32 v[130:131], v[130:131], s[84:85]\n"
"v_cvt_pk_bf16_f32 v32, v128, v129\n"
"v_cvt_pk_bf16_f32 v33, v130, v131\n"
"ds_write_b64 v105, v[32:33] offset:0\n"
"v_mfma_f32_16x16x32_bf16 v[148:151], v[200:203], v[216:219], v[148:151]\n"
"v_pk_mul_f32 v[144:145], v[144:145], s[84:85]\n"
"v_pk_mul_f32 v[146:147], v[146:147], s[84:85]\n"
"v_cvt_pk_bf16_f32 v34, v144, v145\n"
"v_cvt_pk_bf16_f32 v35, v146, v147\n"
"ds_write_b64 v105, v[34:35] offset:8192\n"
"s_add_u32 m0, s61, 69632\n"
"s_nop 0\n"
"buffer_load_dwordx4 v249, s[44:47], s69 offen nt lds\n"
"v_mfma_f32_16x16x32_bf16 v[148:151], v[204:207], v[220:223], v[148:151]\n"
"v_pk_mul_f32 v[160:161], v[160:161], s[84:85]\n"
"v_pk_mul_f32 v[162:163], v[162:163], s[84:85]\n"
"v_cvt_pk_bf16_f32 v36, v160, v161\n"
"v_cvt_pk_bf16_f32 v37, v162, v163\n"
"ds_write_b64 v105, v[36:37] offset:16384\n"
"v_mfma_f32_16x16x32_bf16 v[164:167], v[200:203], v[224:227], v[164:167]\n"
"v_pk_mul_f32 v[176:177], v[176:177], s[84:85]\n"
"v_pk_mul_f32 v[178:179], v[178:179], s[84:85]\n"
"v_cvt_pk_bf16_f32 v38, v176, v177\n"
"v_cvt_pk_bf16_f32 v39, v178, v179\n"
"ds_write_b64 v105, v[38:39] offset:24576\n"
"s_add_u32 m0, s62, 69632\n"
"s_nop 0\n"
"buffer_load_dwordx4 v249, s[44:47], s70 offen nt lds\n"
"v_mfma_f32_16x16x32_bf16 v[164:167], v[204:207], v[228:231], v[164:167]\n"
"v_mfma_f32_16x16x32_bf16 v[180:183], v[200:203], v[232:235], v[180:183]\n"
"s_add_u32 m0, s63, 69632\n"
"s_nop 0\n"
"buffer_load_dwordx4 v249, s[44:47], s71 offen nt lds\n"
"v_add_u32 v249, 32768, v249\n"
"v_mfma_f32_16x16x32_bf16 v[180:183], v[204:207], v[236:239], v[180:183]\n"
"ds_read_b64_tr_b16 v[200:201], v103 offset:64\n"
"ds_read_b64_tr_b16 v[202:203], v103 offset:8768\n"
"ds_read_b64_tr_b16 v[204:205], v103 offset:17472\n"
"ds_read_b64_tr_b16 v[206:207], v103 offset:26176\n"
"s_waitcnt lgkmcnt(10)\n"
"v_mfma_f32_16x16x32_bf16 v[136:139], v[192:195], v[208:211], v[136:139]\n"
"s_waitcnt lgkmcnt(8)\n"
"v_mfma_f32_16x16x32_bf16 v[136:139], v[196:199], v[212:215], v[136:139]\n"
"v_pk_mul_f32 v[132:133], v[132:133], s[84:85]\n"
"v_pk_mul_f32 v[134:135], v[134:135], s[84:85]\n"
"v_cvt_pk_bf16_f32 v40, v132, v133\n"
"v_cvt_pk_bf16_f32 v41, v134, v135\n"
"ds_write_b64 v105, v[40:41] offset:512\n"
"v_mfma_f32_16x16x32_bf16 v[152:155], v[192:195], v[216:219], v[152:155]\n"
"v_pk_mul_f32 v[148:149], v[148:149], s[84:85]\n"
"v_pk_mul_f32 v[150:151], v[150:151], s[84:85]\n"
"v_cvt_pk_bf16_f32 v42, v148, v149\n"
"v_cvt_pk_bf16_f32 v43, v150, v151\n"
"ds_write_b64 v105, v[42:43] offset:8704\n"
"v_mfma_f32_16x16x32_bf16 v[152:155], v[196:199], v[220:223], v[152:155]\n"
"v_pk_mul_f32 v[164:165], v[164:165], s[84:85]\n"
"v_pk_mul_f32 v[166:167], v[166:167], s[84:85]\n"
"v_cvt_pk_bf16_f32 v44, v164, v165\n"
"v_cvt_pk_bf16_f32 v45, v166, v167\n"
"ds_write_b64 v105, v[44:45] offset:16896\n"
"v_mfma_f32_16x16x32_bf16 v[168:171], v[192:195], v[224:227], v[168:171]\n"
"v_pk_mul_f32 v[180:181], v[180:181], s[84:85]\n"
"v_pk_mul_f32 v[182:183], v[182:183], s[84:85]\n"
"v_cvt_pk_bf16_f32 v46, v180, v181\n"
"v_cvt_pk_bf16_f32 v47, v182, v183\n"
"ds_write_b64 v105, v[46:47] offset:25088\n"
"v_mfma_f32_16x16x32_bf16 v[168:171], v[196:199], v[228:231], v[168:171]\n"
"v_mfma_f32_16x16x32_bf16 v[184:187], v[192:195], v[232:235], v[184:187]\n"
"v_mfma_f32_16x16x32_bf16 v[184:187], v[196:199], v[236:239], v[184:187]\n"
"s_waitcnt lgkmcnt(6)\n"
"v_mfma_f32_16x16x32_bf16 v[140:143], v[200:203], v[208:211], v[140:143]\n"
"s_waitcnt lgkmcnt(4)\n"
"v_mfma_f32_16x16x32_bf16 v[140:143], v[204:207], v[212:215], v[140:143]\n"
"v_pk_mul_f32 v[136:137], v[136:137], s[84:85]\n"
"v_pk_mul_f32 v[138:139], v[138:139], s[84:85]\n"
"v_cvt_pk_bf16_f32 v48, v136, v137\n"
"v_cvt_pk_bf16_f32 v49, v138, v139\n"
"ds_write_b64 v105, v[48:49] offset:1024\n"
"v_mfma_f32_16x16x32_bf16 v[156:159], v[200:203], v[216:219], v[156:159]\n"
"v_pk_mul_f32 v[152:153], v[152:153], s[84:85]\n"
"v_pk_mul_f32 v[154:155], v[154:155], s[84:85]\n"
"v_cvt_pk_bf16_f32 v50, v152, v153\n"
"v_cvt_pk_bf16_f32 v51, v154, v155\n"
"ds_write_b64 v105, v[50:51] offset:9216\n"
"v_mfma_f32_16x16x32_bf16 v[156:159], v[204:207], v[220:223], v[156:159]\n"
"v_pk_mul_f32 v[168:169], v[168:169], s[84:85]\n"
"v_pk_mul_f32 v[170:171], v[170:171], s[84:85]\n"
"v_cvt_pk_bf16_f32 v52, v168, v169\n"
"v_cvt_pk_bf16_f32 v53, v170, v171\n"
"ds_write_b64 v105, v[52:53] offset:17408\n"
"v_mfma_f32_16x16x32_bf16 v[172:175], v[200:203], v[224:227], v[172:175]\n"
"v_pk_mul_f32 v[184:185], v[184:185], s[84:85]\n"
"v_pk_mul_f32 v[186:187], v[186:187], s[84:85]\n"
"v_cvt_pk_bf16_f32 v54, v184, v185\n"
"v_cvt_pk_bf16_f32 v55, v186, v187\n"
"ds_write_b64 v105, v[54:55] offset:25600\n"
"v_mfma_f32_16x16x32_bf16 v[172:175], v[204:207], v[228:231], v[172:175]\n"
"v_mfma_f32_16x16x32_bf16 v[188:191], v[200:203], v[232:235], v[188:191]\n"
"v_mfma_f32_16x16x32_bf16 v[188:191], v[204:207], v[236:239], v[188:191]\n"
"v_pk_mul_f32 v[140:141], v[140:141], s[84:85]\n"
"v_pk_mul_f32 v[142:143], v[142:143], s[84:85]\n"
"v_cvt_pk_bf16_f32 v56, v140, v141\n"
"v_cvt_pk_bf16_f32 v57, v142, v143\n"
"ds_write_b64 v105, v[56:57] offset:1536\n"
"v_pk_mul_f32 v[156:157], v[156:157], s[84:85]\n"
"v_pk_mul_f32 v[158:159], v[158:159], s[84:85]\n"
"v_cvt_pk_bf16_f32 v58, v156, v157\n"
"v_cvt_pk_bf16_f32 v59, v158, v159\n"
"ds_write_b64 v105, v[58:59] offset:9728\n"
"v_pk_mul_f32 v[172:173], v[172:173], s[84:85]\n"
"v_pk_mul_f32 v[174:175], v[174:175], s[84:85]\n"
"v_cvt_pk_bf16_f32 v60, v172, v173\n"
"v_cvt_pk_bf16_f32 v61, v174, v175\n"
"ds_write_b64 v105, v[60:61] offset:17920\n"
"v_pk_mul_f32 v[188:189], v[188:189], s[84:85]\n"
"v_pk_mul_f32 v[190:191], v[190:191], s[84:85]\n"
"v_cvt_pk_bf16_f32 v62, v188, v189\n"
"v_cvt_pk_bf16_f32 v63, v190, v191\n"
"ds_write_b64 v105, v[62:63] offset:26112\n"
"s_waitcnt lgkmcnt(0)\n"
"v_mov_b32 v250, v109\n"
"s_waitcnt lgkmcnt(0)\n"
"s_barrier\n"
"ds_read_b128 v[0:3], v250 offset:0\n"
"ds_read_b128 v[4:7], v250 offset:1024\n"
"ds_read_b128 v[8:11], v250 offset:2048\n"
"ds_read_b128 v[12:15], v250 offset:3072\n"
"ds_read_b128 v[16:19], v250 offset:4096\n"
"ds_read_b128 v[20:23], v250 offset:5120\n"
"ds_read_b128 v[24:27], v250 offset:6144\n"
"ds_read_b128 v[28:31], v250 offset:7168\n"
"ds_read_b128 v[32:35], v250 offset:8192\n"
"ds_read_b128 v[36:39], v250 offset:9216\n"
"ds_read_b128 v[40:43], v250 offset:10240\n"
"ds_read_b128 v[44:47], v250 offset:11264\n"
"ds_read_b128 v[48:51], v250 offset:12288\n"
"ds_read_b128 v[52:55], v250 offset:13312\n"
"ds_read_b128 v[56:59], v250 offset:14336\n"
"ds_read_b128 v[60:63], v250 offset:15360\n"
"ds_read_b128 v[64:67], v250 offset:16384\n"
"ds_read_b128 v[68:71], v250 offset:17408\n"
"ds_read_b128 v[72:75], v250 offset:18432\n"
"ds_read_b128 v[76:79], v250 offset:19456\n"
"ds_read_b128 v[80:83], v250 offset:20480\n"
"ds_read_b128 v[84:87], v250 offset:21504\n"
"ds_read_b128 v[88:91], v250 offset:22528\n"
"ds_read_b128 v[92:95], v250 offset:23552\n"
"ds_read_b128 v[96:99], v250 offset:24576\n"
"ds_read_b128 v[100:103], v250 offset:25600\n"
"ds_read_b128 v[104:107], v250 offset:26624\n"
"ds_read_b128 v[108:111], v250 offset:27648\n"
"ds_read_b128 v[112:115], v250 offset:28672\n"
"ds_read_b128 v[116:119], v250 offset:29696\n"
"ds_read_b128 v[120:123], v250 offset:30720\n"
"ds_read_b128 v[124:127], v250 offset:31744\n"
"v_mov_b64 v[128:129], 0\n"
"v_mov_b64 v[130:131], 0\n"
"v_mov_b64 v[132:133], 0\n"
"v_mov_b64 v[134:135], 0\n"
"v_mov_b64 v[136:137], 0\n"
"v_mov_b64 v[138:139], 0\n"
"v_mov_b64 v[140:141], 0\n"
"v_mov_b64 v[142:143], 0\n"
"v_mov_b64 v[144:145], 0\n"
"v_mov_b64 v[146:147], 0\n"
"v_mov_b64 v[148:149], 0\n"
"v_mov_b64 v[150:151], 0\n"
"v_mov_b64 v[152:153], 0\n"
"v_mov_b64 v[154:155], 0\n"
"v_mov_b64 v[156:157], 0\n"
"v_mov_b64 v[158:159], 0\n"
"v_mov_b64 v[160:161], 0\n"
"v_mov_b64 v[162:163], 0\n"
"v_mov_b64 v[164:165], 0\n"
"v_mov_b64 v[166:167], 0\n"
"v_mov_b64 v[168:169], 0\n"
"v_mov_b64 v[170:171], 0\n"
"v_mov_b64 v[172:173], 0\n"
"v_mov_b64 v[174:175], 0\n"
"v_mov_b64 v[176:177], 0\n"
"v_mov_b64 v[178:179], 0\n"
"v_mov_b64 v[180:181], 0\n"
"v_mov_b64 v[182:183], 0\n"
"v_mov_b64 v[184:185], 0\n"
"v_mov_b64 v[186:187], 0\n"
"v_mov_b64 v[188:189], 0\n"
"v_mov_b64 v[190:191], 0\n"
"v_mov_b32 v240, 0\n"
"v_mov_b32 v241, 0\n"
"v_mov_b32 v242, 0\n"
"v_mov_b32 v243, 0\n"
"v_mov_b64 v[200:201], 0\n"
"v_mov_b64 v[202:203], 0\n"
"v_mov_b64 v[204:205], 0\n"
"v_mov_b64 v[206:207], 0\n"
"v_mov_b64 v[224:225], 0\n"
"v_mov_b64 v[226:227], 0\n"
"v_mov_b64 v[228:229], 0\n"
"v_mov_b64 v[230:231], 0\n"
"v_mov_b64 v[232:233], 0\n"
"v_mov_b64 v[234:235], 0\n"
"v_mov_b64 v[236:237], 0\n"
"v_mov_b64 v[238:239], 0\n"
"s_bfe_u32 s84, %8, 0x40004\n"
"s_mul_i32 s84, s84, 0\n"
"s_xor_b32 s85, s84, 0\n"
"s_lshl_b32 s85, s85, 2\n"
"s_add_u32 s85, s85, s87\n"
"s_mul_i32 s36, s85, 1792\n"
"s_xor_b32 s85, s84, 1\n"
"s_lshl_b32 s85, s85, 2\n"
"s_add_u32 s85, s85, s87\n"
"s_mul_i32 s37, s85, 1792\n"
"s_xor_b32 s85, s84, 2\n"
"s_lshl_b32 s85, s85, 2\n"
"s_add_u32 s85, s85, s87\n"
"s_mul_i32 s38, s85, 1792\n"
"s_xor_b32 s85, s84, 3\n"
"s_lshl_b32 s85, s85, 2\n"
"s_add_u32 s85, s85, s87\n"
"s_mul_i32 s39, s85, 1792\n"
"s_xor_b32 s85, s84, 4\n"
"s_lshl_b32 s85, s85, 2\n"
"s_add_u32 s85, s85, s87\n"
"s_mul_i32 s40, s85, 1792\n"
"s_xor_b32 s85, s84, 5\n"
"s_lshl_b32 s85, s85, 2\n"
"s_add_u32 s85, s85, s87\n"
"s_mul_i32 s41, s85, 1792\n"
"s_xor_b32 s85, s84, 6\n"
"s_lshl_b32 s85, s85, 2\n"
"s_add_u32 s85, s85, s87\n"
"s_mul_i32 s42, s85, 1792\n"
"s_xor_b32 s85, s84, 7\n"
"s_lshl_b32 s85, s85, 2\n"
"s_add_u32 s85, s85, s87\n"
"s_mul_i32 s43, s85, 1792\n"
"s_waitcnt lgkmcnt(0)\n"
"s_barrier\n"
"s_mov_b32 s90, 0x3f803f80\n"
"s_cmp_lg_u32 s91, 0\n"
"s_cbranch_scc1 L_safeA\n"
"s_mov_b32 s83, 15\n"
"L_loopA:\n"
"s_setprio 1\n"
"v_mfma_f32_16x16x32_bf16 v[172:175], v[200:203], v[224:227], v[172:175]\n"
"ds_read_b128 v[208:211], v244 offset:34816\n"
"v_mfma_f32_16x16x32_bf16 v[172:175], v[204:207], v[228:231], v[172:175]\n"
"ds_read_b128 v[212:215], v244 offset:34880\n"
"v_mfma_f32_16x16x32_bf16 v[188:191], v[200:203], v[232:235], v[188:191]\n"
"ds_read_b128 v[216:219], v244 offset:34944\n"
"v_mfma_f32_16x16x32_bf16 v[188:191], v[204:207], v[236:239], v[188:191]\n"
"ds_read_b128 v[220:223], v244 offset:35008\n"
"ds_read_b128 v[224:227], v244 offset:35072\n"
"ds_read_b128 v[228:231], v244 offset:35136\n"
"ds_read_b128 v[232:235], v244 offset:35200\n"
"ds_read_b128 v[236:239], v244 offset:35264\n"
"s_waitcnt lgkmcnt(7)\n"
"v_mfma_f32_16x16x32_bf16 v[192:195], v[208:211], v[0:3], 0\n"
"s_waitcnt lgkmcnt(6)\n"
"v_mfma_f32_16x16x32_bf16 v[192:195], v[212:215], v[4:7], v[192:195]\n"
"s_waitcnt lgkmcnt(5)\n"
"v_mfma_f32_16x16x32_bf16 v[192:195], v[216:219], v[8:11], v[192:195]\n"
"s_waitcnt lgkmcnt(4)\n"
"v_mfma_f32_16x16x32_bf16 v[192:195], v[220:223], v[12:15], v[192:195]\n"
"v_mfma_f32_16x16x32_bf16 v[196:199], v[208:211], v[32:35], 0\n"
"v_mfma_f32_16x16x32_bf16 v[196:199], v[212:215], v[36:39], v[196:199]\n"
"v_mfma_f32_16x16x32_bf16 v[196:199], v[216:219], v[40:43], v[196:199]\n"
"v_mfma_f32_16x16x32_bf16 v[196:199], v[220:223], v[44:47], v[196:199]\n"
"s_waitcnt lgkmcnt(3)\n"
"v_mfma_f32_16x16x32_bf16 v[192:195], v[224:227], v[16:19], v[192:195]\n"
"s_waitcnt lgkmcnt(2)\n"
"v_mfma_f32_16x16x32_bf16 v[192:195], v[228:231], v[20:23], v[192:195]\n"
"s_waitcnt lgkmcnt(1)\n"
"v_mfma_f32_16x16x32_bf16 v[192:195], v[232:235], v[24:27], v[192:195]\n"
"s_waitcnt lgkmcnt(0)\n"
"v_mfma_f32_16x16x32_bf16 v[192:195], v[236:239], v[28:31], v[192:195]\n"
"s_nop 1\n"
"v_mfma_f32_16x16x32_bf16 v[196:199], v[224:227], v[48:51], v[196:199]\n"
"s_nop 1\n"
"v_mfma_f32_16x16x32_bf16 v[196:199], v[228:231], v[52:55], v[196:199]\n"
"s_nop 1\n"
"v_mfma_f32_16x16x32_bf16 v[196:199], v[232:235], v[56:59], v[196:199]\n"
"v_exp_f32 v192, v192\n"
"v_mfma_f32_16x16x32_bf16 v[196:199], v[236:239], v[60:63], v[196:199]\n"
"v_exp_f32 v193, v193\n"
"v_mfma_f32_16x16x32_bf16 v[200:203], v[208:211], v[64:67], 0\n"
"v_exp_f32 v194, v194\n"
"v_mfma_f32_16x16x32_bf16 v[200:203], v[212:215], v[68:71], v[200:203]\n"
"v_exp_f32 v195, v195\n"
"v_mfma_f32_16x16x32_bf16 v[200:203], v[216:219], v[72:75], v[200:203]\n"
"v_add_f32 v240, v240, v192\n"
"v_cvt_pk_bf16_f32 v250, v192, v193\n"
"v_mfma_f32_16x16x32_bf16 v[200:203], v[220:223], v[76:79], v[200:203]\n"
"v_add_f32 v240, v240, v193\n"
"v_cvt_pk_bf16_f32 v251, v194, v195\n"
"v_mfma_f32_16x16x32_bf16 v[200:203], v[224:227], v[80:83], v[200:203]\n"
"v_add_f32 v240, v240, v194\n"
"ds_write_b64 v247, v[250:251] offset:0\n"
"v_mfma_f32_16x16x32_bf16 v[200:203], v[228:231], v[84:87], v[200:203]\n"
"v_add_f32 v240, v240, v195\n"
"v_exp_f32 v196, v196\n"
"v_mfma_f32_16x16x32_bf16 v[200:203], v[232:235], v[88:91], v[200:203]\n"
"v_exp_f32 v197, v197\n"
"v_mfma_f32_16x16x32_bf16 v[200:203], v[236:239], v[92:95], v[200:203]\n"
"v_exp_f32 v198, v198\n"
"v_mfma_f32_16x16x32_bf16 v[204:207], v[208:211], v[96:99], 0\n"
"v_exp_f32 v199, v199\n"
"v_mfma_f32_16x16x32_bf16 v[204:207], v[212:215], v[100:103], v[204:207]\n"
"v_add_f32 v241, v241, v196\n"
"v_cvt_pk_bf16_f32 v252, v196, v197\n"
"v_mfma_f32_16x16x32_bf16 v[204:207], v[216:219], v[104:107], v[204:207]\n"
"v_add_f32 v241, v241, v197\n"
"v_cvt_pk_bf16_f32 v253, v198, v199\n"
"v_mfma_f32_16x16x32_bf16 v[204:207], v[220:223], v[108:111], v[204:207]\n"
"v_add_f32 v241, v241, v198\n"
"ds_write_b64 v247, v[252:253] offset:2048\n"
"v_mfma_f32_16x16x32_bf16 v[204:207], v[224:227], v[112:115], v[204:207]\n"
"v_add_f32 v241, v241, v199\n"
"v_exp_f32 v200, v200\n"
"v_mfma_f32_16x16x32_bf16 v[204:207], v[228:231], v[116:119], v[204:207]\n"
"v_exp_f32 v201, v201\n"
"v_mfma_f32_16x16x32_bf16 v[204:207], v[232:235], v[120:123], v[204:207]\n"
"v_exp_f32 v202, v202\n"
"v_mfma_f32_16x16x32_bf16 v[204:207], v[236:239], v[124:127], v[204:207]\n"
"v_exp_f32 v203, v203\n"
"v_add_f32 v242, v242, v200\n"
"v_cvt_pk_bf16_f32 v254, v200, v201\n"
"v_add_f32 v242, v242, v201\n"
"v_cvt_pk_bf16_f32 v255, v202, v203\n"
"v_add_f32 v242, v242, v202\n"
"ds_write_b64 v247, v[254:255] offset:4096\n"
"v_add_f32 v242, v242, v203\n"
"v_exp_f32 v204, v204\n"
"v_exp_f32 v205, v205\n"
"v_exp_f32 v206, v206\n"
"v_exp_f32 v207, v207\n"
"v_add_f32 v243, v243, v204\n"
"v_cvt_pk_bf16_f32 v250, v204, v205\n"
"v_add_f32 v243, v243, v205\n"
"v_cvt_pk_bf16_f32 v251, v206, v207\n"
"v_add_f32 v243, v243, v206\n"
"ds_write_b64 v247, v[250:251] offset:6144\n"
"v_add_f32 v243, v243, v207\n"
"s_setprio 0\n"
"s_waitcnt vmcnt(0) lgkmcnt(0)\n"
"s_barrier\n"
"ds_read_b128 v[208:211], v248 offset:0\n"
"ds_read_b64_tr_b16 v[192:193], v245 offset:0\n"
"ds_read_b64_tr_b16 v[194:195], v245 offset:8704\n"
"ds_read_b128 v[212:215], v248 offset:1024\n"
"ds_read_b64_tr_b16 v[196:197], v245 offset:17408\n"
"ds_read_b64_tr_b16 v[198:199], v245 offset:26112\n"
"ds_read_b128 v[216:219], v248 offset:2048\n"
"ds_read_b128 v[220:223], v248 offset:3072\n"
"ds_read_b128 v[224:227], v248 offset:4096\n"
"ds_read_b128 v[228:231], v248 offset:5120\n"
"ds_read_b128 v[232:235], v248 offset:6144\n"
"ds_read_b128 v[236:239], v248 offset:7168\n"
"s_add_u32 m0, s56, 104448\n"
"ds_read_b64_tr_b16 v[200:201], v246 offset:0\n"
"buffer_load_dwordx4 v249, s[44:47], s64 offen nt lds\n"
"s_add_u32 m0, s57, 104448\n"
"ds_read_b64_tr_b16 v[202:203], v246 offset:8704\n"
"buffer_load_dwordx4 v249, s[44:47], s65 offen nt lds\n"
"s_add_u32 m0, s58, 104448\n"
"ds_read_b64_tr_b16 v[204:205], v246 offset:17408\n"
"buffer_load_dwordx4 v249, s[44:47], s66 offen nt lds\n"
"s_add_u32 m0, s59, 104448\n"
"ds_read_b64_tr_b16 v[206:207], v246 offset:26112\n"
"buffer_load_dwordx4 v249, s[44:47], s67 offen nt lds\n"
"s_add_u32 m0, s60, 104448\n"
"s_nop 0\n"
"buffer_load_dwordx4 v249, s[44:47], s68 offen nt lds\n"
"s_add_u32 m0, s61, 104448\n"
"s_nop 0\n"
"buffer_load_dwordx4 v249, s[44:47], s69 offen nt lds\n"
"v_add_u32 v249, 32768, v249\n"
"s_waitcnt lgkmcnt(13)\n"
"v_mfma_f32_16x16x32_bf16 v[128:131], v[192:195], v[208:211], v[128:131]\n"
"s_waitcnt lgkmcnt(10)\n"
"v_mfma_f32_16x16x32_bf16 v[128:131], v[196:199], v[212:215], v[128:131]\n"
"s_waitcnt lgkmcnt(9)\n"
"v_mfma_f32_16x16x32_bf16 v[144:147], v[192:195], v[216:219], v[144:147]\n"
"s_waitcnt lgkmcnt(8)\n"
"v_mfma_f32_16x16x32_bf16 v[144:147], v[196:199], v[220:223], v[144:147]\n"
"s_waitcnt lgkmcnt(7)\n"
"v_mfma_f32_16x16x32_bf16 v[160:163], v[192:195], v[224:227], v[160:163]\n"
"s_waitcnt lgkmcnt(6)\n"
"v_mfma_f32_16x16x32_bf16 v[160:163], v[196:199], v[228:231], v[160:163]\n"
"s_waitcnt lgkmcnt(5)\n"
"v_mfma_f32_16x16x32_bf16 v[176:179], v[192:195], v[232:235], v[176:179]\n"
"s_waitcnt lgkmcnt(4)\n"
"v_mfma_f32_16x16x32_bf16 v[176:179], v[196:199], v[236:239], v[176:179]\n"
"ds_read_b64_tr_b16 v[192:193], v245 offset:64\n"
"ds_read_b64_tr_b16 v[194:195], v245 offset:8768\n"
"ds_read_b64_tr_b16 v[196:197], v245 offset:17472\n"
"ds_read_b64_tr_b16 v[198:199], v245 offset:26176\n"
"s_waitcnt lgkmcnt(6)\n"
"v_mfma_f32_16x16x32_bf16 v[132:135], v[200:203], v[208:211], v[132:135]\n"
"s_waitcnt lgkmcnt(4)\n"
"v_mfma_f32_16x16x32_bf16 v[132:135], v[204:207], v[212:215], v[132:135]\n"
"v_mfma_f32_16x16x32_bf16 v[148:151], v[200:203], v[216:219], v[148:151]\n"
"v_mfma_f32_16x16x32_bf16 v[148:151], v[204:207], v[220:223], v[148:151]\n"
"v_mfma_f32_16x16x32_bf16 v[164:167], v[200:203], v[224:227], v[164:167]\n"
"v_mfma_f32_16x16x32_bf16 v[164:167], v[204:207], v[228:231], v[164:167]\n"
"v_mfma_f32_16x16x32_bf16 v[180:183], v[200:203], v[232:235], v[180:183]\n"
"v_mfma_f32_16x16x32_bf16 v[180:183], v[204:207], v[236:239], v[180:183]\n"
"ds_read_b64_tr_b16 v[200:201], v246 offset:64\n"
"ds_read_b64_tr_b16 v[202:203], v246 offset:8768\n"
"ds_read_b64_tr_b16 v[204:205], v246 offset:17472\n"
"ds_read_b64_tr_b16 v[206:207], v246 offset:26176\n"
"s_waitcnt lgkmcnt(6)\n"
"v_mfma_f32_16x16x32_bf16 v[136:139], v[192:195], v[208:211], v[136:139]\n"
"s_waitcnt lgkmcnt(4)\n"
"v_mfma_f32_16x16x32_bf16 v[136:139], v[196:199], v[212:215], v[136:139]\n"
"v_mfma_f32_16x16x32_bf16 v[152:155], v[192:195], v[216:219], v[152:155]\n"
"v_mfma_f32_16x16x32_bf16 v[152:155], v[196:199], v[220:223], v[152:155]\n"
"v_mfma_f32_16x16x32_bf16 v[168:171], v[192:195], v[224:227], v[168:171]\n"
"v_mfma_f32_16x16x32_bf16 v[168:171], v[196:199], v[228:231], v[168:171]\n"
"v_mfma_f32_16x16x32_bf16 v[184:187], v[192:195], v[232:235], v[184:187]\n"
"v_mfma_f32_16x16x32_bf16 v[184:187], v[196:199], v[236:239], v[184:187]\n"
"s_waitcnt lgkmcnt(2)\n"
"v_mfma_f32_16x16x32_bf16 v[140:143], v[200:203], v[208:211], v[140:143]\n"
"s_waitcnt lgkmcnt(0)\n"
"v_mfma_f32_16x16x32_bf16 v[140:143], v[204:207], v[212:215], v[140:143]\n"
"v_mfma_f32_16x16x32_bf16 v[156:159], v[200:203], v[216:219], v[156:159]\n"
"v_mfma_f32_16x16x32_bf16 v[156:159], v[204:207], v[220:223], v[156:159]\n"
"s_waitcnt lgkmcnt(0)\n"
"s_barrier\n"
"s_setprio 1\n"
"v_mfma_f32_16x16x32_bf16 v[172:175], v[200:203], v[224:227], v[172:175]\n"
"ds_read_b128 v[208:211], v244 offset:0\n"
"v_mfma_f32_16x16x32_bf16 v[172:175], v[204:207], v[228:231], v[172:175]\n"
"ds_read_b128 v[212:215], v244 offset:64\n"
"v_mfma_f32_16x16x32_bf16 v[188:191], v[200:203], v[232:235], v[188:191]\n"
"ds_read_b128 v[216:219], v244 offset:128\n"
"v_mfma_f32_16x16x32_bf16 v[188:191], v[204:207], v[236:239], v[188:191]\n"
"ds_read_b128 v[220:223], v244 offset:192\n"
"ds_read_b128 v[224:227], v244 offset:256\n"
"ds_read_b128 v[228:231], v244 offset:320\n"
"ds_read_b128 v[232:235], v244 offset:384\n"
"ds_read_b128 v[236:239], v244 offset:448\n"
"s_waitcnt lgkmcnt(7)\n"
"v_mfma_f32_16x16x32_bf16 v[192:195], v[208:211], v[0:3], 0\n"
"s_waitcnt lgkmcnt(6)\n"
"v_mfma_f32_16x16x32_bf16 v[192:195], v[212:215], v[4:7], v[192:195]\n"
"s_waitcnt lgkmcnt(5)\n"
"v_mfma_f32_16x16x32_bf16 v[192:195], v[216:219], v[8:11], v[192:195]\n"
"s_waitcnt lgkmcnt(4)\n"
"v_mfma_f32_16x16x32_bf16 v[192:195], v[220:223], v[12:15], v[192:195]\n"
"v_mfma_f32_16x16x32_bf16 v[196:199], v[208:211], v[32:35], 0\n"
"v_mfma_f32_16x16x32_bf16 v[196:199], v[212:215], v[36:39], v[196:199]\n"
"v_mfma_f32_16x16x32_bf16 v[196:199], v[216:219], v[40:43], v[196:199]\n"
"v_mfma_f32_16x16x32_bf16 v[196:199], v[220:223], v[44:47], v[196:199]\n"
"s_waitcnt lgkmcnt(3)\n"
"v_mfma_f32_16x16x32_bf16 v[192:195], v[224:227], v[16:19], v[192:195]\n"
"s_waitcnt lgkmcnt(2)\n"
"v_mfma_f32_16x16x32_bf16 v[192:195], v[228:231], v[20:23], v[192:195]\n"
"s_waitcnt lgkmcnt(1)\n"
"v_mfma_f32_16x16x32_bf16 v[192:195], v[232:235], v[24:27], v[192:195]\n"
"s_waitcnt lgkmcnt(0)\n"
"v_mfma_f32_16x16x32_bf16 v[192:195], v[236:239], v[28:31], v[192:195]\n"
"s_nop 1\n"
"v_mfma_f32_16x16x32_bf16 v[196:199], v[224:227], v[48:51], v[196:199]\n"
"s_nop 1\n"
"v_mfma_f32_16x16x32_bf16 v[196:199], v[228:231], v[52:55], v[196:199]\n"
"s_nop 1\n"
"v_mfma_f32_16x16x32_bf16 v[196:199], v[232:235], v[56:59], v[196:199]\n"
"v_exp_f32 v192, v192\n"
"v_mfma_f32_16x16x32_bf16 v[196:199], v[236:239], v[60:63], v[196:199]\n"
"v_exp_f32 v193, v193\n"
"v_mfma_f32_16x16x32_bf16 v[200:203], v[208:211], v[64:67], 0\n"
"v_exp_f32 v194, v194\n"
"v_mfma_f32_16x16x32_bf16 v[200:203], v[212:215], v[68:71], v[200:203]\n"
"v_exp_f32 v195, v195\n"
"v_mfma_f32_16x16x32_bf16 v[200:203], v[216:219], v[72:75], v[200:203]\n"
"v_add_f32 v240, v240, v192\n"
"v_cvt_pk_bf16_f32 v250, v192, v193\n"
"v_mfma_f32_16x16x32_bf16 v[200:203], v[220:223], v[76:79], v[200:203]\n"
"v_add_f32 v240, v240, v193\n"
"v_cvt_pk_bf16_f32 v251, v194, v195\n"
"v_mfma_f32_16x16x32_bf16 v[200:203], v[224:227], v[80:83], v[200:203]\n"
"v_add_f32 v240, v240, v194\n"
"ds_write_b64 v247, v[250:251] offset:0\n"
"v_mfma_f32_16x16x32_bf16 v[200:203], v[228:231], v[84:87], v[200:203]\n"
"v_add_f32 v240, v240, v195\n"
"v_exp_f32 v196, v196\n"
"v_mfma_f32_16x16x32_bf16 v[200:203], v[232:235], v[88:91], v[200:203]\n"
"v_exp_f32 v197, v197\n"
"v_mfma_f32_16x16x32_bf16 v[200:203], v[236:239], v[92:95], v[200:203]\n"
"v_exp_f32 v198, v198\n"
"v_mfma_f32_16x16x32_bf16 v[204:207], v[208:211], v[96:99], 0\n"
"v_exp_f32 v199, v199\n"
"v_mfma_f32_16x16x32_bf16 v[204:207], v[212:215], v[100:103], v[204:207]\n"
"v_add_f32 v241, v241, v196\n"
"v_cvt_pk_bf16_f32 v252, v196, v197\n"
"v_mfma_f32_16x16x32_bf16 v[204:207], v[216:219], v[104:107], v[204:207]\n"
"v_add_f32 v241, v241, v197\n"
"v_cvt_pk_bf16_f32 v253, v198, v199\n"
"v_mfma_f32_16x16x32_bf16 v[204:207], v[220:223], v[108:111], v[204:207]\n"
"v_add_f32 v241, v241, v198\n"
"ds_write_b64 v247, v[252:253] offset:2048\n"
"v_mfma_f32_16x16x32_bf16 v[204:207], v[224:227], v[112:115], v[204:207]\n"
"v_add_f32 v241, v241, v199\n"
"v_exp_f32 v200, v200\n"
"v_mfma_f32_16x16x32_bf16 v[204:207], v[228:231], v[116:119], v[204:207]\n"
"v_exp_f32 v201, v201\n"
"v_mfma_f32_16x16x32_bf16 v[204:207], v[232:235], v[120:123], v[204:207]\n"
"v_exp_f32 v202, v202\n"
"v_mfma_f32_16x16x32_bf16 v[204:207], v[236:239], v[124:127], v[204:207]\n"
"v_exp_f32 v203, v203\n"
"v_add_f32 v242, v242, v200\n"
"v_cvt_pk_bf16_f32 v254, v200, v201\n"
"v_add_f32 v242, v242, v201\n"
"v_cvt_pk_bf16_f32 v255, v202, v203\n"
"v_add_f32 v242, v242, v202\n"
"ds_write_b64 v247, v[254:255] offset:4096\n"
"v_add_f32 v242, v242, v203\n"
"v_exp_f32 v204, v204\n"
"v_exp_f32 v205, v205\n"
"v_exp_f32 v206, v206\n"
"v_exp_f32 v207, v207\n"
"v_add_f32 v243, v243, v204\n"
"v_cvt_pk_bf16_f32 v250, v204, v205\n"
"v_add_f32 v243, v243, v205\n"
"v_cvt_pk_bf16_f32 v251, v206, v207\n"
"v_add_f32 v243, v243, v206\n"
"ds_write_b64 v247, v[250:251] offset:6144\n"
"v_add_f32 v243, v243, v207\n"
"s_setprio 0\n"
"s_waitcnt vmcnt(0) lgkmcnt(0)\n"
"s_barrier\n"
"ds_read_b128 v[208:211], v248 offset:0\n"
"ds_read_b64_tr_b16 v[192:193], v245 offset:34816\n"
"ds_read_b64_tr_b16 v[194:195], v245 offset:43520\n"
"ds_read_b128 v[212:215], v248 offset:1024\n"
"ds_read_b64_tr_b16 v[196:197], v245 offset:52224\n"
"ds_read_b64_tr_b16 v[198:199], v245 offset:60928\n"
"ds_read_b128 v[216:219], v248 offset:2048\n"
"ds_read_b128 v[220:223], v248 offset:3072\n"
"ds_read_b128 v[224:227], v248 offset:4096\n"
"ds_read_b128 v[228:231], v248 offset:5120\n"
"ds_read_b128 v[232:235], v248 offset:6144\n"
"ds_read_b128 v[236:239], v248 offset:7168\n"
"s_add_u32 m0, s56, 69632\n"
"ds_read_b64_tr_b16 v[200:201], v246 offset:34816\n"
"buffer_load_dwordx4 v249, s[44:47], s64 offen nt lds\n"
"s_add_u32 m0, s57, 69632\n"
"ds_read_b64_tr_b16 v[202:203], v246 offset:43520\n"
"buffer_load_dwordx4 v249, s[44:47], s65 offen nt lds\n"
"s_add_u32 m0, s58, 69632\n"
"ds_read_b64_tr_b16 v[204:205], v246 offset:52224\n"
"buffer_load_dwordx4 v249, s[44:47], s66 offen nt lds\n"
"s_add_u32 m0, s59, 69632\n"
"ds_read_b64_tr_b16 v[206:207], v246 offset:60928\n"
"buffer_load_dwordx4 v249, s[44:47], s67 offen nt lds\n"
"s_add_u32 m0, s60, 69632\n"
"s_nop 0\n"
"buffer_load_dwordx4 v249, s[44:47], s68 offen nt lds\n"
"s_add_u32 m0, s61, 69632\n"
"s_nop 0\n"
"buffer_load_dwordx4 v249, s[44:47], s69 offen nt lds\n"
"v_add_u32 v249, 32768, v249\n"
"s_waitcnt lgkmcnt(13)\n"
"v_mfma_f32_16x16x32_bf16 v[128:131], v[192:195], v[208:211], v[128:131]\n"
"s_waitcnt lgkmcnt(10)\n"
"v_mfma_f32_16x16x32_bf16 v[128:131], v[196:199], v[212:215], v[128:131]\n"
"s_waitcnt lgkmcnt(9)\n"
"v_mfma_f32_16x16x32_bf16 v[144:147], v[192:195], v[216:219], v[144:147]\n"
"s_waitcnt lgkmcnt(8)\n"
"v_mfma_f32_16x16x32_bf16 v[144:147], v[196:199], v[220:223], v[144:147]\n"
"s_waitcnt lgkmcnt(7)\n"
"v_mfma_f32_16x16x32_bf16 v[160:163], v[192:195], v[224:227], v[160:163]\n"
"s_waitcnt lgkmcnt(6)\n"
"v_mfma_f32_16x16x32_bf16 v[160:163], v[196:199], v[228:231], v[160:163]\n"
"s_waitcnt lgkmcnt(5)\n"
"v_mfma_f32_16x16x32_bf16 v[176:179], v[192:195], v[232:235], v[176:179]\n"
"s_waitcnt lgkmcnt(4)\n"
"v_mfma_f32_16x16x32_bf16 v[176:179], v[196:199], v[236:239], v[176:179]\n"
"ds_read_b64_tr_b16 v[192:193], v245 offset:34880\n"
"ds_read_b64_tr_b16 v[194:195], v245 offset:43584\n"
"ds_read_b64_tr_b16 v[196:197], v245 offset:52288\n"
"ds_read_b64_tr_b16 v[198:199], v245 offset:60992\n"
"s_waitcnt lgkmcnt(6)\n"
"v_mfma_f32_16x16x32_bf16 v[132:135], v[200:203], v[208:211], v[132:135]\n"
"s_waitcnt lgkmcnt(4)\n"
"v_mfma_f32_16x16x32_bf16 v[132:135], v[204:207], v[212:215], v[132:135]\n"
"v_mfma_f32_16x16x32_bf16 v[148:151], v[200:203], v[216:219], v[148:151]\n"
"v_mfma_f32_16x16x32_bf16 v[148:151], v[204:207], v[220:223], v[148:151]\n"
"v_mfma_f32_16x16x32_bf16 v[164:167], v[200:203], v[224:227], v[164:167]\n"
"v_mfma_f32_16x16x32_bf16 v[164:167], v[204:207], v[228:231], v[164:167]\n"
"v_mfma_f32_16x16x32_bf16 v[180:183], v[200:203], v[232:235], v[180:183]\n"
"v_mfma_f32_16x16x32_bf16 v[180:183], v[204:207], v[236:239], v[180:183]\n"
"ds_read_b64_tr_b16 v[200:201], v246 offset:34880\n"
"ds_read_b64_tr_b16 v[202:203], v246 offset:43584\n"
"ds_read_b64_tr_b16 v[204:205], v246 offset:52288\n"
"ds_read_b64_tr_b16 v[206:207], v246 offset:60992\n"
"s_waitcnt lgkmcnt(6)\n"
"v_mfma_f32_16x16x32_bf16 v[136:139], v[192:195], v[208:211], v[136:139]\n"
"s_waitcnt lgkmcnt(4)\n"
"v_mfma_f32_16x16x32_bf16 v[136:139], v[196:199], v[212:215], v[136:139]\n"
"v_mfma_f32_16x16x32_bf16 v[152:155], v[192:195], v[216:219], v[152:155]\n"
"v_mfma_f32_16x16x32_bf16 v[152:155], v[196:199], v[220:223], v[152:155]\n"
"v_mfma_f32_16x16x32_bf16 v[168:171], v[192:195], v[224:227], v[168:171]\n"
"v_mfma_f32_16x16x32_bf16 v[168:171], v[196:199], v[228:231], v[168:171]\n"
"v_mfma_f32_16x16x32_bf16 v[184:187], v[192:195], v[232:235], v[184:187]\n"
"v_mfma_f32_16x16x32_bf16 v[184:187], v[196:199], v[236:239], v[184:187]\n"
"s_waitcnt lgkmcnt(2)\n"
"v_mfma_f32_16x16x32_bf16 v[140:143], v[200:203], v[208:211], v[140:143]\n"
"s_waitcnt lgkmcnt(0)\n"
"v_mfma_f32_16x16x32_bf16 v[140:143], v[204:207], v[212:215], v[140:143]\n"
"v_mfma_f32_16x16x32_bf16 v[156:159], v[200:203], v[216:219], v[156:159]\n"
"v_mfma_f32_16x16x32_bf16 v[156:159], v[204:207], v[220:223], v[156:159]\n"
"s_waitcnt lgkmcnt(0)\n"
"s_barrier\n"
"s_sub_u32 s83, s83, 1\n"
"s_cmp_lg_u32 s83, 0\n"
"s_cbranch_scc1 L_loopA\n"
"s_setprio 1\n"
"v_mfma_f32_16x16x32_bf16 v[172:175], v[200:203], v[224:227], v[172:175]\n"
"ds_read_b128 v[208:211], v244 offset:34816\n"
"v_mfma_f32_16x16x32_bf16 v[172:175], v[204:207], v[228:231], v[172:175]\n"
"ds_read_b128 v[212:215], v244 offset:34880\n"
"v_mfma_f32_16x16x32_bf16 v[188:191], v[200:203], v[232:235], v[188:191]\n"
"ds_read_b128 v[216:219], v244 offset:34944\n"
"v_mfma_f32_16x16x32_bf16 v[188:191], v[204:207], v[236:239], v[188:191]\n"
"ds_read_b128 v[220:223], v244 offset:35008\n"
"ds_read_b128 v[224:227], v244 offset:35072\n"
"ds_read_b128 v[228:231], v244 offset:35136\n"
"ds_read_b128 v[232:235], v244 offset:35200\n"
"ds_read_b128 v[236:239], v244 offset:35264\n"
"s_waitcnt lgkmcnt(7)\n"
"v_mfma_f32_16x16x32_bf16 v[192:195], v[208:211], v[0:3], 0\n"
"s_waitcnt lgkmcnt(6)\n"
"v_mfma_f32_16x16x32_bf16 v[192:195], v[212:215], v[4:7], v[192:195]\n"
"s_waitcnt lgkmcnt(5)\n"
"v_mfma_f32_16x16x32_bf16 v[192:195], v[216:219], v[8:11], v[192:195]\n"
"s_waitcnt lgkmcnt(4)\n"
"v_mfma_f32_16x16x32_bf16 v[192:195], v[220:223], v[12:15], v[192:195]\n"
"v_mfma_f32_16x16x32_bf16 v[196:199], v[208:211], v[32:35], 0\n"
"v_mfma_f32_16x16x32_bf16 v[196:199], v[212:215], v[36:39], v[196:199]\n"
"v_mfma_f32_16x16x32_bf16 v[196:199], v[216:219], v[40:43], v[196:199]\n"
"v_mfma_f32_16x16x32_bf16 v[196:199], v[220:223], v[44:47], v[196:199]\n"
"s_waitcnt lgkmcnt(3)\n"
"v_mfma_f32_16x16x32_bf16 v[192:195], v[224:227], v[16:19], v[192:195]\n"
"s_waitcnt lgkmcnt(2)\n"
"v_mfma_f32_16x16x32_bf16 v[192:195], v[228:231], v[20:23], v[192:195]\n"
"s_waitcnt lgkmcnt(1)\n"
"v_mfma_f32_16x16x32_bf16 v[192:195], v[232:235], v[24:27], v[192:195]\n"
"s_waitcnt lgkmcnt(0)\n"
"v_mfma_f32_16x16x32_bf16 v[192:195], v[236:239], v[28:31], v[192:195]\n"
"s_nop 1\n"
"v_mfma_f32_16x16x32_bf16 v[196:199], v[224:227], v[48:51], v[196:199]\n"
"s_nop 1\n"
"v_mfma_f32_16x16x32_bf16 v[196:199], v[228:231], v[52:55], v[196:199]\n"
"s_nop 1\n"
"v_mfma_f32_16x16x32_bf16 v[196:199], v[232:235], v[56:59], v[196:199]\n"
"v_exp_f32 v192, v192\n"
"v_mfma_f32_16x16x32_bf16 v[196:199], v[236:239], v[60:63], v[196:199]\n"
"v_exp_f32 v193, v193\n"
"v_mfma_f32_16x16x32_bf16 v[200:203], v[208:211], v[64:67], 0\n"
"v_exp_f32 v194, v194\n"
"v_mfma_f32_16x16x32_bf16 v[200:203], v[212:215], v[68:71], v[200:203]\n"
"v_exp_f32 v195, v195\n"
"v_mfma_f32_16x16x32_bf16 v[200:203], v[216:219], v[72:75], v[200:203]\n"
"v_add_f32 v240, v240, v192\n"
"v_cvt_pk_bf16_f32 v250, v192, v193\n"
"v_mfma_f32_16x16x32_bf16 v[200:203], v[220:223], v[76:79], v[200:203]\n"
"v_add_f32 v240, v240, v193\n"
"v_cvt_pk_bf16_f32 v251, v194, v195\n"
"v_mfma_f32_16x16x32_bf16 v[200:203], v[224:227], v[80:83], v[200:203]\n"
"v_add_f32 v240, v240, v194\n"
"ds_write_b64 v247, v[250:251] offset:0\n"
"v_mfma_f32_16x16x32_bf16 v[200:203], v[228:231], v[84:87], v[200:203]\n"
"v_add_f32 v240, v240, v195\n"
"v_exp_f32 v196, v196\n"
"v_mfma_f32_16x16x32_bf16 v[200:203], v[232:235], v[88:91], v[200:203]\n"
"v_exp_f32 v197, v197\n"
"v_mfma_f32_16x16x32_bf16 v[200:203], v[236:239], v[92:95], v[200:203]\n"
"v_exp_f32 v198, v198\n"
"v_mfma_f32_16x16x32_bf16 v[204:207], v[208:211], v[96:99], 0\n"
"v_exp_f32 v199, v199\n"
"v_mfma_f32_16x16x32_bf16 v[204:207], v[212:215], v[100:103], v[204:207]\n"
"v_add_f32 v241, v241, v196\n"
"v_cvt_pk_bf16_f32 v252, v196, v197\n"
"v_mfma_f32_16x16x32_bf16 v[204:207], v[216:219], v[104:107], v[204:207]\n"
"v_add_f32 v241, v241, v197\n"
"v_cvt_pk_bf16_f32 v253, v198, v199\n"
"v_mfma_f32_16x16x32_bf16 v[204:207], v[220:223], v[108:111], v[204:207]\n"
"v_add_f32 v241, v241, v198\n"
"ds_write_b64 v247, v[252:253] offset:2048\n"
"v_mfma_f32_16x16x32_bf16 v[204:207], v[224:227], v[112:115], v[204:207]\n"
"v_add_f32 v241, v241, v199\n"
"v_exp_f32 v200, v200\n"
"v_mfma_f32_16x16x32_bf16 v[204:207], v[228:231], v[116:119], v[204:207]\n"
"v_exp_f32 v201, v201\n"
"v_mfma_f32_16x16x32_bf16 v[204:207], v[232:235], v[120:123], v[204:207]\n"
"v_exp_f32 v202, v202\n"
"v_mfma_f32_16x16x32_bf16 v[204:207], v[236:239], v[124:127], v[204:207]\n"
"v_exp_f32 v203, v203\n"
"v_add_f32 v242, v242, v200\n"
"v_cvt_pk_bf16_f32 v254, v200, v201\n"
"v_add_f32 v242, v242, v201\n"
"v_cvt_pk_bf16_f32 v255, v202, v203\n"
"v_add_f32 v242, v242, v202\n"
"ds_write_b64 v247, v[254:255] offset:4096\n"
"v_add_f32 v242, v242, v203\n"
"v_exp_f32 v204, v204\n"
"v_exp_f32 v205, v205\n"
"v_exp_f32 v206, v206\n"
"v_exp_f32 v207, v207\n"
"v_add_f32 v243, v243, v204\n"
"v_cvt_pk_bf16_f32 v250, v204, v205\n"
"v_add_f32 v243, v243, v205\n"
"v_cvt_pk_bf16_f32 v251, v206, v207\n"
"v_add_f32 v243, v243, v206\n"
"ds_write_b64 v247, v[250:251] offset:6144\n"
"v_add_f32 v243, v243, v207\n"
"s_setprio 0\n"
"s_waitcnt vmcnt(0) lgkmcnt(0)\n"
"s_barrier\n"
"ds_read_b128 v[208:211], v248 offset:0\n"
"ds_read_b64_tr_b16 v[192:193], v245 offset:0\n"
"ds_read_b64_tr_b16 v[194:195], v245 offset:8704\n"
"ds_read_b128 v[212:215], v248 offset:1024\n"
"ds_read_b64_tr_b16 v[196:197], v245 offset:17408\n"
"ds_read_b64_tr_b16 v[198:199], v245 offset:26112\n"
"ds_read_b128 v[216:219], v248 offset:2048\n"
"ds_read_b128 v[220:223], v248 offset:3072\n"
"ds_read_b128 v[224:227], v248 offset:4096\n"
"ds_read_b128 v[228:231], v248 offset:5120\n"
"ds_read_b128 v[232:235], v248 offset:6144\n"
"ds_read_b128 v[236:239], v248 offset:7168\n"
"s_add_u32 m0, s56, 104448\n"
"ds_read_b64_tr_b16 v[200:201], v246 offset:0\n"
"buffer_load_dwordx4 v249, s[44:47], s64 offen nt lds\n"
"s_add_u32 m0, s57, 104448\n"
"ds_read_b64_tr_b16 v[202:203], v246 offset:8704\n"
"buffer_load_dwordx4 v249, s[44:47], s65 offen nt lds\n"
"s_add_u32 m0, s58, 104448\n"
"ds_read_b64_tr_b16 v[204:205], v246 offset:17408\n"
"buffer_load_dwordx4 v249, s[44:47], s66 offen nt lds\n"
"s_add_u32 m0, s59, 104448\n"
"ds_read_b64_tr_b16 v[206:207], v246 offset:26112\n"
"buffer_load_dwordx4 v249, s[44:47], s67 offen nt lds\n"
"s_add_u32 m0, s60, 104448\n"
"s_nop 0\n"
"buffer_load_dwordx4 v249, s[44:47], s68 offen nt lds\n"
"s_add_u32 m0, s61, 104448\n"
"s_nop 0\n"
"buffer_load_dwordx4 v249, s[44:47], s69 offen nt lds\n"
"v_add_u32 v249, 32768, v249\n"
"s_waitcnt lgkmcnt(13)\n"
"v_mfma_f32_16x16x32_bf16 v[128:131], v[192:195], v[208:211], v[128:131]\n"
"s_waitcnt lgkmcnt(10)\n"
"v_mfma_f32_16x16x32_bf16 v[128:131], v[196:199], v[212:215], v[128:131]\n"
"s_waitcnt lgkmcnt(9)\n"
"v_mfma_f32_16x16x32_bf16 v[144:147], v[192:195], v[216:219], v[144:147]\n"
"s_waitcnt lgkmcnt(8)\n"
"v_mfma_f32_16x16x32_bf16 v[144:147], v[196:199], v[220:223], v[144:147]\n"
"s_waitcnt lgkmcnt(7)\n"
"v_mfma_f32_16x16x32_bf16 v[160:163], v[192:195], v[224:227], v[160:163]\n"
"s_waitcnt lgkmcnt(6)\n"
"v_mfma_f32_16x16x32_bf16 v[160:163], v[196:199], v[228:231], v[160:163]\n"
"s_waitcnt lgkmcnt(5)\n"
"v_mfma_f32_16x16x32_bf16 v[176:179], v[192:195], v[232:235], v[176:179]\n"
"s_waitcnt lgkmcnt(4)\n"
"v_mfma_f32_16x16x32_bf16 v[176:179], v[196:199], v[236:239], v[176:179]\n"
"ds_read_b64_tr_b16 v[192:193], v245 offset:64\n"
"ds_read_b64_tr_b16 v[194:195], v245 offset:8768\n"
"ds_read_b64_tr_b16 v[196:197], v245 offset:17472\n"
"ds_read_b64_tr_b16 v[198:199], v245 offset:26176\n"
"s_waitcnt lgkmcnt(6)\n"
"v_mfma_f32_16x16x32_bf16 v[132:135], v[200:203], v[208:211], v[132:135]\n"
"s_waitcnt lgkmcnt(4)\n"
"v_mfma_f32_16x16x32_bf16 v[132:135], v[204:207], v[212:215], v[132:135]\n"
"v_mfma_f32_16x16x32_bf16 v[148:151], v[200:203], v[216:219], v[148:151]\n"
"v_mfma_f32_16x16x32_bf16 v[148:151], v[204:207], v[220:223], v[148:151]\n"
"v_mfma_f32_16x16x32_bf16 v[164:167], v[200:203], v[224:227], v[164:167]\n"
"v_mfma_f32_16x16x32_bf16 v[164:167], v[204:207], v[228:231], v[164:167]\n"
"v_mfma_f32_16x16x32_bf16 v[180:183], v[200:203], v[232:235], v[180:183]\n"
"v_mfma_f32_16x16x32_bf16 v[180:183], v[204:207], v[236:239], v[180:183]\n"
"ds_read_b64_tr_b16 v[200:201], v246 offset:64\n"
"ds_read_b64_tr_b16 v[202:203], v246 offset:8768\n"
"ds_read_b64_tr_b16 v[204:205], v246 offset:17472\n"
"ds_read_b64_tr_b16 v[206:207], v246 offset:26176\n"
"s_waitcnt lgkmcnt(6)\n"
"v_mfma_f32_16x16x32_bf16 v[136:139], v[192:195], v[208:211], v[136:139]\n"
"s_waitcnt lgkmcnt(4)\n"
"v_mfma_f32_16x16x32_bf16 v[136:139], v[196:199], v[212:215], v[136:139]\n"
"v_mfma_f32_16x16x32_bf16 v[152:155], v[192:195], v[216:219], v[152:155]\n"
"v_mfma_f32_16x16x32_bf16 v[152:155], v[196:199], v[220:223], v[152:155]\n"
"v_mfma_f32_16x16x32_bf16 v[168:171], v[192:195], v[224:227], v[168:171]\n"
"v_mfma_f32_16x16x32_bf16 v[168:171], v[196:199], v[228:231], v[168:171]\n"
"v_mfma_f32_16x16x32_bf16 v[184:187], v[192:195], v[232:235], v[184:187]\n"
"v_mfma_f32_16x16x32_bf16 v[184:187], v[196:199], v[236:239], v[184:187]\n"
"s_waitcnt lgkmcnt(2)\n"
"v_mfma_f32_16x16x32_bf16 v[140:143], v[200:203], v[208:211], v[140:143]\n"
"s_waitcnt lgkmcnt(0)\n"
"v_mfma_f32_16x16x32_bf16 v[140:143], v[204:207], v[212:215], v[140:143]\n"
"v_mfma_f32_16x16x32_bf16 v[156:159], v[200:203], v[216:219], v[156:159]\n"
"v_mfma_f32_16x16x32_bf16 v[156:159], v[204:207], v[220:223], v[156:159]\n"
"s_waitcnt lgkmcnt(0)\n"
"s_barrier\n"
"s_setprio 1\n"
"v_mfma_f32_16x16x32_bf16 v[172:175], v[200:203], v[224:227], v[172:175]\n"
"ds_read_b128 v[208:211], v244 offset:0\n"
"v_mfma_f32_16x16x32_bf16 v[172:175], v[204:207], v[228:231], v[172:175]\n"
"ds_read_b128 v[212:215], v244 offset:64\n"
"v_mfma_f32_16x16x32_bf16 v[188:191], v[200:203], v[232:235], v[188:191]\n"
"ds_read_b128 v[216:219], v244 offset:128\n"
"v_mfma_f32_16x16x32_bf16 v[188:191], v[204:207], v[236:239], v[188:191]\n"
"ds_read_b128 v[220:223], v244 offset:192\n"
"ds_read_b128 v[224:227], v244 offset:256\n"
"ds_read_b128 v[228:231], v244 offset:320\n"
"ds_read_b128 v[232:235], v244 offset:384\n"
"ds_read_b128 v[236:239], v244 offset:448\n"
"s_waitcnt lgkmcnt(7)\n"
"v_mfma_f32_16x16x32_bf16 v[192:195], v[208:211], v[0:3], 0\n"
"s_waitcnt lgkmcnt(6)\n"
"v_mfma_f32_16x16x32_bf16 v[192:195], v[212:215], v[4:7], v[192:195]\n"
"s_waitcnt lgkmcnt(5)\n"
"v_mfma_f32_16x16x32_bf16 v[192:195], v[216:219], v[8:11], v[192:195]\n"
"s_waitcnt lgkmcnt(4)\n"
"v_mfma_f32_16x16x32_bf16 v[192:195], v[220:223], v[12:15], v[192:195]\n"
"v_mfma_f32_16x16x32_bf16 v[196:199], v[208:211], v[32:35], 0\n"
"v_mfma_f32_16x16x32_bf16 v[196:199], v[212:215], v[36:39], v[196:199]\n"
"v_mfma_f32_16x16x32_bf16 v[196:199], v[216:219], v[40:43], v[196:199]\n"
"v_mfma_f32_16x16x32_bf16 v[196:199], v[220:223], v[44:47], v[196:199]\n"
"s_waitcnt lgkmcnt(3)\n"
"v_mfma_f32_16x16x32_bf16 v[192:195], v[224:227], v[16:19], v[192:195]\n"
"s_waitcnt lgkmcnt(2)\n"
"v_mfma_f32_16x16x32_bf16 v[192:195], v[228:231], v[20:23], v[192:195]\n"
"s_waitcnt lgkmcnt(1)\n"
"v_mfma_f32_16x16x32_bf16 v[192:195], v[232:235], v[24:27], v[192:195]\n"
"s_waitcnt lgkmcnt(0)\n"
"v_mfma_f32_16x16x32_bf16 v[192:195], v[236:239], v[28:31], v[192:195]\n"
"s_nop 1\n"
"v_mfma_f32_16x16x32_bf16 v[196:199], v[224:227], v[48:51], v[196:199]\n"
"s_nop 1\n"
"v_mfma_f32_16x16x32_bf16 v[196:199], v[228:231], v[52:55], v[196:199]\n"
"s_nop 1\n"
"v_mfma_f32_16x16x32_bf16 v[196:199], v[232:235], v[56:59], v[196:199]\n"
"v_exp_f32 v192, v192\n"
"v_mfma_f32_16x16x32_bf16 v[196:199], v[236:239], v[60:63], v[196:199]\n"
"v_exp_f32 v193, v193\n"
"v_mfma_f32_16x16x32_bf16 v[200:203], v[208:211], v[64:67], 0\n"
"v_exp_f32 v194, v194\n"
"v_mfma_f32_16x16x32_bf16 v[200:203], v[212:215], v[68:71], v[200:203]\n"
"v_exp_f32 v195, v195\n"
"v_mfma_f32_16x16x32_bf16 v[200:203], v[216:219], v[72:75], v[200:203]\n"
"v_add_f32 v240, v240, v192\n"
"v_cvt_pk_bf16_f32 v250, v192, v193\n"
"v_mfma_f32_16x16x32_bf16 v[200:203], v[220:223], v[76:79], v[200:203]\n"
"v_add_f32 v240, v240, v193\n"
"v_cvt_pk_bf16_f32 v251, v194, v195\n"
"v_mfma_f32_16x16x32_bf16 v[200:203], v[224:227], v[80:83], v[200:203]\n"
"v_add_f32 v240, v240, v194\n"
"ds_write_b64 v247, v[250:251] offset:0\n"
"v_mfma_f32_16x16x32_bf16 v[200:203], v[228:231], v[84:87], v[200:203]\n"
"v_add_f32 v240, v240, v195\n"
"v_exp_f32 v196, v196\n"
"v_mfma_f32_16x16x32_bf16 v[200:203], v[232:235], v[88:91], v[200:203]\n"
"v_exp_f32 v197, v197\n"
"v_mfma_f32_16x16x32_bf16 v[200:203], v[236:239], v[92:95], v[200:203]\n"
"v_exp_f32 v198, v198\n"
"v_mfma_f32_16x16x32_bf16 v[204:207], v[208:211], v[96:99], 0\n"
"v_exp_f32 v199, v199\n"
"v_mfma_f32_16x16x32_bf16 v[204:207], v[212:215], v[100:103], v[204:207]\n"
"v_add_f32 v241, v241, v196\n"
"v_cvt_pk_bf16_f32 v252, v196, v197\n"
"v_mfma_f32_16x16x32_bf16 v[204:207], v[216:219], v[104:107], v[204:207]\n"
"v_add_f32 v241, v241, v197\n"
"v_cvt_pk_bf16_f32 v253, v198, v199\n"
"v_mfma_f32_16x16x32_bf16 v[204:207], v[220:223], v[108:111], v[204:207]\n"
"v_add_f32 v241, v241, v198\n"
"ds_write_b64 v247, v[252:253] offset:2048\n"
"v_mfma_f32_16x16x32_bf16 v[204:207], v[224:227], v[112:115], v[204:207]\n"
"v_add_f32 v241, v241, v199\n"
"v_exp_f32 v200, v200\n"
"v_mfma_f32_16x16x32_bf16 v[204:207], v[228:231], v[116:119], v[204:207]\n"
"v_exp_f32 v201, v201\n"
"v_mfma_f32_16x16x32_bf16 v[204:207], v[232:235], v[120:123], v[204:207]\n"
"v_exp_f32 v202, v202\n"
"v_mfma_f32_16x16x32_bf16 v[204:207], v[236:239], v[124:127], v[204:207]\n"
"v_exp_f32 v203, v203\n"
"v_add_f32 v242, v242, v200\n"
"v_cvt_pk_bf16_f32 v254, v200, v201\n"
"v_add_f32 v242, v242, v201\n"
"v_cvt_pk_bf16_f32 v255, v202, v203\n"
"v_add_f32 v242, v242, v202\n"
"ds_write_b64 v247, v[254:255] offset:4096\n"
"v_add_f32 v242, v242, v203\n"
"v_exp_f32 v204, v204\n"
"v_exp_f32 v205, v205\n"
"v_exp_f32 v206, v206\n"
"v_exp_f32 v207, v207\n"
"v_add_f32 v243, v243, v204\n"
"v_cvt_pk_bf16_f32 v250, v204, v205\n"
"v_add_f32 v243, v243, v205\n"
"v_cvt_pk_bf16_f32 v251, v206, v207\n"
"v_add_f32 v243, v243, v206\n"
"ds_write_b64 v247, v[250:251] offset:6144\n"
"v_add_f32 v243, v243, v207\n"
"v_mov_b32 v208, v240\n"
"s_nop 1\n"
"v_permlane16_swap_b32 v240, v208\n"
"v_add_f32 v240, v240, v208\n"
"v_mov_b32 v208, v240\n"
"s_nop 1\n"
"v_permlane32_swap_b32 v240, v208\n"
"v_add_f32 v240, v240, v208\n"
"v_mov_b32 v209, v241\n"
"s_nop 1\n"
"v_permlane16_swap_b32 v241, v209\n"
"v_add_f32 v241, v241, v209\n"
"v_mov_b32 v209, v241\n"
"s_nop 1\n"
"v_permlane32_swap_b32 v241, v209\n"
"v_add_f32 v241, v241, v209\n"
"v_mov_b32 v210, v242\n"
"s_nop 1\n"
"v_permlane16_swap_b32 v242, v210\n"
"v_add_f32 v242, v242, v210\n"
"v_mov_b32 v210, v242\n"
"s_nop 1\n"
"v_permlane32_swap_b32 v242, v210\n"
"v_add_f32 v242, v242, v210\n"
"v_mov_b32 v211, v243\n"
"s_nop 1\n"
"v_permlane16_swap_b32 v243, v211\n"
"v_add_f32 v243, v243, v211\n"
"v_mov_b32 v211, v243\n"
"s_nop 1\n"
"v_permlane32_swap_b32 v243, v211\n"
"v_add_f32 v243, v243, v211\n"
"v_mbcnt_lo_u32_b32 v216, -1, 0\n"
"v_mbcnt_hi_u32_b32 v216, -1, v216\n"
"v_and_b32 v216, 15, v216\n"
"v_lshlrev_b32 v216, 4, v216\n"
"s_lshl_b32 s84, s87, 2\n"
"s_add_u32 s84, s84, 161792\n"
"v_add_u32 v216, s84, v216\n"
"ds_write_b32 v216, v240 offset:0\n"
"ds_write_b32 v216, v241 offset:256\n"
"ds_write_b32 v216, v242 offset:512\n"
"ds_write_b32 v216, v243 offset:768\n"
"s_setprio 0\n"
"s_waitcnt vmcnt(0) lgkmcnt(0)\n"
"s_barrier\n"
"ds_read_b128 v[208:211], v248 offset:0\n"
"ds_read_b64_tr_b16 v[192:193], v245 offset:34816\n"
"ds_read_b64_tr_b16 v[194:195], v245 offset:43520\n"
"ds_read_b128 v[212:215], v248 offset:1024\n"
"ds_read_b64_tr_b16 v[196:197], v245 offset:52224\n"
"ds_read_b64_tr_b16 v[198:199], v245 offset:60928\n"
"ds_read_b128 v[216:219], v248 offset:2048\n"
"ds_read_b128 v[220:223], v248 offset:3072\n"
"ds_read_b128 v[224:227], v248 offset:4096\n"
"ds_read_b128 v[228:231], v248 offset:5120\n"
"ds_read_b128 v[232:235], v248 offset:6144\n"
"ds_read_b128 v[236:239], v248 offset:7168\n"
"v_mbcnt_lo_u32_b32 v24, -1, 0\n"
"v_mbcnt_hi_u32_b32 v24, -1, v24\n"
"v_lshrrev_b32 v22, 5, v24\n"
"v_and_b32 v24, 31, v24\n"
"v_lshlrev_b32 v23, 1, v22\n"
"v_xor_b32 v24, v24, v23\n"
"v_mul_u32_u24 v22, 896, v22\n"
"v_lshrrev_b32 v23, 3, v24\n"
"v_mul_u32_u24 v23, 224, v23\n"
"v_add_u32 v22, v22, v23\n"
"v_and_b32 v24, 7, v24\n"
"v_lshl_add_u32 v24, v24, 4, v22\n"
"v_add_u32 v24, 57344, v24\n"
"v_mbcnt_lo_u32_b32 v20, -1, 0\n"
"v_mbcnt_hi_u32_b32 v20, -1, v20\n"
"v_and_b32 v20, 15, v20\n"
"v_lshlrev_b32 v20, 4, v20\n"
"v_add_u32 v20, 161792, v20\n"
"ds_read_b128 v[0:3], v20 offset:0\n"
"ds_read_b128 v[4:7], v20 offset:256\n"
"ds_read_b128 v[8:11], v20 offset:512\n"
"ds_read_b128 v[12:15], v20 offset:768\n"
"s_add_u32 m0, s56, 69632\n"
"ds_read_b64_tr_b16 v[200:201], v246 offset:34816\n"
"buffer_load_dwordx4 v24, s[48:51], s36 offen lds\n"
"s_add_u32 m0, s57, 69632\n"
"ds_read_b64_tr_b16 v[202:203], v246 offset:43520\n"
"buffer_load_dwordx4 v24, s[48:51], s37 offen lds\n"
"s_add_u32 m0, s58, 69632\n"
"ds_read_b64_tr_b16 v[204:205], v246 offset:52224\n"
"buffer_load_dwordx4 v24, s[48:51], s38 offen lds\n"
"s_add_u32 m0, s59, 69632\n"
"ds_read_b64_tr_b16 v[206:207], v246 offset:60928\n"
"buffer_load_dwordx4 v24, s[48:51], s39 offen lds\n"
"s_add_u32 m0, s60, 69632\n"
"s_nop 0\n"
"buffer_load_dwordx4 v24, s[48:51], s40 offen lds\n"
"s_add_u32 m0, s61, 69632\n"
"s_nop 0\n"
"buffer_load_dwordx4 v24, s[48:51], s41 offen lds\n"
"s_add_u32 m0, s62, 69632\n"
"s_nop 0\n"
"buffer_load_dwordx4 v24, s[48:51], s42 offen lds\n"
"s_add_u32 m0, s63, 69632\n"
"s_nop 0\n"
"buffer_load_dwordx4 v24, s[48:51], s43 offen lds\n"
"s_waitcnt lgkmcnt(15)\n"
"v_mfma_f32_16x16x32_bf16 v[128:131], v[192:195], v[208:211], v[128:131]\n"
"s_waitcnt lgkmcnt(14)\n"
"v_mfma_f32_16x16x32_bf16 v[128:131], v[196:199], v[212:215], v[128:131]\n"
"s_waitcnt lgkmcnt(13)\n"
"v_mfma_f32_16x16x32_bf16 v[144:147], v[192:195], v[216:219], v[144:147]\n"
"s_waitcnt lgkmcnt(12)\n"
"v_mfma_f32_16x16x32_bf16 v[144:147], v[196:199], v[220:223], v[144:147]\n"
"s_waitcnt lgkmcnt(11)\n"
"v_mfma_f32_16x16x32_bf16 v[160:163], v[192:195], v[224:227], v[160:163]\n"
"s_waitcnt lgkmcnt(10)\n"
"v_mfma_f32_16x16x32_bf16 v[160:163], v[196:199], v[228:231], v[160:163]\n"
"s_waitcnt lgkmcnt(9)\n"
"v_mfma_f32_16x16x32_bf16 v[176:179], v[192:195], v[232:235], v[176:179]\n"
"s_waitcnt lgkmcnt(8)\n"
"v_mfma_f32_16x16x32_bf16 v[176:179], v[196:199], v[236:239], v[176:179]\n"
"ds_read_b64_tr_b16 v[192:193], v245 offset:34880\n"
"ds_read_b64_tr_b16 v[194:195], v245 offset:43584\n"
"ds_read_b64_tr_b16 v[196:197], v245 offset:52288\n"
"ds_read_b64_tr_b16 v[198:199], v245 offset:60992\n"
"s_waitcnt lgkmcnt(6)\n"
"v_mfma_f32_16x16x32_bf16 v[132:135], v[200:203], v[208:211], v[132:135]\n"
"s_waitcnt lgkmcnt(4)\n"
"v_mfma_f32_16x16x32_bf16 v[132:135], v[204:207], v[212:215], v[132:135]\n"
"v_mfma_f32_16x16x32_bf16 v[148:151], v[200:203], v[216:219], v[148:151]\n"
"v_mfma_f32_16x16x32_bf16 v[148:151], v[204:207], v[220:223], v[148:151]\n"
"v_mfma_f32_16x16x32_bf16 v[164:167], v[200:203], v[224:227], v[164:167]\n"
"v_mfma_f32_16x16x32_bf16 v[164:167], v[204:207], v[228:231], v[164:167]\n"
"v_mfma_f32_16x16x32_bf16 v[180:183], v[200:203], v[232:235], v[180:183]\n"
"v_mfma_f32_16x16x32_bf16 v[180:183], v[204:207], v[236:239], v[180:183]\n"
"ds_read_b64_tr_b16 v[200:201], v246 offset:34880\n"
"ds_read_b64_tr_b16 v[202:203], v246 offset:43584\n"
"ds_read_b64_tr_b16 v[204:205], v246 offset:52288\n"
"ds_read_b64_tr_b16 v[206:207], v246 offset:60992\n"
"s_waitcnt lgkmcnt(6)\n"
"v_mfma_f32_16x16x32_bf16 v[136:139], v[192:195], v[208:211], v[136:139]\n"
"s_waitcnt lgkmcnt(4)\n"
"v_mfma_f32_16x16x32_bf16 v[136:139], v[196:199], v[212:215], v[136:139]\n"
"v_mfma_f32_16x16x32_bf16 v[152:155], v[192:195], v[216:219], v[152:155]\n"
"v_mfma_f32_16x16x32_bf16 v[152:155], v[196:199], v[220:223], v[152:155]\n"
"v_mfma_f32_16x16x32_bf16 v[168:171], v[192:195], v[224:227], v[168:171]\n"
"v_mfma_f32_16x16x32_bf16 v[168:171], v[196:199], v[228:231], v[168:171]\n"
"v_mfma_f32_16x16x32_bf16 v[184:187], v[192:195], v[232:235], v[184:187]\n"
"v_mfma_f32_16x16x32_bf16 v[184:187], v[196:199], v[236:239], v[184:187]\n"
"s_waitcnt lgkmcnt(2)\n"
"v_mfma_f32_16x16x32_bf16 v[140:143], v[200:203], v[208:211], v[140:143]\n"
"s_waitcnt lgkmcnt(0)\n"
"v_mfma_f32_16x16x32_bf16 v[140:143], v[204:207], v[212:215], v[140:143]\n"
"v_mfma_f32_16x16x32_bf16 v[156:159], v[200:203], v[216:219], v[156:159]\n"
"v_mfma_f32_16x16x32_bf16 v[156:159], v[204:207], v[220:223], v[156:159]\n"
"v_mfma_f32_16x16x32_bf16 v[172:175], v[200:203], v[224:227], v[172:175]\n"
"v_mfma_f32_16x16x32_bf16 v[172:175], v[204:207], v[228:231], v[172:175]\n"
"v_mfma_f32_16x16x32_bf16 v[188:191], v[200:203], v[232:235], v[188:191]\n"
"v_mfma_f32_16x16x32_bf16 v[188:191], v[204:207], v[236:239], v[188:191]\n"
"s_barrier\n"
"L_postA:\n"
"v_add_f32 v0, v0, v1\n"
"v_add_f32 v2, v2, v3\n"
"v_add_f32 v0, v0, v2\n"
"v_add_f32 v4, v4, v5\n"
"v_add_f32 v6, v6, v7\n"
"v_add_f32 v4, v4, v6\n"
"v_add_f32 v8, v8, v9\n"
"v_add_f32 v10, v10, v11\n"
"v_add_f32 v8, v8, v10\n"
"v_add_f32 v12, v12, v13\n"
"v_add_f32 v14, v14, v15\n"
"v_add_f32 v12, v12, v14\n"
"s_mov_b64 s[84:85], 0\n"
"v_cmp_nle_f32 vcc, 0x18000000, v0\n"
"s_nop 1\n"
"s_or_b64 s[84:85], s[84:85], vcc\n"
"v_cmp_nge_f32 vcc, 0x5f800000, v0\n"
"s_nop 1\n"
"s_or_b64 s[84:85], s[84:85], vcc\n"
"v_cmp_nle_f32 vcc, 0x18000000, v4\n"
"s_nop 1\n"
"s_or_b64 s[84:85], s[84:85], vcc\n"
"v_cmp_nge_f32 vcc, 0x5f800000, v4\n"
"s_nop 1\n"
"s_or_b64 s[84:85], s[84:85], vcc\n"
"v_cmp_nle_f32 vcc, 0x18000000, v8\n"
"s_nop 1\n"
"s_or_b64 s[84:85], s[84:85], vcc\n"
"v_cmp_nge_f32 vcc, 0x5f800000, v8\n"
"s_nop 1\n"
"s_or_b64 s[84:85], s[84:85], vcc\n"
"v_cmp_nle_f32 vcc, 0x18000000, v12\n"
"s_nop 1\n"
"s_or_b64 s[84:85], s[84:85], vcc\n"
"v_cmp_nge_f32 vcc, 0x5f800000, v12\n"
"s_nop 1\n"
"s_or_b64 s[84:85], s[84:85], vcc\n"
"v_rcp_f32 v0, v0\n"
"v_rcp_f32 v4, v4\n"
"v_rcp_f32 v8, v8\n"
"v_rcp_f32 v12, v12\n"
"v_mov_b64 v[32:33], 0\n"
"v_mov_b64 v[34:35], 0\n"
"v_mov_b32 v1, v0\n"
"v_mov_b32 v5, v4\n"
"v_mov_b32 v9, v8\n"
"v_mov_b32 v13, v12\n"
"v_pk_fma_f32 v[32:33], v[128:129], v[34:35], v[32:33]\n"
"v_pk_mul_f32 v[128:129], v[128:129], v[0:1]\n"
"v_pk_fma_f32 v[32:33], v[130:131], v[34:35], v[32:33]\n"
"v_pk_mul_f32 v[130:131], v[130:131], v[0:1]\n"
"v_pk_fma_f32 v[32:33], v[132:133], v[34:35], v[32:33]\n"
"v_pk_mul_f32 v[132:133], v[132:133], v[0:1]\n"
"v_pk_fma_f32 v[32:33], v[134:135], v[34:35], v[32:33]\n"
"v_pk_mul_f32 v[134:135], v[134:135], v[0:1]\n"
"v_pk_fma_f32 v[32:33], v[136:137], v[34:35], v[32:33]\n"
"v_pk_mul_f32 v[136:137], v[136:137], v[0:1]\n"
"v_pk_fma_f32 v[32:33], v[138:139], v[34:35], v[32:33]\n"
"v_pk_mul_f32 v[138:139], v[138:139], v[0:1]\n"
"v_pk_fma_f32 v[32:33], v[140:141], v[34:35], v[32:33]\n"
"v_pk_mul_f32 v[140:141], v[140:141], v[0:1]\n"
"v_pk_fma_f32 v[32:33], v[142:143], v[34:35], v[32:33]\n"
"v_pk_mul_f32 v[142:143], v[142:143], v[0:1]\n"
"v_pk_fma_f32 v[32:33], v[144:145], v[34:35], v[32:33]\n"
"v_pk_mul_f32 v[144:145], v[144:145], v[4:5]\n"
"v_pk_fma_f32 v[32:33], v[146:147], v[34:35], v[32:33]\n"
"v_pk_mul_f32 v[146:147], v[146:147], v[4:5]\n"
"v_pk_fma_f32 v[32:33], v[148:149], v[34:35], v[32:33]\n"
"v_pk_mul_f32 v[148:149], v[148:149], v[4:5]\n"
"v_pk_fma_f32 v[32:33], v[150:151], v[34:35], v[32:33]\n"
"v_pk_mul_f32 v[150:151], v[150:151], v[4:5]\n"
"v_pk_fma_f32 v[32:33], v[152:153], v[34:35], v[32:33]\n"
"v_pk_mul_f32 v[152:153], v[152:153], v[4:5]\n"
"v_pk_fma_f32 v[32:33], v[154:155], v[34:35], v[32:33]\n"
"v_pk_mul_f32 v[154:155], v[154:155], v[4:5]\n"
"v_pk_fma_f32 v[32:33], v[156:157], v[34:35], v[32:33]\n"
"v_pk_mul_f32 v[156:157], v[156:157], v[4:5]\n"
"v_pk_fma_f32 v[32:33], v[158:159], v[34:35], v[32:33]\n"
"v_pk_mul_f32 v[158:159], v[158:159], v[4:5]\n"
"v_pk_fma_f32 v[32:33], v[160:161], v[34:35], v[32:33]\n"
"v_pk_mul_f32 v[160:161], v[160:161], v[8:9]\n"
"v_pk_fma_f32 v[32:33], v[162:163], v[34:35], v[32:33]\n"
"v_pk_mul_f32 v[162:163], v[162:163], v[8:9]\n"
"v_pk_fma_f32 v[32:33], v[164:165], v[34:35], v[32:33]\n"
"v_pk_mul_f32 v[164:165], v[164:165], v[8:9]\n"
"v_pk_fma_f32 v[32:33], v[166:167], v[34:35], v[32:33]\n"
"v_pk_mul_f32 v[166:167], v[166:167], v[8:9]\n"
"v_pk_fma_f32 v[32:33], v[168:169], v[34:35], v[32:33]\n"
"v_pk_mul_f32 v[168:169], v[168:169], v[8:9]\n"
"v_pk_fma_f32 v[32:33], v[170:171], v[34:35], v[32:33]\n"
"v_pk_mul_f32 v[170:171], v[170:171], v[8:9]\n"
"v_pk_fma_f32 v[32:33], v[172:173], v[34:35], v[32:33]\n"
"v_pk_mul_f32 v[172:173], v[172:173], v[8:9]\n"
"v_pk_fma_f32 v[32:33], v[174:175], v[34:35], v[32:33]\n"
"v_pk_mul_f32 v[174:175], v[174:175], v[8:9]\n"
"v_pk_fma_f32 v[32:33], v[176:177], v[34:35], v[32:33]\n"
"v_pk_mul_f32 v[176:177], v[176:177], v[12:13]\n"
"v_pk_fma_f32 v[32:33], v[178:179], v[34:35], v[32:33]\n"
"v_pk_mul_f32 v[178:179], v[178:179], v[12:13]\n"
"v_pk_fma_f32 v[32:33], v[180:181], v[34:35], v[32:33]\n"
"v_pk_mul_f32 v[180:181], v[180:181], v[12:13]\n"
"v_pk_fma_f32 v[32:33], v[182:183], v[34:35], v[32:33]\n"
"v_pk_mul_f32 v[182:183], v[182:183], v[12:13]\n"
"v_pk_fma_f32 v[32:33], v[184:185], v[34:35], v[32:33]\n"
"v_pk_mul_f32 v[184:185], v[184:185], v[12:13]\n"
"v_pk_fma_f32 v[32:33], v[186:187], v[34:35], v[32:33]\n"
"v_pk_mul_f32 v[186:187], v[186:187], v[12:13]\n"
"v_pk_fma_f32 v[32:33], v[188:189], v[34:35], v[32:33]\n"
"v_pk_mul_f32 v[188:189], v[188:189], v[12:13]\n"
"v_pk_fma_f32 v[32:33], v[190:191], v[34:35], v[32:33]\n"
"v_pk_mul_f32 v[190:191], v[190:191], v[12:13]\n"
"v_add_f32 v32, v32, v33\n"
"v_cmp_u_f32 vcc, v32, v32\n"
"s_nop 1\n"
"s_or_b64 s[84:85], s[84:85], vcc\n"
"v_cvt_pk_bf16_f32 v192, v128, v129\n"
"v_cvt_pk_bf16_f32 v193, v130, v131\n"
"v_cvt_pk_bf16_f32 v194, v132, v133\n"
"v_cvt_pk_bf16_f32 v195, v134, v135\n"
"v_cvt_pk_bf16_f32 v196, v136, v137\n"
"v_cvt_pk_bf16_f32 v197, v138, v139\n"
"v_cvt_pk_bf16_f32 v198, v140, v141\n"
"v_cvt_pk_bf16_f32 v199, v142, v143\n"
"v_cvt_pk_bf16_f32 v200, v144, v145\n"
"v_cvt_pk_bf16_f32 v201, v146, v147\n"
"v_cvt_pk_bf16_f32 v202, v148, v149\n"
"v_cvt_pk_bf16_f32 v203, v150, v151\n"
"v_cvt_pk_bf16_f32 v204, v152, v153\n"
"v_cvt_pk_bf16_f32 v205, v154, v155\n"
"v_cvt_pk_bf16_f32 v206, v156, v157\n"
"v_cvt_pk_bf16_f32 v207, v158, v159\n"
"v_cvt_pk_bf16_f32 v208, v160, v161\n"
"v_cvt_pk_bf16_f32 v209, v162, v163\n"
"v_cvt_pk_bf16_f32 v210, v164, v165\n"
"v_cvt_pk_bf16_f32 v211, v166, v167\n"
"v_cvt_pk_bf16_f32 v212, v168, v169\n"
"v_cvt_pk_bf16_f32 v213, v170, v171\n"
"v_cvt_pk_bf16_f32 v214, v172, v173\n"
"v_cvt_pk_bf16_f32 v215, v174, v175\n"
"v_cvt_pk_bf16_f32 v216, v176, v177\n"
"v_cvt_pk_bf16_f32 v217, v178, v179\n"
"v_cvt_pk_bf16_f32 v218, v180, v181\n"
"v_cvt_pk_bf16_f32 v219, v182, v183\n"
"v_cvt_pk_bf16_f32 v220, v184, v185\n"
"v_cvt_pk_bf16_f32 v221, v186, v187\n"
"v_cvt_pk_bf16_f32 v222, v188, v189\n"
"v_cvt_pk_bf16_f32 v223, v190, v191\n"
"v_mbcnt_lo_u32_b32 v36, -1, 0\n"
"v_mbcnt_hi_u32_b32 v36, -1, v36\n"
"v_lshlrev_b32 v248, 4, v36\n"
"v_add_u32 v240, 104448, v248\n"
"s_lshl_b32 s89, s87, 11\n"
"v_add_u32 v241, s89, v240\n"
"v_subrev_u32 v242, 69632, v245\n"
"v_subrev_u32 v243, 69632, v246\n"
"v_mbcnt_lo_u32_b32 v244, -1, 0\n"
"v_mbcnt_hi_u32_b32 v244, -1, v244\n"
"v_lshrrev_b32 v40, 5, v244\n"
"v_and_b32 v244, 31, v244\n"
"v_lshlrev_b32 v41, 1, v40\n"
"v_xor_b32 v244, v244, v41\n"
"v_mul_u32_u24 v40, 896, v40\n"
"v_lshrrev_b32 v41, 3, v244\n"
"v_mul_u32_u24 v41, 224, v41\n"
"v_add_u32 v40, v40, v41\n"
"v_and_b32 v244, 7, v244\n"
"v_lshl_add_u32 v244, v244, 4, v40\n"
"v_add_u32 v244, 172032, v244\n"
"v_mbcnt_lo_u32_b32 v247, -1, 0\n"
"v_mbcnt_hi_u32_b32 v247, -1, v247\n"
"v_lshrrev_b32 v40, 5, v247\n"
"v_and_b32 v247, 31, v247\n"
"v_lshlrev_b32 v41, 1, v40\n"
"v_xor_b32 v247, v247, v41\n"
"v_mul_u32_u24 v40, 896, v40\n"
"v_lshrrev_b32 v41, 3, v247\n"
"v_mul_u32_u24 v41, 224, v41\n"
"v_add_u32 v40, v40, v41\n"
"v_and_b32 v247, 7, v247\n"
"v_min_u32 v247, 5, v247\n"
"v_lshl_add_u32 v247, v247, 4, v40\n"
"v_add_u32 v247, 128, v247\n"
"v_and_b32 v251, 15, v36\n"
"v_lshlrev_b32 v251, 8, v251\n"
"v_mov_b32 v249, 163584\n"
"v_mov_b32 v42, 1\n"
"s_barrier\n"
"s_cmp_eq_u64 s[84:85], 0\n"
"s_cbranch_scc1 L_okA\n"
"ds_write_b32 v249, v42\n"
"L_okA:\n"
"ds_write_b128 v241, v[192:195] offset:0\n"
"ds_write_b128 v241, v[196:199] offset:1024\n"
"ds_write_b128 v241, v[200:203] offset:8192\n"
"ds_write_b128 v241, v[204:207] offset:9216\n"
"ds_write_b128 v241, v[208:211] offset:16384\n"
"ds_write_b128 v241, v[212:215] offset:17408\n"
"ds_write_b128 v241, v[216:219] offset:24576\n"
"ds_write_b128 v241, v[220:223] offset:25600\n"
"v_mov_b64 v[128:129], 0\n"
"v_mov_b64 v[130:131], 0\n"
"v_mov_b64 v[132:133], 0\n"
"v_mov_b64 v[134:135], 0\n"
"v_mov_b64 v[136:137], 0\n"
"v_mov_b64 v[138:139], 0\n"
"v_mov_b64 v[140:141], 0\n"
"v_mov_b64 v[142:143], 0\n"
"v_mov_b64 v[144:145], 0\n"
"v_mov_b64 v[146:147], 0\n"
"v_mov_b64 v[148:149], 0\n"
"v_mov_b64 v[150:151], 0\n"
"v_mov_b64 v[152:153], 0\n"
"v_mov_b64 v[154:155], 0\n"
"v_mov_b64 v[156:157], 0\n"
"v_mov_b64 v[158:159], 0\n"
"v_mov_b64 v[160:161], 0\n"
"v_mov_b64 v[162:163], 0\n"
"v_mov_b64 v[164:165], 0\n"
"v_mov_b64 v[166:167], 0\n"
"v_mov_b64 v[168:169], 0\n"
"v_mov_b64 v[170:171], 0\n"
"v_mov_b64 v[172:173], 0\n"
"v_mov_b64 v[174:175], 0\n"
"v_mov_b64 v[176:177], 0\n"
"v_mov_b64 v[178:179], 0\n"
"v_mov_b64 v[180:181], 0\n"
"v_mov_b64 v[182:183], 0\n"
"v_mov_b64 v[184:185], 0\n"
"v_mov_b64 v[186:187], 0\n"
"v_mov_b64 v[188:189], 0\n"
"v_mov_b64 v[190:191], 0\n"
"v_mov_b64 v[0:1], 0\n"
"v_mov_b64 v[2:3], 0\n"
"v_mov_b64 v[4:5], 0\n"
"v_mov_b64 v[6:7], 0\n"
"v_mov_b64 v[8:9], 0\n"
"v_mov_b64 v[10:11], 0\n"
"v_mov_b64 v[12:13], 0\n"
"v_mov_b64 v[14:15], 0\n"
"v_mov_b64 v[16:17], 0\n"
"v_mov_b64 v[18:19], 0\n"
"v_mov_b64 v[20:21], 0\n"
"v_mov_b64 v[22:23], 0\n"
"v_mov_b64 v[24:25], 0\n"
"v_mov_b64 v[26:27], 0\n"
"v_mov_b64 v[28:29], 0\n"
"v_mov_b64 v[30:31], 0\n"
"v_mov_b64 v[32:33], 0\n"
"v_mov_b64 v[34:35], 0\n"
"v_mov_b64 v[36:37], 0\n"
"v_mov_b64 v[38:39], 0\n"
"v_mov_b64 v[40:41], 0\n"
"v_mov_b64 v[42:43], 0\n"
"v_mov_b64 v[44:45], 0\n"
"v_mov_b64 v[46:47], 0\n"
"s_mov_b64 s[52:53], %5\n"
"s_and_b32 s53, s53, 0xffff\n"
"s_mov_b32 s54, 1638400\n"
"s_mov_b32 s55, 0x20000\n"
"s_mov_b64 s[72:73], %6\n"
"s_and_b32 s73, s73, 0xffff\n"
"s_mov_b32 s74, 8448\n"
"s_mov_b32 s75, 0x20000\n"
"s_waitcnt vmcnt(0) lgkmcnt(0)\n"
"s_barrier\n"
"ds_read_b32 v250, v249\n"
"s_waitcnt lgkmcnt(0)\n"
"v_readfirstlane_b32 s84, v250\n"
"s_xor_b32 s85, s91, 1\n"
"s_nop 0\n"
"s_and_b32 s84, s84, s85\n"
"s_cmp_lg_u32 s84, 0\n"
"s_cbranch_scc1 L_fbA\n"
"ds_read_b128 v[208:211], v240 offset:0\n"
"ds_read_b128 v[212:215], v240 offset:1024\n"
"ds_read_b128 v[216:219], v240 offset:8192\n"
"ds_read_b128 v[220:223], v240 offset:9216\n"
"ds_read_b128 v[224:227], v240 offset:16384\n"
"ds_read_b128 v[228:231], v240 offset:17408\n"
"ds_read_b128 v[232:235], v240 offset:24576\n"
"ds_read_b128 v[236:239], v240 offset:25600\n"
"ds_read_b64_tr_b16 v[192:193], v242 offset:34816\n"
"ds_read_b64_tr_b16 v[194:195], v242 offset:43520\n"
"ds_read_b64_tr_b16 v[196:197], v242 offset:52224\n"
"ds_read_b64_tr_b16 v[198:199], v242 offset:60928\n"
"ds_read_b64_tr_b16 v[200:201], v243 offset:34816\n"
"ds_read_b64_tr_b16 v[202:203], v243 offset:43520\n"
"ds_read_b64_tr_b16 v[204:205], v243 offset:52224\n"
"ds_read_b64_tr_b16 v[206:207], v243 offset:60928\n"
"s_waitcnt lgkmcnt(6)\n"
"v_mfma_f32_16x16x32_bf16 v[128:131], v[192:195], v[208:211], v[128:131]\n"
"s_waitcnt lgkmcnt(4)\n"
"v_mfma_f32_16x16x32_bf16 v[128:131], v[196:199], v[212:215], v[128:131]\n"
"v_mfma_f32_16x16x32_bf16 v[144:147], v[192:195], v[216:219], v[144:147]\n"
"v_mfma_f32_16x16x32_bf16 v[144:147], v[196:199], v[220:223], v[144:147]\n"
"v_mfma_f32_16x16x32_bf16 v[160:163], v[192:195], v[224:227], v[160:163]\n"
"v_mfma_f32_16x16x32_bf16 v[160:163], v[196:199], v[228:231], v[160:163]\n"
"v_mfma_f32_16x16x32_bf16 v[176:179], v[192:195], v[232:235], v[176:179]\n"
"v_mfma_f32_16x16x32_bf16 v[176:179], v[196:199], v[236:239], v[176:179]\n"
"ds_read_b64_tr_b16 v[192:193], v242 offset:34880\n"
"ds_read_b64_tr_b16 v[194:195], v242 offset:43584\n"
"ds_read_b64_tr_b16 v[196:197], v242 offset:52288\n"
"ds_read_b64_tr_b16 v[198:199], v242 offset:60992\n"
"s_waitcnt lgkmcnt(6)\n"
"v_mfma_f32_16x16x32_bf16 v[132:135], v[200:203], v[208:211], v[132:135]\n"
"s_waitcnt lgkmcnt(4)\n"
"v_mfma_f32_16x16x32_bf16 v[132:135], v[204:207], v[212:215], v[132:135]\n"
"v_mfma_f32_16x16x32_bf16 v[148:151], v[200:203], v[216:219], v[148:151]\n"
"v_mfma_f32_16x16x32_bf16 v[148:151], v[204:207], v[220:223], v[148:151]\n"
"v_mfma_f32_16x16x32_bf16 v[164:167], v[200:203], v[224:227], v[164:167]\n"
"v_mfma_f32_16x16x32_bf16 v[164:167], v[204:207], v[228:231], v[164:167]\n"
"v_mfma_f32_16x16x32_bf16 v[180:183], v[200:203], v[232:235], v[180:183]\n"
"v_mfma_f32_16x16x32_bf16 v[180:183], v[204:207], v[236:239], v[180:183]\n"
"ds_read_b64_tr_b16 v[200:201], v243 offset:34880\n"
"ds_read_b64_tr_b16 v[202:203], v243 offset:43584\n"
"ds_read_b64_tr_b16 v[204:205], v243 offset:52288\n"
"ds_read_b64_tr_b16 v[206:207], v243 offset:60992\n"
"s_waitcnt lgkmcnt(6)\n"
"v_mfma_f32_16x16x32_bf16 v[136:139], v[192:195], v[208:211], v[136:139]\n"
"ds_read_b128 v[64:67], v240 offset:2048\n"
"s_waitcnt lgkmcnt(5)\n"
"v_mfma_f32_16x16x32_bf16 v[136:139], v[196:199], v[212:215], v[136:139]\n"
"ds_read_b128 v[68:71], v240 offset:3072\n"
"v_mfma_f32_16x16x32_bf16 v[152:155], v[192:195], v[216:219], v[152:155]\n"
"ds_read_b128 v[72:75], v240 offset:10240\n"
"v_mfma_f32_16x16x32_bf16 v[152:155], v[196:199], v[220:223], v[152:155]\n"
"ds_read_b128 v[76:79], v240 offset:11264\n"
"v_mfma_f32_16x16x32_bf16 v[168:171], v[192:195], v[224:227], v[168:171]\n"
"ds_read_b128 v[80:83], v240 offset:18432\n"
"v_mfma_f32_16x16x32_bf16 v[168:171], v[196:199], v[228:231], v[168:171]\n"
"ds_read_b128 v[84:87], v240 offset:19456\n"
"v_mfma_f32_16x16x32_bf16 v[184:187], v[192:195], v[232:235], v[184:187]\n"
"ds_read_b128 v[88:91], v240 offset:26624\n"
"v_mfma_f32_16x16x32_bf16 v[184:187], v[196:199], v[236:239], v[184:187]\n"
"ds_read_b128 v[92:95], v240 offset:27648\n"
"s_waitcnt lgkmcnt(10)\n"
"v_mfma_f32_16x16x32_bf16 v[140:143], v[200:203], v[208:211], v[140:143]\n"
"s_waitcnt lgkmcnt(8)\n"
"v_mfma_f32_16x16x32_bf16 v[140:143], v[204:207], v[212:215], v[140:143]\n"
"v_mfma_f32_16x16x32_bf16 v[156:159], v[200:203], v[216:219], v[156:159]\n"
"v_mfma_f32_16x16x32_bf16 v[156:159], v[204:207], v[220:223], v[156:159]\n"
"s_waitcnt lgkmcnt(0)\n"
"s_waitcnt vmcnt(0) lgkmcnt(0)\n"
"s_barrier\n"
"v_mfma_f32_16x16x32_bf16 v[172:175], v[200:203], v[224:227], v[172:175]\n"
"ds_read_b64_tr_b16 v[192:193], v245 offset:0\n"
"v_mfma_f32_16x16x32_bf16 v[172:175], v[204:207], v[228:231], v[172:175]\n"
"ds_read_b64_tr_b16 v[194:195], v245 offset:8704\n"
"v_mfma_f32_16x16x32_bf16 v[188:191], v[200:203], v[232:235], v[188:191]\n"
"ds_read_b64_tr_b16 v[196:197], v245 offset:17408\n"
"v_mfma_f32_16x16x32_bf16 v[188:191], v[204:207], v[236:239], v[188:191]\n"
"ds_read_b64_tr_b16 v[198:199], v245 offset:26112\n"
"ds_read_b64_tr_b16 v[200:201], v246 offset:0\n"
"ds_read_b64_tr_b16 v[202:203], v246 offset:8704\n"
"ds_read_b64_tr_b16 v[204:205], v246 offset:17408\n"
"ds_read_b64_tr_b16 v[206:207], v246 offset:26112\n"
"s_waitcnt lgkmcnt(6)\n"
"v_mfma_f32_16x16x32_bf16 v[128:131], v[192:195], v[64:67], v[128:131]\n"
"s_add_u32 m0, s56, 34816\n"
"s_nop 0\n"
"buffer_load_dwordx4 v244, s[48:51], s36 offen lds\n"
"s_waitcnt lgkmcnt(4)\n"
"v_mfma_f32_16x16x32_bf16 v[128:131], v[196:199], v[68:71], v[128:131]\n"
"v_mfma_f32_16x16x32_bf16 v[144:147], v[192:195], v[72:75], v[144:147]\n"
"v_mfma_f32_16x16x32_bf16 v[144:147], v[196:199], v[76:79], v[144:147]\n"
"s_add_u32 m0, s57, 34816\n"
"s_nop 0\n"
"buffer_load_dwordx4 v244, s[48:51], s37 offen lds\n"
"v_mfma_f32_16x16x32_bf16 v[160:163], v[192:195], v[80:83], v[160:163]\n"
"v_mfma_f32_16x16x32_bf16 v[160:163], v[196:199], v[84:87], v[160:163]\n"
"v_mfma_f32_16x16x32_bf16 v[176:179], v[192:195], v[88:91], v[176:179]\n"
"s_add_u32 m0, s58, 34816\n"
"s_nop 0\n"
"buffer_load_dwordx4 v244, s[48:51], s38 offen lds\n"
"v_mfma_f32_16x16x32_bf16 v[176:179], v[196:199], v[92:95], v[176:179]\n"
"ds_read_b64_tr_b16 v[192:193], v245 offset:64\n"
"ds_read_b64_tr_b16 v[194:195], v245 offset:8768\n"
"ds_read_b64_tr_b16 v[196:197], v245 offset:17472\n"
"ds_read_b64_tr_b16 v[198:199], v245 offset:26176\n"
"s_waitcnt lgkmcnt(6)\n"
"v_mfma_f32_16x16x32_bf16 v[132:135], v[200:203], v[64:67], v[132:135]\n"
"s_waitcnt lgkmcnt(4)\n"
"v_mfma_f32_16x16x32_bf16 v[132:135], v[204:207], v[68:71], v[132:135]\n"
"s_add_u32 m0, s59, 34816\n"
"s_nop 0\n"
"buffer_load_dwordx4 v244, s[48:51], s39 offen lds\n"
"v_add_u32 v244, 57344, v244\n"
"v_mfma_f32_16x16x32_bf16 v[148:151], v[200:203], v[72:75], v[148:151]\n"
"v_mfma_f32_16x16x32_bf16 v[148:151], v[204:207], v[76:79], v[148:151]\n"
"v_mfma_f32_16x16x32_bf16 v[164:167], v[200:203], v[80:83], v[164:167]\n"
"v_mfma_f32_16x16x32_bf16 v[164:167], v[204:207], v[84:87], v[164:167]\n"
"v_mfma_f32_16x16x32_bf16 v[180:183], v[200:203], v[88:91], v[180:183]\n"
"v_mfma_f32_16x16x32_bf16 v[180:183], v[204:207], v[92:95], v[180:183]\n"
"ds_read_b64_tr_b16 v[200:201], v246 offset:64\n"
"ds_read_b64_tr_b16 v[202:203], v246 offset:8768\n"
"ds_read_b64_tr_b16 v[204:205], v246 offset:17472\n"
"ds_read_b64_tr_b16 v[206:207], v246 offset:26176\n"
"s_waitcnt lgkmcnt(6)\n"
"v_mfma_f32_16x16x32_bf16 v[136:139], v[192:195], v[64:67], v[136:139]\n"
"ds_read_b128 v[208:211], v240 offset:4096\n"
"s_waitcnt lgkmcnt(5)\n"
"v_mfma_f32_16x16x32_bf16 v[136:139], v[196:199], v[68:71], v[136:139]\n"
"ds_read_b128 v[212:215], v240 offset:5120\n"
"v_mfma_f32_16x16x32_bf16 v[152:155], v[192:195], v[72:75], v[152:155]\n"
"ds_read_b128 v[216:219], v240 offset:12288\n"
"v_mfma_f32_16x16x32_bf16 v[152:155], v[196:199], v[76:79], v[152:155]\n"
"ds_read_b128 v[220:223], v240 offset:13312\n"
"v_mfma_f32_16x16x32_bf16 v[168:171], v[192:195], v[80:83], v[168:171]\n"
"ds_read_b128 v[224:227], v240 offset:20480\n"
"v_mfma_f32_16x16x32_bf16 v[168:171], v[196:199], v[84:87], v[168:171]\n"
"ds_read_b128 v[228:231], v240 offset:21504\n"
"v_mfma_f32_16x16x32_bf16 v[184:187], v[192:195], v[88:91], v[184:187]\n"
"ds_read_b128 v[232:235], v240 offset:28672\n"
"v_mfma_f32_16x16x32_bf16 v[184:187], v[196:199], v[92:95], v[184:187]\n"
"ds_read_b128 v[236:239], v240 offset:29696\n"
"s_waitcnt lgkmcnt(10)\n"
"v_mfma_f32_16x16x32_bf16 v[140:143], v[200:203], v[64:67], v[140:143]\n"
"s_waitcnt lgkmcnt(8)\n"
"v_mfma_f32_16x16x32_bf16 v[140:143], v[204:207], v[68:71], v[140:143]\n"
"v_mfma_f32_16x16x32_bf16 v[156:159], v[200:203], v[72:75], v[156:159]\n"
"v_mfma_f32_16x16x32_bf16 v[156:159], v[204:207], v[76:79], v[156:159]\n"
"s_waitcnt lgkmcnt(0)\n"
"s_waitcnt vmcnt(4) lgkmcnt(0)\n"
"s_barrier\n"
"v_mfma_f32_16x16x32_bf16 v[172:175], v[200:203], v[80:83], v[172:175]\n"
"ds_read_b64_tr_b16 v[192:193], v242 offset:0\n"
"v_mfma_f32_16x16x32_bf16 v[172:175], v[204:207], v[84:87], v[172:175]\n"
"ds_read_b64_tr_b16 v[194:195], v242 offset:8704\n"
"v_mfma_f32_16x16x32_bf16 v[188:191], v[200:203], v[88:91], v[188:191]\n"
"ds_read_b64_tr_b16 v[196:197], v242 offset:17408\n"
"v_mfma_f32_16x16x32_bf16 v[188:191], v[204:207], v[92:95], v[188:191]\n"
"ds_read_b64_tr_b16 v[198:199], v242 offset:26112\n"
"ds_read_b64_tr_b16 v[200:201], v243 offset:0\n"
"ds_read_b64_tr_b16 v[202:203], v243 offset:8704\n"
"ds_read_b64_tr_b16 v[204:205], v243 offset:17408\n"
"ds_read_b64_tr_b16 v[206:207], v243 offset:26112\n"
"s_waitcnt lgkmcnt(6)\n"
"v_mfma_f32_16x16x32_bf16 v[128:131], v[192:195], v[208:211], v[128:131]\n"
"s_add_u32 m0, s56, 69632\n"
"s_nop 0\n"
"buffer_load_dwordx4 v247, s[48:51], s36 offen lds\n"
"s_waitcnt lgkmcnt(4)\n"
"v_mfma_f32_16x16x32_bf16 v[128:131], v[196:199], v[212:215], v[128:131]\n"
"v_mfma_f32_16x16x32_bf16 v[144:147], v[192:195], v[216:219], v[144:147]\n"
"v_mfma_f32_16x16x32_bf16 v[144:147], v[196:199], v[220:223], v[144:147]\n"
"s_add_u32 m0, s57, 69632\n"
"s_nop 0\n"
"buffer_load_dwordx4 v247, s[48:51], s37 offen lds\n"
"v_mfma_f32_16x16x32_bf16 v[160:163], v[192:195], v[224:227], v[160:163]\n"
"v_mfma_f32_16x16x32_bf16 v[160:163], v[196:199], v[228:231], v[160:163]\n"
"v_mfma_f32_16x16x32_bf16 v[176:179], v[192:195], v[232:235], v[176:179]\n"
"s_add_u32 m0, s58, 69632\n"
"s_nop 0\n"
"buffer_load_dwordx4 v247, s[48:51], s38 offen lds\n"
"v_mfma_f32_16x16x32_bf16 v[176:179], v[196:199], v[236:239], v[176:179]\n"
"ds_read_b64_tr_b16 v[192:193], v242 offset:64\n"
"ds_read_b64_tr_b16 v[194:195], v242 offset:8768\n"
"ds_read_b64_tr_b16 v[196:197], v242 offset:17472\n"
"ds_read_b64_tr_b16 v[198:199], v242 offset:26176\n"
"s_waitcnt lgkmcnt(6)\n"
"v_mfma_f32_16x16x32_bf16 v[132:135], v[200:203], v[208:211], v[132:135]\n"
"s_waitcnt lgkmcnt(4)\n"
"v_mfma_f32_16x16x32_bf16 v[132:135], v[204:207], v[212:215], v[132:135]\n"
"s_add_u32 m0, s59, 69632\n"
"s_nop 0\n"
"buffer_load_dwordx4 v247, s[48:51], s39 offen lds\n"
"v_add_u32 v247, 57344, v247\n"
"v_mfma_f32_16x16x32_bf16 v[148:151], v[200:203], v[216:219], v[148:151]\n"
"v_mfma_f32_16x16x32_bf16 v[148:151], v[204:207], v[220:223], v[148:151]\n"
"v_mfma_f32_16x16x32_bf16 v[164:167], v[200:203], v[224:227], v[164:167]\n"
"v_mfma_f32_16x16x32_bf16 v[164:167], v[204:207], v[228:231], v[164:167]\n"
"v_mfma_f32_16x16x32_bf16 v[180:183], v[200:203], v[232:235], v[180:183]\n"
"v_mfma_f32_16x16x32_bf16 v[180:183], v[204:207], v[236:239], v[180:183]\n"
"ds_read_b64_tr_b16 v[200:201], v243 offset:64\n"
"ds_read_b64_tr_b16 v[202:203], v243 offset:8768\n"
"ds_read_b64_tr_b16 v[204:205], v243 offset:17472\n"
"ds_read_b64_tr_b16 v[206:207], v243 offset:26176\n"
"s_waitcnt lgkmcnt(6)\n"
"v_mfma_f32_16x16x32_bf16 v[136:139], v[192:195], v[208:211], v[136:139]\n"
"ds_read_b128 v[64:67], v240 offset:6144\n"
"s_waitcnt lgkmcnt(5)\n"
"v_mfma_f32_16x16x32_bf16 v[136:139], v[196:199], v[212:215], v[136:139]\n"
"ds_read_b128 v[68:71], v240 offset:7168\n"
"v_mfma_f32_16x16x32_bf16 v[152:155], v[192:195], v[216:219], v[152:155]\n"
"ds_read_b128 v[72:75], v240 offset:14336\n"
"v_mfma_f32_16x16x32_bf16 v[152:155], v[196:199], v[220:223], v[152:155]\n"
"ds_read_b128 v[76:79], v240 offset:15360\n"
"v_mfma_f32_16x16x32_bf16 v[168:171], v[192:195], v[224:227], v[168:171]\n"
"ds_read_b128 v[80:83], v240 offset:22528\n"
"v_mfma_f32_16x16x32_bf16 v[168:171], v[196:199], v[228:231], v[168:171]\n"
"ds_read_b128 v[84:87], v240 offset:23552\n"
"v_mfma_f32_16x16x32_bf16 v[184:187], v[192:195], v[232:235], v[184:187]\n"
"ds_read_b128 v[88:91], v240 offset:30720\n"
"v_mfma_f32_16x16x32_bf16 v[184:187], v[196:199], v[236:239], v[184:187]\n"
"ds_read_b128 v[92:95], v240 offset:31744\n"
"s_waitcnt lgkmcnt(10)\n"
"v_mfma_f32_16x16x32_bf16 v[140:143], v[200:203], v[208:211], v[140:143]\n"
"s_waitcnt lgkmcnt(8)\n"
"v_mfma_f32_16x16x32_bf16 v[140:143], v[204:207], v[212:215], v[140:143]\n"
"v_mfma_f32_16x16x32_bf16 v[156:159], v[200:203], v[216:219], v[156:159]\n"
"v_mfma_f32_16x16x32_bf16 v[156:159], v[204:207], v[220:223], v[156:159]\n"
"s_waitcnt lgkmcnt(0)\n"
"s_waitcnt vmcnt(4) lgkmcnt(0)\n"
"s_barrier\n"
"v_mfma_f32_16x16x32_bf16 v[172:175], v[200:203], v[224:227], v[172:175]\n"
"ds_read_b64_tr_b16 v[192:193], v242 offset:34816\n"
"v_mfma_f32_16x16x32_bf16 v[172:175], v[204:207], v[228:231], v[172:175]\n"
"ds_read_b64_tr_b16 v[194:195], v242 offset:43520\n"
"v_mfma_f32_16x16x32_bf16 v[188:191], v[200:203], v[232:235], v[188:191]\n"
"ds_read_b64_tr_b16 v[196:197], v242 offset:52224\n"
"v_mfma_f32_16x16x32_bf16 v[188:191], v[204:207], v[236:239], v[188:191]\n"
"ds_read_b64_tr_b16 v[198:199], v242 offset:60928\n"
"ds_read_b64_tr_b16 v[200:201], v243 offset:34816\n"
"ds_read_b64_tr_b16 v[202:203], v243 offset:43520\n"
"ds_read_b64_tr_b16 v[204:205], v243 offset:52224\n"
"ds_read_b64_tr_b16 v[206:207], v243 offset:60928\n"
"s_waitcnt lgkmcnt(6)\n"
"v_mfma_f32_16x16x32_bf16 v[128:131], v[192:195], v[64:67], v[128:131]\n"
"s_add_u32 m0, s56, 0\n"
"s_nop 0\n"
"buffer_load_dwordx4 v247, s[48:51], s36 offen lds\n"
"s_waitcnt lgkmcnt(4)\n"
"v_mfma_f32_16x16x32_bf16 v[128:131], v[196:199], v[68:71], v[128:131]\n"
"v_mfma_f32_16x16x32_bf16 v[144:147], v[192:195], v[72:75], v[144:147]\n"
"v_mfma_f32_16x16x32_bf16 v[144:147], v[196:199], v[76:79], v[144:147]\n"
"s_add_u32 m0, s57, 0\n"
"s_nop 0\n"
"buffer_load_dwordx4 v247, s[48:51], s37 offen lds\n"
"v_mfma_f32_16x16x32_bf16 v[160:163], v[192:195], v[80:83], v[160:163]\n"
"v_mfma_f32_16x16x32_bf16 v[160:163], v[196:199], v[84:87], v[160:163]\n"
"v_mfma_f32_16x16x32_bf16 v[176:179], v[192:195], v[88:91], v[176:179]\n"
"s_add_u32 m0, s58, 0\n"
"s_nop 0\n"
"buffer_load_dwordx4 v247, s[48:51], s38 offen lds\n"
"v_mfma_f32_16x16x32_bf16 v[176:179], v[196:199], v[92:95], v[176:179]\n"
"ds_read_b64_tr_b16 v[192:193], v242 offset:34880\n"
"ds_read_b64_tr_b16 v[194:195], v242 offset:43584\n"
"ds_read_b64_tr_b16 v[196:197], v242 offset:52288\n"
"ds_read_b64_tr_b16 v[198:199], v242 offset:60992\n"
"s_waitcnt lgkmcnt(6)\n"
"v_mfma_f32_16x16x32_bf16 v[132:135], v[200:203], v[64:67], v[132:135]\n"
"s_waitcnt lgkmcnt(4)\n"
"v_mfma_f32_16x16x32_bf16 v[132:135], v[204:207], v[68:71], v[132:135]\n"
"s_add_u32 m0, s59, 0\n"
"s_nop 0\n"
"buffer_load_dwordx4 v247, s[48:51], s39 offen lds\n"
"v_add_u32 v247, 57344, v247\n"
"v_mfma_f32_16x16x32_bf16 v[148:151], v[200:203], v[72:75], v[148:151]\n"
"v_mfma_f32_16x16x32_bf16 v[148:151], v[204:207], v[76:79], v[148:151]\n"
"v_mfma_f32_16x16x32_bf16 v[164:167], v[200:203], v[80:83], v[164:167]\n"
"v_mfma_f32_16x16x32_bf16 v[164:167], v[204:207], v[84:87], v[164:167]\n"
"v_mfma_f32_16x16x32_bf16 v[180:183], v[200:203], v[88:91], v[180:183]\n"
"v_mfma_f32_16x16x32_bf16 v[180:183], v[204:207], v[92:95], v[180:183]\n"
"ds_read_b64_tr_b16 v[200:201], v243 offset:34880\n"
"ds_read_b64_tr_b16 v[202:203], v243 offset:43584\n"
"ds_read_b64_tr_b16 v[204:205], v243 offset:52288\n"
"ds_read_b64_tr_b16 v[206:207], v243 offset:60992\n"
"s_waitcnt lgkmcnt(6)\n"
"v_mfma_f32_16x16x32_bf16 v[136:139], v[192:195], v[64:67], v[136:139]\n"
"ds_read_b128 v[208:211], v240 offset:0\n"
"s_waitcnt lgkmcnt(5)\n"
"v_mfma_f32_16x16x32_bf16 v[136:139], v[196:199], v[68:71], v[136:139]\n"
"ds_read_b128 v[212:215], v240 offset:1024\n"
"v_mfma_f32_16x16x32_bf16 v[152:155], v[192:195], v[72:75], v[152:155]\n"
"ds_read_b128 v[216:219], v240 offset:8192\n"
"v_mfma_f32_16x16x32_bf16 v[152:155], v[196:199], v[76:79], v[152:155]\n"
"ds_read_b128 v[220:223], v240 offset:9216\n"
"v_mfma_f32_16x16x32_bf16 v[168:171], v[192:195], v[80:83], v[168:171]\n"
"ds_read_b128 v[224:227], v240 offset:16384\n"
"v_mfma_f32_16x16x32_bf16 v[168:171], v[196:199], v[84:87], v[168:171]\n"
"ds_read_b128 v[228:231], v240 offset:17408\n"
"v_mfma_f32_16x16x32_bf16 v[184:187], v[192:195], v[88:91], v[184:187]\n"
"ds_read_b128 v[232:235], v240 offset:24576\n"
"v_mfma_f32_16x16x32_bf16 v[184:187], v[196:199], v[92:95], v[184:187]\n"
"ds_read_b128 v[236:239], v240 offset:25600\n"
"s_waitcnt lgkmcnt(10)\n"
"v_mfma_f32_16x16x32_bf16 v[140:143], v[200:203], v[64:67], v[140:143]\n"
"s_waitcnt lgkmcnt(8)\n"
"v_mfma_f32_16x16x32_bf16 v[140:143], v[204:207], v[68:71], v[140:143]\n"
"v_mfma_f32_16x16x32_bf16 v[156:159], v[200:203], v[72:75], v[156:159]\n"
"v_mfma_f32_16x16x32_bf16 v[156:159], v[204:207], v[76:79], v[156:159]\n"
"s_waitcnt lgkmcnt(0)\n"
"s_waitcnt vmcnt(4) lgkmcnt(0)\n"
"s_barrier\n"
"v_mfma_f32_16x16x32_bf16 v[172:175], v[200:203], v[80:83], v[172:175]\n"
"ds_read_b64_tr_b16 v[192:193], v245 offset:0\n"
"v_mfma_f32_16x16x32_bf16 v[172:175], v[204:207], v[84:87], v[172:175]\n"
"ds_read_b64_tr_b16 v[194:195], v245 offset:8704\n"
"v_mfma_f32_16x16x32_bf16 v[188:191], v[200:203], v[88:91], v[188:191]\n"
"ds_read_b64_tr_b16 v[196:197], v245 offset:17408\n"
"v_mfma_f32_16x16x32_bf16 v[188:191], v[204:207], v[92:95], v[188:191]\n"
"ds_read_b64_tr_b16 v[198:199], v245 offset:26112\n"
"ds_read_b64_tr_b16 v[200:201], v246 offset:0\n"
"ds_read_b64_tr_b16 v[202:203], v246 offset:8704\n"
"ds_read_b64_tr_b16 v[204:205], v246 offset:17408\n"
"ds_read_b64_tr_b16 v[206:207], v246 offset:26112\n"
"s_waitcnt lgkmcnt(6)\n"
"v_mfma_f32_16x16x32_bf16 v[0:3], v[192:195], v[208:211], v[0:3]\n"
"s_waitcnt lgkmcnt(4)\n"
"v_mfma_f32_16x16x32_bf16 v[0:3], v[196:199], v[212:215], v[0:3]\n"
"s_add_u32 m0, s56, 34816\n"
"s_nop 0\n"
"buffer_load_dwordx4 v247, s[48:51], s36 offen lds\n"
"v_mfma_f32_16x16x32_bf16 v[12:15], v[192:195], v[216:219], v[12:15]\n"
"v_mfma_f32_16x16x32_bf16 v[12:15], v[196:199], v[220:223], v[12:15]\n"
"v_mfma_f32_16x16x32_bf16 v[24:27], v[192:195], v[224:227], v[24:27]\n"
"s_add_u32 m0, s57, 34816\n"
"s_nop 0\n"
"buffer_load_dwordx4 v247, s[48:51], s37 offen lds\n"
"v_mfma_f32_16x16x32_bf16 v[24:27], v[196:199], v[228:231], v[24:27]\n"
"v_mfma_f32_16x16x32_bf16 v[36:39], v[192:195], v[232:235], v[36:39]\n"
"v_mfma_f32_16x16x32_bf16 v[36:39], v[196:199], v[236:239], v[36:39]\n"
"ds_read_b64_tr_b16 v[192:193], v245 offset:64\n"
"ds_read_b64_tr_b16 v[194:195], v245 offset:8768\n"
"ds_read_b64_tr_b16 v[196:197], v245 offset:17472\n"
"ds_read_b64_tr_b16 v[198:199], v245 offset:26176\n"
"s_add_u32 m0, s58, 34816\n"
"s_nop 0\n"
"buffer_load_dwordx4 v247, s[48:51], s38 offen lds\n"
"s_waitcnt lgkmcnt(6)\n"
"v_mfma_f32_16x16x32_bf16 v[4:7], v[200:203], v[208:211], v[4:7]\n"
"ds_read_b128 v[64:67], v240 offset:2048\n"
"s_waitcnt lgkmcnt(5)\n"
"v_mfma_f32_16x16x32_bf16 v[4:7], v[204:207], v[212:215], v[4:7]\n"
"ds_read_b128 v[68:71], v240 offset:3072\n"
"v_mfma_f32_16x16x32_bf16 v[16:19], v[200:203], v[216:219], v[16:19]\n"
"ds_read_b128 v[72:75], v240 offset:10240\n"
"s_add_u32 m0, s59, 34816\n"
"s_nop 0\n"
"buffer_load_dwordx4 v247, s[48:51], s39 offen lds\n"
"v_add_u32 v247, 57344, v247\n"
"v_mfma_f32_16x16x32_bf16 v[16:19], v[204:207], v[220:223], v[16:19]\n"
"ds_read_b128 v[76:79], v240 offset:11264\n"
"v_mfma_f32_16x16x32_bf16 v[28:31], v[200:203], v[224:227], v[28:31]\n"
"ds_read_b128 v[80:83], v240 offset:18432\n"
"v_mfma_f32_16x16x32_bf16 v[28:31], v[204:207], v[228:231], v[28:31]\n"
"ds_read_b128 v[84:87], v240 offset:19456\n"
"s_bfe_u32 s85, %8, 0x80008\n"
"s_lshl_b32 s85, s85, 10\n"
"s_mul_i32 s89, s87, 131072\n"
"s_add_u32 s89, s89, 0\n"
"s_add_u32 s89, s89, s85\n"
"v_cvt_pk_bf16_f32 v48, v128, v129\n"
"v_cvt_pk_bf16_f32 v49, v130, v131\n"
"v_cvt_pk_bf16_f32 v50, v144, v145\n"
"v_cvt_pk_bf16_f32 v51, v146, v147\n"
"buffer_store_dwordx4 v[48:51], v248, s[52:55], s89 offen sc1\n"
"s_add_u32 s89, s89, 16384\n"
"v_mfma_f32_16x16x32_bf16 v[40:43], v[200:203], v[232:235], v[40:43]\n"
"ds_read_b128 v[88:91], v240 offset:26624\n"
"v_mfma_f32_16x16x32_bf16 v[40:43], v[204:207], v[236:239], v[40:43]\n"
"ds_read_b128 v[92:95], v240 offset:27648\n"
"s_waitcnt lgkmcnt(10)\n"
"v_mfma_f32_16x16x32_bf16 v[8:11], v[192:195], v[208:211], v[8:11]\n"
"s_waitcnt lgkmcnt(8)\n"
"v_mfma_f32_16x16x32_bf16 v[8:11], v[196:199], v[212:215], v[8:11]\n"
"v_mfma_f32_16x16x32_bf16 v[20:23], v[192:195], v[216:219], v[20:23]\n"
"v_mfma_f32_16x16x32_bf16 v[20:23], v[196:199], v[220:223], v[20:23]\n"
"s_waitcnt lgkmcnt(0)\n"
"s_waitcnt vmcnt(5) lgkmcnt(0)\n"
"s_barrier\n"
"v_mfma_f32_16x16x32_bf16 v[32:35], v[192:195], v[224:227], v[32:35]\n"
"ds_read_b64_tr_b16 v[200:201], v242 offset:0\n"
"v_mfma_f32_16x16x32_bf16 v[32:35], v[196:199], v[228:231], v[32:35]\n"
"ds_read_b64_tr_b16 v[202:203], v242 offset:8704\n"
"v_mfma_f32_16x16x32_bf16 v[44:47], v[192:195], v[232:235], v[44:47]\n"
"ds_read_b64_tr_b16 v[204:205], v242 offset:17408\n"
"v_mfma_f32_16x16x32_bf16 v[44:47], v[196:199], v[236:239], v[44:47]\n"
"ds_read_b64_tr_b16 v[206:207], v242 offset:26112\n"
"ds_read_b64_tr_b16 v[192:193], v243 offset:0\n"
"ds_read_b64_tr_b16 v[194:195], v243 offset:8704\n"
"ds_read_b64_tr_b16 v[196:197], v243 offset:17408\n"
"ds_read_b64_tr_b16 v[198:199], v243 offset:26112\n"
"s_waitcnt lgkmcnt(6)\n"
"v_mfma_f32_16x16x32_bf16 v[0:3], v[200:203], v[64:67], v[0:3]\n"
"s_waitcnt lgkmcnt(4)\n"
"v_mfma_f32_16x16x32_bf16 v[0:3], v[204:207], v[68:71], v[0:3]\n"
"s_add_u32 m0, s56, 69632\n"
"s_nop 0\n"
"buffer_load_dwordx4 v247, s[48:51], s36 offen lds\n"
"v_mfma_f32_16x16x32_bf16 v[12:15], v[200:203], v[72:75], v[12:15]\n"
"v_mfma_f32_16x16x32_bf16 v[12:15], v[204:207], v[76:79], v[12:15]\n"
"v_mfma_f32_16x16x32_bf16 v[24:27], v[200:203], v[80:83], v[24:27]\n"
"s_add_u32 m0, s57, 69632\n"
"s_nop 0\n"
"buffer_load_dwordx4 v247, s[48:51], s37 offen lds\n"
"v_mfma_f32_16x16x32_bf16 v[24:27], v[204:207], v[84:87], v[24:27]\n"
"v_mfma_f32_16x16x32_bf16 v[36:39], v[200:203], v[88:91], v[36:39]\n"
"v_mfma_f32_16x16x32_bf16 v[36:39], v[204:207], v[92:95], v[36:39]\n"
"ds_read_b64_tr_b16 v[200:201], v242 offset:64\n"
"ds_read_b64_tr_b16 v[202:203], v242 offset:8768\n"
"ds_read_b64_tr_b16 v[204:205], v242 offset:17472\n"
"ds_read_b64_tr_b16 v[206:207], v242 offset:26176\n"
"s_add_u32 m0, s58, 69632\n"
"s_nop 0\n"
"buffer_load_dwordx4 v247, s[48:51], s38 offen lds\n"
"s_waitcnt lgkmcnt(6)\n"
"v_mfma_f32_16x16x32_bf16 v[4:7], v[192:195], v[64:67], v[4:7]\n"
"ds_read_b128 v[208:211], v240 offset:4096\n"
"s_waitcnt lgkmcnt(5)\n"
"v_mfma_f32_16x16x32_bf16 v[4:7], v[196:199], v[68:71], v[4:7]\n"
"ds_read_b128 v[212:215], v240 offset:5120\n"
"v_mfma_f32_16x16x32_bf16 v[16:19], v[192:195], v[72:75], v[16:19]\n"
"ds_read_b128 v[216:219], v240 offset:12288\n"
"s_add_u32 m0, s59, 69632\n"
"s_nop 0\n"
"buffer_load_dwordx4 v247, s[48:51], s39 offen lds\n"
"v_add_u32 v247, 57344, v247\n"
"v_mfma_f32_16x16x32_bf16 v[16:19], v[196:199], v[76:79], v[16:19]\n"
"ds_read_b128 v[220:223], v240 offset:13312\n"
"v_mfma_f32_16x16x32_bf16 v[28:31], v[192:195], v[80:83], v[28:31]\n"
"ds_read_b128 v[224:227], v240 offset:20480\n"
"v_mfma_f32_16x16x32_bf16 v[28:31], v[196:199], v[84:87], v[28:31]\n"
"ds_read_b128 v[228:231], v240 offset:21504\n"
"v_cvt_pk_bf16_f32 v52, v132, v133\n"
"v_cvt_pk_bf16_f32 v53, v134, v135\n"
"v_cvt_pk_bf16_f32 v54, v148, v149\n"
"v_cvt_pk_bf16_f32 v55, v150, v151\n"
"buffer_store_dwordx4 v[52:55], v248, s[52:55], s89 offen sc1\n"
"s_add_u32 s89, s89, 16384\n"
"v_mfma_f32_16x16x32_bf16 v[40:43], v[192:195], v[88:91], v[40:43]\n"
"ds_read_b128 v[232:235], v240 offset:28672\n"
"v_mfma_f32_16x16x32_bf16 v[40:43], v[196:199], v[92:95], v[40:43]\n"
"ds_read_b128 v[236:239], v240 offset:29696\n"
"s_waitcnt lgkmcnt(10)\n"
"v_mfma_f32_16x16x32_bf16 v[8:11], v[200:203], v[64:67], v[8:11]\n"
"s_waitcnt lgkmcnt(8)\n"
"v_mfma_f32_16x16x32_bf16 v[8:11], v[204:207], v[68:71], v[8:11]\n"
"v_mfma_f32_16x16x32_bf16 v[20:23], v[200:203], v[72:75], v[20:23]\n"
"v_mfma_f32_16x16x32_bf16 v[20:23], v[204:207], v[76:79], v[20:23]\n"
"s_waitcnt lgkmcnt(0)\n"
"s_waitcnt vmcnt(6) lgkmcnt(0)\n"
"s_barrier\n"
"v_mfma_f32_16x16x32_bf16 v[32:35], v[200:203], v[80:83], v[32:35]\n"
"ds_read_b64_tr_b16 v[192:193], v242 offset:34816\n"
"v_mfma_f32_16x16x32_bf16 v[32:35], v[204:207], v[84:87], v[32:35]\n"
"ds_read_b64_tr_b16 v[194:195], v242 offset:43520\n"
"v_mfma_f32_16x16x32_bf16 v[44:47], v[200:203], v[88:91], v[44:47]\n"
"ds_read_b64_tr_b16 v[196:197], v242 offset:52224\n"
"v_mfma_f32_16x16x32_bf16 v[44:47], v[204:207], v[92:95], v[44:47]\n"
"ds_read_b64_tr_b16 v[198:199], v242 offset:60928\n"
"ds_read_b64_tr_b16 v[200:201], v243 offset:34816\n"
"ds_read_b64_tr_b16 v[202:203], v243 offset:43520\n"
"ds_read_b64_tr_b16 v[204:205], v243 offset:52224\n"
"ds_read_b64_tr_b16 v[206:207], v243 offset:60928\n"
"s_waitcnt lgkmcnt(6)\n"
"v_mfma_f32_16x16x32_bf16 v[0:3], v[192:195], v[208:211], v[0:3]\n"
"s_waitcnt lgkmcnt(4)\n"
"v_mfma_f32_16x16x32_bf16 v[0:3], v[196:199], v[212:215], v[0:3]\n"
"v_cvt_pk_bf16_f32 v56, v136, v137\n"
"v_cvt_pk_bf16_f32 v57, v138, v139\n"
"v_cvt_pk_bf16_f32 v58, v152, v153\n"
"v_cvt_pk_bf16_f32 v59, v154, v155\n"
"buffer_store_dwordx4 v[56:59], v248, s[52:55], s89 offen sc1\n"
"s_add_u32 s89, s89, 16384\n"
"v_mfma_f32_16x16x32_bf16 v[12:15], v[192:195], v[216:219], v[12:15]\n"
"v_mfma_f32_16x16x32_bf16 v[12:15], v[196:199], v[220:223], v[12:15]\n"
"v_mfma_f32_16x16x32_bf16 v[24:27], v[192:195], v[224:227], v[24:27]\n"
"v_mfma_f32_16x16x32_bf16 v[24:27], v[196:199], v[228:231], v[24:27]\n"
"v_mfma_f32_16x16x32_bf16 v[36:39], v[192:195], v[232:235], v[36:39]\n"
"v_mfma_f32_16x16x32_bf16 v[36:39], v[196:199], v[236:239], v[36:39]\n"
"ds_read_b64_tr_b16 v[192:193], v242 offset:34880\n"
"ds_read_b64_tr_b16 v[194:195], v242 offset:43584\n"
"ds_read_b64_tr_b16 v[196:197], v242 offset:52288\n"
"ds_read_b64_tr_b16 v[198:199], v242 offset:60992\n"
"v_cvt_pk_bf16_f32 v60, v140, v141\n"
"v_cvt_pk_bf16_f32 v61, v142, v143\n"
"v_cvt_pk_bf16_f32 v62, v156, v157\n"
"v_cvt_pk_bf16_f32 v63, v158, v159\n"
"buffer_store_dwordx4 v[60:63], v248, s[52:55], s89 offen sc1\n"
"s_add_u32 s89, s89, 16384\n"
"s_waitcnt lgkmcnt(6)\n"
"v_mfma_f32_16x16x32_bf16 v[4:7], v[200:203], v[208:211], v[4:7]\n"
"ds_read_b128 v[64:67], v240 offset:6144\n"
"s_waitcnt lgkmcnt(5)\n"
"v_mfma_f32_16x16x32_bf16 v[4:7], v[204:207], v[212:215], v[4:7]\n"
"ds_read_b128 v[68:71], v240 offset:7168\n"
"v_mfma_f32_16x16x32_bf16 v[16:19], v[200:203], v[216:219], v[16:19]\n"
"ds_read_b128 v[72:75], v240 offset:14336\n"
"v_mfma_f32_16x16x32_bf16 v[16:19], v[204:207], v[220:223], v[16:19]\n"
"ds_read_b128 v[76:79], v240 offset:15360\n"
"v_mfma_f32_16x16x32_bf16 v[28:31], v[200:203], v[224:227], v[28:31]\n"
"ds_read_b128 v[80:83], v240 offset:22528\n"
"v_mfma_f32_16x16x32_bf16 v[28:31], v[204:207], v[228:231], v[28:31]\n"
"ds_read_b128 v[84:87], v240 offset:23552\n"
"v_cvt_pk_bf16_f32 v48, v160, v161\n"
"v_cvt_pk_bf16_f32 v49, v162, v163\n"
"v_cvt_pk_bf16_f32 v50, v176, v177\n"
"v_cvt_pk_bf16_f32 v51, v178, v179\n"
"buffer_store_dwordx4 v[48:51], v248, s[52:55], s89 offen sc1\n"
"s_add_u32 s89, s89, 16384\n"
"v_mfma_f32_16x16x32_bf16 v[40:43], v[200:203], v[232:235], v[40:43]\n"
"ds_read_b128 v[88:91], v240 offset:30720\n"
"v_mfma_f32_16x16x32_bf16 v[40:43], v[204:207], v[236:239], v[40:43]\n"
"ds_read_b128 v[92:95], v240 offset:31744\n"
"s_waitcnt lgkmcnt(10)\n"
"v_mfma_f32_16x16x32_bf16 v[8:11], v[192:195], v[208:211], v[8:11]\n"
"s_waitcnt lgkmcnt(8)\n"
"v_mfma_f32_16x16x32_bf16 v[8:11], v[196:199], v[212:215], v[8:11]\n"
"v_mfma_f32_16x16x32_bf16 v[20:23], v[192:195], v[216:219], v[20:23]\n"
"v_mfma_f32_16x16x32_bf16 v[20:23], v[196:199], v[220:223], v[20:23]\n"
"s_waitcnt lgkmcnt(0)\n"
"s_waitcnt vmcnt(4) lgkmcnt(0)\n"
"s_barrier\n"
"v_mfma_f32_16x16x32_bf16 v[32:35], v[192:195], v[224:227], v[32:35]\n"
"ds_read_b64_tr_b16 v[200:201], v245 offset:0\n"
"v_mfma_f32_16x16x32_bf16 v[32:35], v[196:199], v[228:231], v[32:35]\n"
"ds_read_b64_tr_b16 v[202:203], v245 offset:8704\n"
"v_mfma_f32_16x16x32_bf16 v[44:47], v[192:195], v[232:235], v[44:47]\n"
"ds_read_b64_tr_b16 v[204:205], v245 offset:17408\n"
"v_mfma_f32_16x16x32_bf16 v[44:47], v[196:199], v[236:239], v[44:47]\n"
"ds_read_b64_tr_b16 v[206:207], v245 offset:26112\n"
"ds_read_b64_tr_b16 v[192:193], v246 offset:0\n"
"ds_read_b64_tr_b16 v[194:195], v246 offset:8704\n"
"ds_read_b64_tr_b16 v[196:197], v246 offset:17408\n"
"ds_read_b64_tr_b16 v[198:199], v246 offset:26112\n"
"s_waitcnt lgkmcnt(6)\n"
"v_mfma_f32_16x16x32_bf16 v[0:3], v[200:203], v[64:67], v[0:3]\n"
"s_waitcnt lgkmcnt(4)\n"
"v_mfma_f32_16x16x32_bf16 v[0:3], v[204:207], v[68:71], v[0:3]\n"
"v_cvt_pk_bf16_f32 v52, v164, v165\n"
"v_cvt_pk_bf16_f32 v53, v166, v167\n"
"v_cvt_pk_bf16_f32 v54, v180, v181\n"
"v_cvt_pk_bf16_f32 v55, v182, v183\n"
"buffer_store_dwordx4 v[52:55], v248, s[52:55], s89 offen sc1\n"
"s_add_u32 s89, s89, 16384\n"
"v_mfma_f32_16x16x32_bf16 v[12:15], v[200:203], v[72:75], v[12:15]\n"
"v_mfma_f32_16x16x32_bf16 v[12:15], v[204:207], v[76:79], v[12:15]\n"
"v_mfma_f32_16x16x32_bf16 v[24:27], v[200:203], v[80:83], v[24:27]\n"
"v_mfma_f32_16x16x32_bf16 v[24:27], v[204:207], v[84:87], v[24:27]\n"
"v_mfma_f32_16x16x32_bf16 v[36:39], v[200:203], v[88:91], v[36:39]\n"
"v_mfma_f32_16x16x32_bf16 v[36:39], v[204:207], v[92:95], v[36:39]\n"
"ds_read_b64_tr_b16 v[200:201], v245 offset:64\n"
"ds_read_b64_tr_b16 v[202:203], v245 offset:8768\n"
"ds_read_b64_tr_b16 v[204:205], v245 offset:17472\n"
"ds_read_b64_tr_b16 v[206:207], v245 offset:26176\n"
"s_waitcnt lgkmcnt(6)\n"
"v_mfma_f32_16x16x32_bf16 v[4:7], v[192:195], v[64:67], v[4:7]\n"
"v_cvt_pk_bf16_f32 v56, v168, v169\n"
"v_cvt_pk_bf16_f32 v57, v170, v171\n"
"v_cvt_pk_bf16_f32 v58, v184, v185\n"
"v_cvt_pk_bf16_f32 v59, v186, v187\n"
"buffer_store_dwordx4 v[56:59], v248, s[52:55], s89 offen sc1\n"
"s_add_u32 s89, s89, 16384\n"
"s_waitcnt lgkmcnt(4)\n"
"v_mfma_f32_16x16x32_bf16 v[4:7], v[196:199], v[68:71], v[4:7]\n"
"v_mfma_f32_16x16x32_bf16 v[16:19], v[192:195], v[72:75], v[16:19]\n"
"v_mfma_f32_16x16x32_bf16 v[16:19], v[196:199], v[76:79], v[16:19]\n"
"v_mfma_f32_16x16x32_bf16 v[28:31], v[192:195], v[80:83], v[28:31]\n"
"v_mfma_f32_16x16x32_bf16 v[28:31], v[196:199], v[84:87], v[28:31]\n"
"v_mfma_f32_16x16x32_bf16 v[40:43], v[192:195], v[88:91], v[40:43]\n"
"v_mfma_f32_16x16x32_bf16 v[40:43], v[196:199], v[92:95], v[40:43]\n"
"v_cvt_pk_bf16_f32 v60, v172, v173\n"
"v_cvt_pk_bf16_f32 v61, v174, v175\n"
"v_cvt_pk_bf16_f32 v62, v188, v189\n"
"v_cvt_pk_bf16_f32 v63, v190, v191\n"
"buffer_store_dwordx4 v[60:63], v248, s[52:55], s89 offen sc1\n"
"s_add_u32 s89, s89, 16384\n"
"s_waitcnt lgkmcnt(2)\n"
"v_mfma_f32_16x16x32_bf16 v[8:11], v[200:203], v[64:67], v[8:11]\n"
"s_waitcnt lgkmcnt(0)\n"
"v_mfma_f32_16x16x32_bf16 v[8:11], v[204:207], v[68:71], v[8:11]\n"
"v_mfma_f32_16x16x32_bf16 v[20:23], v[200:203], v[72:75], v[20:23]\n"
"v_mfma_f32_16x16x32_bf16 v[20:23], v[204:207], v[76:79], v[20:23]\n"
"v_mfma_f32_16x16x32_bf16 v[32:35], v[200:203], v[80:83], v[32:35]\n"
"v_mfma_f32_16x16x32_bf16 v[32:35], v[204:207], v[84:87], v[32:35]\n"
"v_mfma_f32_16x16x32_bf16 v[44:47], v[200:203], v[88:91], v[44:47]\n"
"v_mfma_f32_16x16x32_bf16 v[44:47], v[204:207], v[92:95], v[44:47]\n"
"s_waitcnt lgkmcnt(0)\n"
"s_waitcnt vmcnt(0)\n"
"s_barrier\n"
"s_cmp_lg_u32 s86, 0\n"
"s_cbranch_scc1 L_nfA1\n"
"v_mov_b32 v250, s88\n"
"s_bfe_u32 s84, %8, 0x80008\n"
"s_mul_i32 s84, s84, 256\n"
"v_mov_b32 v252, 0\n"
"s_mov_b64 exec, 1\n"
"buffer_store_dword v250, v252, s[72:75], s84 offen sc1\n"
"s_mov_b64 exec, -1\n"
"L_nfA1:\n"
"s_bfe_u32 s85, %8, 0x80008\n"
"s_lshl_b32 s85, s85, 10\n"
"s_mul_i32 s89, s87, 98304\n"
"s_add_u32 s89, s89, 917504\n"
"s_add_u32 s89, s89, s85\n"
"v_cvt_pk_bf16_f32 v48, v0, v1\n"
"v_cvt_pk_bf16_f32 v49, v2, v3\n"
"v_cvt_pk_bf16_f32 v50, v12, v13\n"
"v_cvt_pk_bf16_f32 v51, v14, v15\n"
"buffer_store_dwordx4 v[48:51], v248, s[52:55], s89 offen sc1\n"
"s_add_u32 s89, s89, 16384\n"
"v_cvt_pk_bf16_f32 v52, v4, v5\n"
"v_cvt_pk_bf16_f32 v53, v6, v7\n"
"v_cvt_pk_bf16_f32 v54, v16, v17\n"
"v_cvt_pk_bf16_f32 v55, v18, v19\n"
"buffer_store_dwordx4 v[52:55], v248, s[52:55], s89 offen sc1\n"
"s_add_u32 s89, s89, 16384\n"
"v_cvt_pk_bf16_f32 v56, v8, v9\n"
"v_cvt_pk_bf16_f32 v57, v10, v11\n"
"v_cvt_pk_bf16_f32 v58, v20, v21\n"
"v_cvt_pk_bf16_f32 v59, v22, v23\n"
"buffer_store_dwordx4 v[56:59], v248, s[52:55], s89 offen sc1\n"
"s_add_u32 s89, s89, 16384\n"
"v_cvt_pk_bf16_f32 v60, v24, v25\n"
"v_cvt_pk_bf16_f32 v61, v26, v27\n"
"v_cvt_pk_bf16_f32 v62, v36, v37\n"
"v_cvt_pk_bf16_f32 v63, v38, v39\n"
"buffer_store_dwordx4 v[60:63], v248, s[52:55], s89 offen sc1\n"
"s_add_u32 s89, s89, 16384\n"
"v_cvt_pk_bf16_f32 v48, v28, v29\n"
"v_cvt_pk_bf16_f32 v49, v30, v31\n"
"v_cvt_pk_bf16_f32 v50, v40, v41\n"
"v_cvt_pk_bf16_f32 v51, v42, v43\n"
"buffer_store_dwordx4 v[48:51], v248, s[52:55], s89 offen sc1\n"
"s_add_u32 s89, s89, 16384\n"
"v_cvt_pk_bf16_f32 v52, v32, v33\n"
"v_cvt_pk_bf16_f32 v53, v34, v35\n"
"v_cvt_pk_bf16_f32 v54, v44, v45\n"
"v_cvt_pk_bf16_f32 v55, v46, v47\n"
"buffer_store_dwordx4 v[52:55], v248, s[52:55], s89 offen sc1\n"
"s_add_u32 s89, s89, 16384\n"
"s_bfe_u32 s84, %8, 0x80008\n"
"s_lshl_b32 s66, s86, 4\n"
"s_add_u32 s66, s66, s84\n"
"s_sub_u32 s56, s66, 32\n"
"s_mul_i32 s60, s56, 43\n"
"s_lshr_b32 s60, s60, 8\n"
"s_mul_i32 s67, s60, 6\n"
"s_sub_u32 s61, s56, s67\n"
"s_mul_i32 s60, s60, 7\n"
"s_add_u32 s57, s60, s61\n"
"s_mov_b32 s58, s57\n"
"s_mov_b32 s62, 4\n"
"s_mov_b32 s63, 5\n"
"s_sub_u32 s67, s61, 4\n"
"s_lshl_b32 s67, s67, 1\n"
"s_add_u32 s67, s67, s60\n"
"s_add_u32 s59, s67, 1\n"
"s_cmp_ge_u32 s61, 4\n"
"s_cselect_b32 s57, s67, s57\n"
"s_cselect_b32 s58, s59, s58\n"
"s_cselect_b32 s62, 6, s62\n"
"s_cselect_b32 s63, 6, s63\n"
"s_lshr_b32 s60, s66, 3\n"
"s_mul_i32 s60, s60, 7\n"
"s_and_b32 s67, s66, 3\n"
"s_add_u32 s67, s67, s60\n"
"s_lshr_b32 s56, s66, 1\n"
"s_and_b32 s56, s56, 2\n"
"s_add_u32 s59, s56, 1\n"
"s_cmp_lt_u32 s66, 32\n"
"s_cselect_b32 s57, s67, s57\n"
"s_cselect_b32 s58, s67, s58\n"
"s_cselect_b32 s62, s56, s62\n"
"s_cselect_b32 s63, s59, s63\n"
"s_mul_i32 s62, s62, 28672\n"
"s_lshl_b32 s57, s57, 5\n"
"s_add_u32 s57, s57, s62\n"
"s_mul_i32 s63, s63, 28672\n"
"s_lshl_b32 s58, s58, 5\n"
"s_add_u32 s58, s58, s63\n"
"s_cmp_lt_u32 s66, 56\n"
"s_cselect_b32 s57, s57, -1\n"
"s_lshl_b32 s66, s66, 14\n"
"s_waitcnt vmcnt(0)\n"
"s_barrier\n"
"s_cmp_lg_u32 s86, 0\n"
"s_cbranch_scc1 L_nfA2\n"
"v_mov_b32 v250, s88\n"
"s_bfe_u32 s84, %8, 0x80008\n"
"s_mul_i32 s84, s84, 256\n"
"s_add_u32 s84, s84, 4096\n"
"v_mov_b32 v252, 0\n"
"s_mov_b64 exec, 1\n"
"buffer_store_dword v250, v252, s[72:75], s84 offen sc1\n"
"s_mov_b64 exec, -1\n"
"L_nfA2:\n"
"s_cmp_eq_u32 s57, -1\n"
"s_cbranch_scc1 L_doneA\n"
"s_mov_b32 s83, 0x10000\n"
"buffer_load_dword v252, v251, s[72:75], 0 offen offset:0 sc1\n"
"s_waitcnt vmcnt(0)\n"
"L_pollAp:\n"
"v_cmp_ne_u32 vcc, s88, v252\n"
"s_nop 4\n"
"s_cbranch_vccz L_seenAp\n"
"s_sleep 1\n"
"s_mov_b32 s84, 0\n"
"buffer_load_dword v252, v251, s[72:75], s84 offen sc1\n"
"s_waitcnt vmcnt(0)\n"
"s_sub_u32 s83, s83, 1\n"
"s_cmp_lg_u32 s83, 0\n"
"s_cbranch_scc1 L_pollAp\n"
"L_seenAp:\n"
"buffer_load_dwordx4 v[64:67], v248, s[52:55], s66 offen offset:0 sc1\n"
"buffer_load_dwordx4 v[68:71], v248, s[52:55], s66 offen offset:1024 sc1\n"
"buffer_load_dwordx4 v[72:75], v248, s[52:55], s66 offen offset:2048 sc1\n"
"buffer_load_dwordx4 v[76:79], v248, s[52:55], s66 offen offset:3072 sc1\n"
"s_add_u32 s66, s66, 4096\n"
"buffer_load_dwordx4 v[80:83], v248, s[52:55], s66 offen offset:0 sc1\n"
"buffer_load_dwordx4 v[84:87], v248, s[52:55], s66 offen offset:1024 sc1\n"
"buffer_load_dwordx4 v[88:91], v248, s[52:55], s66 offen offset:2048 sc1\n"
"buffer_load_dwordx4 v[92:95], v248, s[52:55], s66 offen offset:3072 sc1\n"
"s_add_u32 s66, s66, 4096\n"
"buffer_load_dwordx4 v[96:99], v248, s[52:55], s66 offen offset:0 sc1\n"
"buffer_load_dwordx4 v[100:103], v248, s[52:55], s66 offen offset:1024 sc1\n"
"buffer_load_dwordx4 v[104:107], v248, s[52:55], s66 offen offset:2048 sc1\n"
"buffer_load_dwordx4 v[108:111], v248, s[52:55], s66 offen offset:3072 sc1\n"
"s_add_u32 s66, s66, 4096\n"
"buffer_load_dwordx4 v[112:115], v248, s[52:55], s66 offen offset:0 sc1\n"
"buffer_load_dwordx4 v[116:119], v248, s[52:55], s66 offen offset:1024 sc1\n"
"buffer_load_dwordx4 v[120:123], v248, s[52:55], s66 offen offset:2048 sc1\n"
"buffer_load_dwordx4 v[124:127], v248, s[52:55], s66 offen offset:3072 sc1\n"
"s_mov_b64 s[76:77], %7\n"
"s_and_b32 s77, s77, 0xffff\n"
"s_mov_b32 s78, 199808\n"
"s_mov_b32 s79, 0x20000\n"
"v_lshrrev_b32 v136, 4, v248\n"
"v_and_b32 v137, 15, v136\n"
"v_mul_u32_u24 v137, 1792, v137\n"
"v_lshrrev_b32 v136, 4, v136\n"
"v_lshl_add_u32 v137, v136, 3, v137\n"
"s_mov_b32 s84, 0x00003f80\n"
"s_mov_b32 s85, 0x3f800000\n"
"v_mov_b64 v[128:129], 0\n"
"v_mov_b64 v[130:131], 0\n"
"v_mov_b64 v[132:133], 0\n"
"v_mov_b64 v[134:135], 0\n"
"s_waitcnt vmcnt(15)\n"
"v_dot2_f32_bf16 v128, v64, s84, v128\n"
"v_dot2_f32_bf16 v129, v64, s85, v129\n"
"v_dot2_f32_bf16 v130, v65, s84, v130\n"
"v_dot2_f32_bf16 v131, v65, s85, v131\n"
"v_dot2_f32_bf16 v132, v66, s84, v132\n"
"v_dot2_f32_bf16 v133, v66, s85, v133\n"
"v_dot2_f32_bf16 v134, v67, s84, v134\n"
"v_dot2_f32_bf16 v135, v67, s85, v135\n"
"s_waitcnt vmcnt(14)\n"
"v_dot2_f32_bf16 v128, v68, s84, v128\n"
"v_dot2_f32_bf16 v129, v68, s85, v129\n"
"v_dot2_f32_bf16 v130, v69, s84, v130\n"
"v_dot2_f32_bf16 v131, v69, s85, v131\n"
"v_dot2_f32_bf16 v132, v70, s84, v132\n"
"v_dot2_f32_bf16 v133, v70, s85, v133\n"
"v_dot2_f32_bf16 v134, v71, s84, v134\n"
"v_dot2_f32_bf16 v135, v71, s85, v135\n"
"s_waitcnt vmcnt(13)\n"
"v_dot2_f32_bf16 v128, v72, s84, v128\n"
"v_dot2_f32_bf16 v129, v72, s85, v129\n"
"v_dot2_f32_bf16 v130, v73, s84, v130\n"
"v_dot2_f32_bf16 v131, v73, s85, v131\n"
"v_dot2_f32_bf16 v132, v74, s84, v132\n"
"v_dot2_f32_bf16 v133, v74, s85, v133\n"
"v_dot2_f32_bf16 v134, v75, s84, v134\n"
"v_dot2_f32_bf16 v135, v75, s85, v135\n"
"s_waitcnt vmcnt(12)\n"
"v_dot2_f32_bf16 v128, v76, s84, v128\n"
"v_dot2_f32_bf16 v129, v76, s85, v129\n"
"v_dot2_f32_bf16 v130, v77, s84, v130\n"
"v_dot2_f32_bf16 v131, v77, s85, v131\n"
"v_dot2_f32_bf16 v132, v78, s84, v132\n"
"v_dot2_f32_bf16 v133, v78, s85, v133\n"
"v_dot2_f32_bf16 v134, v79, s84, v134\n"
"v_dot2_f32_bf16 v135, v79, s85, v135\n"
"s_waitcnt vmcnt(11)\n"
"v_dot2_f32_bf16 v128, v80, s84, v128\n"
"v_dot2_f32_bf16 v129, v80, s85, v129\n"
"v_dot2_f32_bf16 v130, v81, s84, v130\n"
"v_dot2_f32_bf16 v131, v81, s85, v131\n"
"v_dot2_f32_bf16 v132, v82, s84, v132\n"
"v_dot2_f32_bf16 v133, v82, s85, v133\n"
"v_dot2_f32_bf16 v134, v83, s84, v134\n"
"v_dot2_f32_bf16 v135, v83, s85, v135\n"
"s_waitcnt vmcnt(10)\n"
"v_dot2_f32_bf16 v128, v84, s84, v128\n"
"v_dot2_f32_bf16 v129, v84, s85, v129\n"
"v_dot2_f32_bf16 v130, v85, s84, v130\n"
"v_dot2_f32_bf16 v131, v85, s85, v131\n"
"v_dot2_f32_bf16 v132, v86, s84, v132\n"
"v_dot2_f32_bf16 v133, v86, s85, v133\n"
"v_dot2_f32_bf16 v134, v87, s84, v134\n"
"v_dot2_f32_bf16 v135, v87, s85, v135\n"
"s_waitcnt vmcnt(9)\n"
"v_dot2_f32_bf16 v128, v88, s84, v128\n"
"v_dot2_f32_bf16 v129, v88, s85, v129\n"
"v_dot2_f32_bf16 v130, v89, s84, v130\n"
"v_dot2_f32_bf16 v131, v89, s85, v131\n"
"v_dot2_f32_bf16 v132, v90, s84, v132\n"
"v_dot2_f32_bf16 v133, v90, s85, v133\n"
"v_dot2_f32_bf16 v134, v91, s84, v134\n"
"v_dot2_f32_bf16 v135, v91, s85, v135\n"
"s_waitcnt vmcnt(8)\n"
"v_dot2_f32_bf16 v128, v92, s84, v128\n"
"v_dot2_f32_bf16 v129, v92, s85, v129\n"
"v_dot2_f32_bf16 v130, v93, s84, v130\n"
"v_dot2_f32_bf16 v131, v93, s85, v131\n"
"v_dot2_f32_bf16 v132, v94, s84, v132\n"
"v_dot2_f32_bf16 v133, v94, s85, v133\n"
"v_dot2_f32_bf16 v134, v95, s84, v134\n"
"v_dot2_f32_bf16 v135, v95, s85, v135\n"
"s_waitcnt vmcnt(7)\n"
"v_dot2_f32_bf16 v128, v96, s84, v128\n"
"v_dot2_f32_bf16 v129, v96, s85, v129\n"
"v_dot2_f32_bf16 v130, v97, s84, v130\n"
"v_dot2_f32_bf16 v131, v97, s85, v131\n"
"v_dot2_f32_bf16 v132, v98, s84, v132\n"
"v_dot2_f32_bf16 v133, v98, s85, v133\n"
"v_dot2_f32_bf16 v134, v99, s84, v134\n"
"v_dot2_f32_bf16 v135, v99, s85, v135\n"
"s_waitcnt vmcnt(6)\n"
"v_dot2_f32_bf16 v128, v100, s84, v128\n"
"v_dot2_f32_bf16 v129, v100, s85, v129\n"
"v_dot2_f32_bf16 v130, v101, s84, v130\n"
"v_dot2_f32_bf16 v131, v101, s85, v131\n"
"v_dot2_f32_bf16 v132, v102, s84, v132\n"
"v_dot2_f32_bf16 v133, v102, s85, v133\n"
"v_dot2_f32_bf16 v134, v103, s84, v134\n"
"v_dot2_f32_bf16 v135, v103, s85, v135\n"
"s_waitcnt vmcnt(5)\n"
"v_dot2_f32_bf16 v128, v104, s84, v128\n"
"v_dot2_f32_bf16 v129, v104, s85, v129\n"
"v_dot2_f32_bf16 v130, v105, s84, v130\n"
"v_dot2_f32_bf16 v131, v105, s85, v131\n"
"v_dot2_f32_bf16 v132, v106, s84, v132\n"
"v_dot2_f32_bf16 v133, v106, s85, v133\n"
"v_dot2_f32_bf16 v134, v107, s84, v134\n"
"v_dot2_f32_bf16 v135, v107, s85, v135\n"
"s_waitcnt vmcnt(4)\n"
"v_dot2_f32_bf16 v128, v108, s84, v128\n"
"v_dot2_f32_bf16 v129, v108, s85, v129\n"
"v_dot2_f32_bf16 v130, v109, s84, v130\n"
"v_dot2_f32_bf16 v131, v109, s85, v131\n"
"v_dot2_f32_bf16 v132, v110, s84, v132\n"
"v_dot2_f32_bf16 v133, v110, s85, v133\n"
"v_dot2_f32_bf16 v134, v111, s84, v134\n"
"v_dot2_f32_bf16 v135, v111, s85, v135\n"
"s_waitcnt vmcnt(3)\n"
"v_dot2_f32_bf16 v128, v112, s84, v128\n"
"v_dot2_f32_bf16 v129, v112, s85, v129\n"
"v_dot2_f32_bf16 v130, v113, s84, v130\n"
"v_dot2_f32_bf16 v131, v113, s85, v131\n"
"v_dot2_f32_bf16 v132, v114, s84, v132\n"
"v_dot2_f32_bf16 v133, v114, s85, v133\n"
"v_dot2_f32_bf16 v134, v115, s84, v134\n"
"v_dot2_f32_bf16 v135, v115, s85, v135\n"
"s_waitcnt vmcnt(2)\n"
"v_dot2_f32_bf16 v128, v116, s84, v128\n"
"v_dot2_f32_bf16 v129, v116, s85, v129\n"
"v_dot2_f32_bf16 v130, v117, s84, v130\n"
"v_dot2_f32_bf16 v131, v117, s85, v131\n"
"v_dot2_f32_bf16 v132, v118, s84, v132\n"
"v_dot2_f32_bf16 v133, v118, s85, v133\n"
"v_dot2_f32_bf16 v134, v119, s84, v134\n"
"v_dot2_f32_bf16 v135, v119, s85, v135\n"
"s_waitcnt vmcnt(1)\n"
"v_dot2_f32_bf16 v128, v120, s84, v128\n"
"v_dot2_f32_bf16 v129, v120, s85, v129\n"
"v_dot2_f32_bf16 v130, v121, s84, v130\n"
"v_dot2_f32_bf16 v131, v121, s85, v131\n"
"v_dot2_f32_bf16 v132, v122, s84, v132\n"
"v_dot2_f32_bf16 v133, v122, s85, v133\n"
"v_dot2_f32_bf16 v134, v123, s84, v134\n"
"v_dot2_f32_bf16 v135, v123, s85, v135\n"
"s_waitcnt vmcnt(0)\n"
"v_dot2_f32_bf16 v128, v124, s84, v128\n"
"v_dot2_f32_bf16 v129, v124, s85, v129\n"
"v_dot2_f32_bf16 v130, v125, s84, v130\n"
"v_dot2_f32_bf16 v131, v125, s85, v131\n"
"v_dot2_f32_bf16 v132, v126, s84, v132\n"
"v_dot2_f32_bf16 v133, v126, s85, v133\n"
"v_dot2_f32_bf16 v134, v127, s84, v134\n"
"v_dot2_f32_bf16 v135, v127, s85, v135\n"
"v_cvt_pk_bf16_f32 v140, v128, v129\n"
"v_cvt_pk_bf16_f32 v141, v130, v131\n"
"v_cvt_pk_bf16_f32 v142, v132, v133\n"
"v_cvt_pk_bf16_f32 v143, v134, v135\n"
"buffer_store_dwordx2 v[140:141], v137, s[76:79], s57 offen\n"
"buffer_store_dwordx2 v[142:143], v137, s[76:79], s58 offen\n"
"L_doneA:\n"
"s_waitcnt vmcnt(0)\n"
"s_endpgm\n"
"L_fbA:\n"
"s_mov_b32 s91, 1\n"
"s_branch L_start\n"
"L_safeA:\n"
"v_mov_b32 v252, 0xff800000\n"
"v_mov_b32 v253, 0xff800000\n"
"v_mov_b32 v254, 0xff800000\n"
"v_mov_b32 v255, 0xff800000\n"
"s_mov_b32 s83, 16\n"
"L_mxA:\n"
"s_barrier\n"
"ds_read_b128 v[208:211], v244 offset:34816\n"
"ds_read_b128 v[212:215], v244 offset:34880\n"
"ds_read_b128 v[216:219], v244 offset:34944\n"
"ds_read_b128 v[220:223], v244 offset:35008\n"
"ds_read_b128 v[224:227], v244 offset:35072\n"
"ds_read_b128 v[228:231], v244 offset:35136\n"
"ds_read_b128 v[232:235], v244 offset:35200\n"
"ds_read_b128 v[236:239], v244 offset:35264\n"
"s_waitcnt lgkmcnt(7)\n"
"v_mfma_f32_16x16x32_bf16 v[192:195], v[208:211], v[0:3], 0\n"
"s_waitcnt lgkmcnt(6)\n"
"v_mfma_f32_16x16x32_bf16 v[192:195], v[212:215], v[4:7], v[192:195]\n"
"s_waitcnt lgkmcnt(5)\n"
"v_mfma_f32_16x16x32_bf16 v[192:195], v[216:219], v[8:11], v[192:195]\n"
"s_waitcnt lgkmcnt(4)\n"
"v_mfma_f32_16x16x32_bf16 v[192:195], v[220:223], v[12:15], v[192:195]\n"
"s_waitcnt lgkmcnt(3)\n"
"v_mfma_f32_16x16x32_bf16 v[192:195], v[224:227], v[16:19], v[192:195]\n"
"s_waitcnt lgkmcnt(2)\n"
"v_mfma_f32_16x16x32_bf16 v[192:195], v[228:231], v[20:23], v[192:195]\n"
"s_waitcnt lgkmcnt(1)\n"
"v_mfma_f32_16x16x32_bf16 v[192:195], v[232:235], v[24:27], v[192:195]\n"
"s_waitcnt lgkmcnt(0)\n"
"v_mfma_f32_16x16x32_bf16 v[192:195], v[236:239], v[28:31], v[192:195]\n"
"v_mfma_f32_16x16x32_bf16 v[196:199], v[208:211], v[32:35], 0\n"
"v_mfma_f32_16x16x32_bf16 v[196:199], v[212:215], v[36:39], v[196:199]\n"
"v_mfma_f32_16x16x32_bf16 v[196:199], v[216:219], v[40:43], v[196:199]\n"
"v_mfma_f32_16x16x32_bf16 v[196:199], v[220:223], v[44:47], v[196:199]\n"
"v_mfma_f32_16x16x32_bf16 v[196:199], v[224:227], v[48:51], v[196:199]\n"
"v_mfma_f32_16x16x32_bf16 v[196:199], v[228:231], v[52:55], v[196:199]\n"
"v_mfma_f32_16x16x32_bf16 v[196:199], v[232:235], v[56:59], v[196:199]\n"
"v_mfma_f32_16x16x32_bf16 v[196:199], v[236:239], v[60:63], v[196:199]\n"
"v_mfma_f32_16x16x32_bf16 v[200:203], v[208:211], v[64:67], 0\n"
"v_mfma_f32_16x16x32_bf16 v[200:203], v[212:215], v[68:71], v[200:203]\n"
"v_mfma_f32_16x16x32_bf16 v[200:203], v[216:219], v[72:75], v[200:203]\n"
"v_mfma_f32_16x16x32_bf16 v[200:203], v[220:223], v[76:79], v[200:203]\n"
"v_mfma_f32_16x16x32_bf16 v[200:203], v[224:227], v[80:83], v[200:203]\n"
"v_mfma_f32_16x16x32_bf16 v[200:203], v[228:231], v[84:87], v[200:203]\n"
"v_mfma_f32_16x16x32_bf16 v[200:203], v[232:235], v[88:91], v[200:203]\n"
"v_mfma_f32_16x16x32_bf16 v[200:203], v[236:239], v[92:95], v[200:203]\n"
"v_mfma_f32_16x16x32_bf16 v[204:207], v[208:211], v[96:99], 0\n"
"v_mfma_f32_16x16x32_bf16 v[204:207], v[212:215], v[100:103], v[204:207]\n"
"v_mfma_f32_16x16x32_bf16 v[204:207], v[216:219], v[104:107], v[204:207]\n"
"v_mfma_f32_16x16x32_bf16 v[204:207], v[220:223], v[108:111], v[204:207]\n"
"v_mfma_f32_16x16x32_bf16 v[204:207], v[224:227], v[112:115], v[204:207]\n"
"v_mfma_f32_16x16x32_bf16 v[204:207], v[228:231], v[116:119], v[204:207]\n"
"v_mfma_f32_16x16x32_bf16 v[204:207], v[232:235], v[120:123], v[204:207]\n"
"v_mfma_f32_16x16x32_bf16 v[204:207], v[236:239], v[124:127], v[204:207]\n"
"v_max3_f32 v252, v192, v193, v252\n"
"v_max3_f32 v252, v194, v195, v252\n"
"v_max3_f32 v253, v196, v197, v253\n"
"v_max3_f32 v253, v198, v199, v253\n"
"v_max3_f32 v254, v200, v201, v254\n"
"v_max3_f32 v254, v202, v203, v254\n"
"s_nop 1\n"
"v_max3_f32 v255, v204, v205, v255\n"
"v_max3_f32 v255, v206, v207, v255\n"
"s_waitcnt lgkmcnt(0)\n"
"s_barrier\n"
"s_barrier\n"
"ds_read_b128 v[208:211], v244 offset:0\n"
"ds_read_b128 v[212:215], v244 offset:64\n"
"ds_read_b128 v[216:219], v244 offset:128\n"
"ds_read_b128 v[220:223], v244 offset:192\n"
"ds_read_b128 v[224:227], v244 offset:256\n"
"ds_read_b128 v[228:231], v244 offset:320\n"
"ds_read_b128 v[232:235], v244 offset:384\n"
"ds_read_b128 v[236:239], v244 offset:448\n"
"s_waitcnt lgkmcnt(7)\n"
"v_mfma_f32_16x16x32_bf16 v[192:195], v[208:211], v[0:3], 0\n"
"s_waitcnt lgkmcnt(6)\n"
"v_mfma_f32_16x16x32_bf16 v[192:195], v[212:215], v[4:7], v[192:195]\n"
"s_waitcnt lgkmcnt(5)\n"
"v_mfma_f32_16x16x32_bf16 v[192:195], v[216:219], v[8:11], v[192:195]\n"
"s_waitcnt lgkmcnt(4)\n"
"v_mfma_f32_16x16x32_bf16 v[192:195], v[220:223], v[12:15], v[192:195]\n"
"s_waitcnt lgkmcnt(3)\n"
"v_mfma_f32_16x16x32_bf16 v[192:195], v[224:227], v[16:19], v[192:195]\n"
"s_waitcnt lgkmcnt(2)\n"
"v_mfma_f32_16x16x32_bf16 v[192:195], v[228:231], v[20:23], v[192:195]\n"
"s_waitcnt lgkmcnt(1)\n"
"v_mfma_f32_16x16x32_bf16 v[192:195], v[232:235], v[24:27], v[192:195]\n"
"s_waitcnt lgkmcnt(0)\n"
"v_mfma_f32_16x16x32_bf16 v[192:195], v[236:239], v[28:31], v[192:195]\n"
"v_mfma_f32_16x16x32_bf16 v[196:199], v[208:211], v[32:35], 0\n"
"v_mfma_f32_16x16x32_bf16 v[196:199], v[212:215], v[36:39], v[196:199]\n"
"v_mfma_f32_16x16x32_bf16 v[196:199], v[216:219], v[40:43], v[196:199]\n"
"v_mfma_f32_16x16x32_bf16 v[196:199], v[220:223], v[44:47], v[196:199]\n"
"v_mfma_f32_16x16x32_bf16 v[196:199], v[224:227], v[48:51], v[196:199]\n"
"v_mfma_f32_16x16x32_bf16 v[196:199], v[228:231], v[52:55], v[196:199]\n"
"v_mfma_f32_16x16x32_bf16 v[196:199], v[232:235], v[56:59], v[196:199]\n"
"v_mfma_f32_16x16x32_bf16 v[196:199], v[236:239], v[60:63], v[196:199]\n"
"v_mfma_f32_16x16x32_bf16 v[200:203], v[208:211], v[64:67], 0\n"
"v_mfma_f32_16x16x32_bf16 v[200:203], v[212:215], v[68:71], v[200:203]\n"
"v_mfma_f32_16x16x32_bf16 v[200:203], v[216:219], v[72:75], v[200:203]\n"
"v_mfma_f32_16x16x32_bf16 v[200:203], v[220:223], v[76:79], v[200:203]\n"
"v_mfma_f32_16x16x32_bf16 v[200:203], v[224:227], v[80:83], v[200:203]\n"
"v_mfma_f32_16x16x32_bf16 v[200:203], v[228:231], v[84:87], v[200:203]\n"
"v_mfma_f32_16x16x32_bf16 v[200:203], v[232:235], v[88:91], v[200:203]\n"
"v_mfma_f32_16x16x32_bf16 v[200:203], v[236:239], v[92:95], v[200:203]\n"
"v_mfma_f32_16x16x32_bf16 v[204:207], v[208:211], v[96:99], 0\n"
"v_mfma_f32_16x16x32_bf16 v[204:207], v[212:215], v[100:103], v[204:207]\n"
"v_mfma_f32_16x16x32_bf16 v[204:207], v[216:219], v[104:107], v[204:207]\n"
"v_mfma_f32_16x16x32_bf16 v[204:207], v[220:223], v[108:111], v[204:207]\n"
"v_mfma_f32_16x16x32_bf16 v[204:207], v[224:227], v[112:115], v[204:207]\n"
"v_mfma_f32_16x16x32_bf16 v[204:207], v[228:231], v[116:119], v[204:207]\n"
"v_mfma_f32_16x16x32_bf16 v[204:207], v[232:235], v[120:123], v[204:207]\n"
"v_mfma_f32_16x16x32_bf16 v[204:207], v[236:239], v[124:127], v[204:207]\n"
"v_max3_f32 v252, v192, v193, v252\n"
"v_max3_f32 v252, v194, v195, v252\n"
"v_max3_f32 v253, v196, v197, v253\n"
"v_max3_f32 v253, v198, v199, v253\n"
"v_max3_f32 v254, v200, v201, v254\n"
"v_max3_f32 v254, v202, v203, v254\n"
"s_nop 1\n"
"v_max3_f32 v255, v204, v205, v255\n"
"v_max3_f32 v255, v206, v207, v255\n"
"s_waitcnt lgkmcnt(0)\n"
"s_barrier\n"
"s_sub_u32 s83, s83, 1\n"
"s_cmp_lg_u32 s83, 0\n"
"s_cbranch_scc1 L_mxA\n"
"v_mov_b32 v208, v252\n"
"s_nop 1\n"
"v_permlane16_swap_b32 v252, v208\n"
"v_max_f32 v252, v252, v208\n"
"v_mov_b32 v208, v252\n"
"s_nop 1\n"
"v_permlane32_swap_b32 v252, v208\n"
"v_max_f32 v252, v252, v208\n"
"v_mov_b32 v209, v253\n"
"s_nop 1\n"
"v_permlane16_swap_b32 v253, v209\n"
"v_max_f32 v253, v253, v209\n"
"v_mov_b32 v209, v253\n"
"s_nop 1\n"
"v_permlane32_swap_b32 v253, v209\n"
"v_max_f32 v253, v253, v209\n"
"v_mov_b32 v210, v254\n"
"s_nop 1\n"
"v_permlane16_swap_b32 v254, v210\n"
"v_max_f32 v254, v254, v210\n"
"v_mov_b32 v210, v254\n"
"s_nop 1\n"
"v_permlane32_swap_b32 v254, v210\n"
"v_max_f32 v254, v254, v210\n"
"v_mov_b32 v211, v255\n"
"s_nop 1\n"
"v_permlane16_swap_b32 v255, v211\n"
"v_max_f32 v255, v255, v211\n"
"v_mov_b32 v211, v255\n"
"s_nop 1\n"
"v_permlane32_swap_b32 v255, v211\n"
"v_max_f32 v255, v255, v211\n"
"v_mbcnt_lo_u32_b32 v216, -1, 0\n"
"v_mbcnt_hi_u32_b32 v216, -1, v216\n"
"v_and_b32 v216, 15, v216\n"
"v_lshlrev_b32 v216, 4, v216\n"
"v_add_u32 v217, 161792, v216\n"
"s_lshl_b32 s84, s87, 2\n"
"v_add_u32 v216, s84, v217\n"
"ds_write_b32 v216, v252 offset:0\n"
"ds_write_b32 v216, v253 offset:256\n"
"ds_write_b32 v216, v254 offset:512\n"
"ds_write_b32 v216, v255 offset:768\n"
"s_waitcnt vmcnt(0) lgkmcnt(0)\n"
"s_barrier\n"
"ds_read_b128 v[220:223], v217 offset:0\n"
"ds_read_b128 v[224:227], v217 offset:256\n"
"ds_read_b128 v[228:231], v217 offset:512\n"
"ds_read_b128 v[232:235], v217 offset:768\n"
"s_waitcnt lgkmcnt(3)\n"
"v_max3_f32 v252, v220, v221, v222\n"
"v_max_f32 v252, v252, v223\n"
"s_waitcnt lgkmcnt(2)\n"
"v_max3_f32 v253, v224, v225, v226\n"
"v_max_f32 v253, v253, v227\n"
"s_waitcnt lgkmcnt(1)\n"
"v_max3_f32 v254, v228, v229, v230\n"
"v_max_f32 v254, v254, v231\n"
"s_waitcnt lgkmcnt(0)\n"
"v_max3_f32 v255, v232, v233, v234\n"
"v_max_f32 v255, v255, v235\n"
"v_mov_b64 v[128:129], 0\n"
"v_mov_b64 v[130:131], 0\n"
"v_mov_b64 v[132:133], 0\n"
"v_mov_b64 v[134:135], 0\n"
"v_mov_b64 v[136:137], 0\n"
"v_mov_b64 v[138:139], 0\n"
"v_mov_b64 v[140:141], 0\n"
"v_mov_b64 v[142:143], 0\n"
"v_mov_b64 v[144:145], 0\n"
"v_mov_b64 v[146:147], 0\n"
"v_mov_b64 v[148:149], 0\n"
"v_mov_b64 v[150:151], 0\n"
"v_mov_b64 v[152:153], 0\n"
"v_mov_b64 v[154:155], 0\n"
"v_mov_b64 v[156:157], 0\n"
"v_mov_b64 v[158:159], 0\n"
"v_mov_b64 v[160:161], 0\n"
"v_mov_b64 v[162:163], 0\n"
"v_mov_b64 v[164:165], 0\n"
"v_mov_b64 v[166:167], 0\n"
"v_mov_b64 v[168:169], 0\n"
"v_mov_b64 v[170:171], 0\n"
"v_mov_b64 v[172:173], 0\n"
"v_mov_b64 v[174:175], 0\n"
"v_mov_b64 v[176:177], 0\n"
"v_mov_b64 v[178:179], 0\n"
"v_mov_b64 v[180:181], 0\n"
"v_mov_b64 v[182:183], 0\n"
"v_mov_b64 v[184:185], 0\n"
"v_mov_b64 v[186:187], 0\n"
"v_mov_b64 v[188:189], 0\n"
"v_mov_b64 v[190:191], 0\n"
"v_mov_b32 v240, 0\n"
"v_mov_b32 v241, 0\n"
"v_mov_b32 v242, 0\n"
"v_mov_b32 v243, 0\n"
"v_mov_b64 v[200:201], 0\n"
"v_mov_b64 v[202:203], 0\n"
"v_mov_b64 v[204:205], 0\n"
"v_mov_b64 v[206:207], 0\n"
"v_mov_b64 v[224:225], 0\n"
"v_mov_b64 v[226:227], 0\n"
"v_mov_b64 v[228:229], 0\n"
"v_mov_b64 v[230:231], 0\n"
"v_mov_b64 v[232:233], 0\n"
"v_mov_b64 v[234:235], 0\n"
"v_mov_b64 v[236:237], 0\n"
"v_mov_b64 v[238:239], 0\n"
"s_waitcnt lgkmcnt(0)\n"
"s_barrier\n"
"s_mov_b32 s83, 15\n"
"L_loopAs:\n"
"s_setprio 1\n"
"v_mfma_f32_16x16x32_bf16 v[172:175], v[200:203], v[224:227], v[172:175]\n"
"ds_read_b128 v[208:211], v244 offset:34816\n"
"v_mfma_f32_16x16x32_bf16 v[172:175], v[204:207], v[228:231], v[172:175]\n"
"ds_read_b128 v[212:215], v244 offset:34880\n"
"v_mfma_f32_16x16x32_bf16 v[188:191], v[200:203], v[232:235], v[188:191]\n"
"ds_read_b128 v[216:219], v244 offset:34944\n"
"v_mfma_f32_16x16x32_bf16 v[188:191], v[204:207], v[236:239], v[188:191]\n"
"ds_read_b128 v[220:223], v244 offset:35008\n"
"ds_read_b128 v[224:227], v244 offset:35072\n"
"ds_read_b128 v[228:231], v244 offset:35136\n"
"ds_read_b128 v[232:235], v244 offset:35200\n"
"ds_read_b128 v[236:239], v244 offset:35264\n"
"s_waitcnt lgkmcnt(7)\n"
"v_mfma_f32_16x16x32_bf16 v[192:195], v[208:211], v[0:3], 0\n"
"s_waitcnt lgkmcnt(6)\n"
"v_mfma_f32_16x16x32_bf16 v[192:195], v[212:215], v[4:7], v[192:195]\n"
"s_waitcnt lgkmcnt(5)\n"
"v_mfma_f32_16x16x32_bf16 v[192:195], v[216:219], v[8:11], v[192:195]\n"
"s_waitcnt lgkmcnt(4)\n"
"v_mfma_f32_16x16x32_bf16 v[192:195], v[220:223], v[12:15], v[192:195]\n"
"v_mfma_f32_16x16x32_bf16 v[196:199], v[208:211], v[32:35], 0\n"
"v_mfma_f32_16x16x32_bf16 v[196:199], v[212:215], v[36:39], v[196:199]\n"
"v_mfma_f32_16x16x32_bf16 v[196:199], v[216:219], v[40:43], v[196:199]\n"
"v_mfma_f32_16x16x32_bf16 v[196:199], v[220:223], v[44:47], v[196:199]\n"
"s_waitcnt lgkmcnt(3)\n"
"v_mfma_f32_16x16x32_bf16 v[192:195], v[224:227], v[16:19], v[192:195]\n"
"s_waitcnt lgkmcnt(2)\n"
"v_mfma_f32_16x16x32_bf16 v[192:195], v[228:231], v[20:23], v[192:195]\n"
"s_waitcnt lgkmcnt(1)\n"
"v_mfma_f32_16x16x32_bf16 v[192:195], v[232:235], v[24:27], v[192:195]\n"
"s_waitcnt lgkmcnt(0)\n"
"v_mfma_f32_16x16x32_bf16 v[192:195], v[236:239], v[28:31], v[192:195]\n"
"s_nop 1\n"
"v_mfma_f32_16x16x32_bf16 v[196:199], v[224:227], v[48:51], v[196:199]\n"
"s_nop 1\n"
"v_mfma_f32_16x16x32_bf16 v[196:199], v[228:231], v[52:55], v[196:199]\n"
"s_nop 1\n"
"v_mfma_f32_16x16x32_bf16 v[196:199], v[232:235], v[56:59], v[196:199]\n"
"v_sub_f32 v192, v192, v252\n"
"v_sub_f32 v193, v193, v252\n"
"v_mfma_f32_16x16x32_bf16 v[196:199], v[236:239], v[60:63], v[196:199]\n"
"v_sub_f32 v194, v194, v252\n"
"v_sub_f32 v195, v195, v252\n"
"v_mfma_f32_16x16x32_bf16 v[200:203], v[208:211], v[64:67], 0\n"
"v_exp_f32 v192, v192\n"
"v_mfma_f32_16x16x32_bf16 v[200:203], v[212:215], v[68:71], v[200:203]\n"
"v_exp_f32 v193, v193\n"
"v_mfma_f32_16x16x32_bf16 v[200:203], v[216:219], v[72:75], v[200:203]\n"
"v_exp_f32 v194, v194\n"
"v_mfma_f32_16x16x32_bf16 v[200:203], v[220:223], v[76:79], v[200:203]\n"
"v_exp_f32 v195, v195\n"
"v_mfma_f32_16x16x32_bf16 v[200:203], v[224:227], v[80:83], v[200:203]\n"
"v_add_f32 v240, v240, v192\n"
"v_cvt_pk_bf16_f32 v250, v192, v193\n"
"v_mfma_f32_16x16x32_bf16 v[200:203], v[228:231], v[84:87], v[200:203]\n"
"v_add_f32 v240, v240, v193\n"
"v_cvt_pk_bf16_f32 v251, v194, v195\n"
"v_mfma_f32_16x16x32_bf16 v[200:203], v[232:235], v[88:91], v[200:203]\n"
"v_add_f32 v240, v240, v194\n"
"ds_write_b64 v247, v[250:251] offset:0\n"
"v_mfma_f32_16x16x32_bf16 v[200:203], v[236:239], v[92:95], v[200:203]\n"
"v_add_f32 v240, v240, v195\n"
"v_sub_f32 v196, v196, v253\n"
"v_mfma_f32_16x16x32_bf16 v[204:207], v[208:211], v[96:99], 0\n"
"v_sub_f32 v197, v197, v253\n"
"v_sub_f32 v198, v198, v253\n"
"v_mfma_f32_16x16x32_bf16 v[204:207], v[212:215], v[100:103], v[204:207]\n"
"v_sub_f32 v199, v199, v253\n"
"v_exp_f32 v196, v196\n"
"v_mfma_f32_16x16x32_bf16 v[204:207], v[216:219], v[104:107], v[204:207]\n"
"v_exp_f32 v197, v197\n"
"v_mfma_f32_16x16x32_bf16 v[204:207], v[220:223], v[108:111], v[204:207]\n"
"v_exp_f32 v198, v198\n"
"v_mfma_f32_16x16x32_bf16 v[204:207], v[224:227], v[112:115], v[204:207]\n"
"v_exp_f32 v199, v199\n"
"v_mfma_f32_16x16x32_bf16 v[204:207], v[228:231], v[116:119], v[204:207]\n"
"v_add_f32 v241, v241, v196\n"
"v_cvt_pk_bf16_f32 v250, v196, v197\n"
"v_mfma_f32_16x16x32_bf16 v[204:207], v[232:235], v[120:123], v[204:207]\n"
"v_add_f32 v241, v241, v197\n"
"v_cvt_pk_bf16_f32 v251, v198, v199\n"
"v_mfma_f32_16x16x32_bf16 v[204:207], v[236:239], v[124:127], v[204:207]\n"
"v_add_f32 v241, v241, v198\n"
"ds_write_b64 v247, v[250:251] offset:2048\n"
"v_add_f32 v241, v241, v199\n"
"v_sub_f32 v200, v200, v254\n"
"v_sub_f32 v201, v201, v254\n"
"v_sub_f32 v202, v202, v254\n"
"v_sub_f32 v203, v203, v254\n"
"v_exp_f32 v200, v200\n"
"v_exp_f32 v201, v201\n"
"v_exp_f32 v202, v202\n"
"v_exp_f32 v203, v203\n"
"v_add_f32 v242, v242, v200\n"
"v_cvt_pk_bf16_f32 v250, v200, v201\n"
"v_add_f32 v242, v242, v201\n"
"v_cvt_pk_bf16_f32 v251, v202, v203\n"
"v_add_f32 v242, v242, v202\n"
"ds_write_b64 v247, v[250:251] offset:4096\n"
"v_add_f32 v242, v242, v203\n"
"v_sub_f32 v204, v204, v255\n"
"v_sub_f32 v205, v205, v255\n"
"v_sub_f32 v206, v206, v255\n"
"v_sub_f32 v207, v207, v255\n"
"v_exp_f32 v204, v204\n"
"v_exp_f32 v205, v205\n"
"v_exp_f32 v206, v206\n"
"v_exp_f32 v207, v207\n"
"v_add_f32 v243, v243, v204\n"
"v_cvt_pk_bf16_f32 v250, v204, v205\n"
"v_add_f32 v243, v243, v205\n"
"v_cvt_pk_bf16_f32 v251, v206, v207\n"
"v_add_f32 v243, v243, v206\n"
"ds_write_b64 v247, v[250:251] offset:6144\n"
"v_add_f32 v243, v243, v207\n"
"s_setprio 0\n"
"s_waitcnt vmcnt(0) lgkmcnt(0)\n"
"s_barrier\n"
"ds_read_b128 v[208:211], v248 offset:0\n"
"ds_read_b64_tr_b16 v[192:193], v245 offset:0\n"
"ds_read_b64_tr_b16 v[194:195], v245 offset:8704\n"
"ds_read_b128 v[212:215], v248 offset:1024\n"
"ds_read_b64_tr_b16 v[196:197], v245 offset:17408\n"
"ds_read_b64_tr_b16 v[198:199], v245 offset:26112\n"
"ds_read_b128 v[216:219], v248 offset:2048\n"
"ds_read_b128 v[220:223], v248 offset:3072\n"
"ds_read_b128 v[224:227], v248 offset:4096\n"
"ds_read_b128 v[228:231], v248 offset:5120\n"
"ds_read_b128 v[232:235], v248 offset:6144\n"
"ds_read_b128 v[236:239], v248 offset:7168\n"
"s_add_u32 m0, s56, 104448\n"
"ds_read_b64_tr_b16 v[200:201], v246 offset:0\n"
"buffer_load_dwordx4 v249, s[44:47], s64 offen nt lds\n"
"s_add_u32 m0, s57, 104448\n"
"ds_read_b64_tr_b16 v[202:203], v246 offset:8704\n"
"buffer_load_dwordx4 v249, s[44:47], s65 offen nt lds\n"
"s_add_u32 m0, s58, 104448\n"
"ds_read_b64_tr_b16 v[204:205], v246 offset:17408\n"
"buffer_load_dwordx4 v249, s[44:47], s66 offen nt lds\n"
"s_add_u32 m0, s59, 104448\n"
"ds_read_b64_tr_b16 v[206:207], v246 offset:26112\n"
"buffer_load_dwordx4 v249, s[44:47], s67 offen nt lds\n"
"s_add_u32 m0, s60, 104448\n"
"s_nop 0\n"
"buffer_load_dwordx4 v249, s[44:47], s68 offen nt lds\n"
"s_add_u32 m0, s61, 104448\n"
"s_nop 0\n"
"buffer_load_dwordx4 v249, s[44:47], s69 offen nt lds\n"
"v_add_u32 v249, 32768, v249\n"
"s_waitcnt lgkmcnt(13)\n"
"v_mfma_f32_16x16x32_bf16 v[128:131], v[192:195], v[208:211], v[128:131]\n"
"s_waitcnt lgkmcnt(10)\n"
"v_mfma_f32_16x16x32_bf16 v[128:131], v[196:199], v[212:215], v[128:131]\n"
"s_waitcnt lgkmcnt(9)\n"
"v_mfma_f32_16x16x32_bf16 v[144:147], v[192:195], v[216:219], v[144:147]\n"
"s_waitcnt lgkmcnt(8)\n"
"v_mfma_f32_16x16x32_bf16 v[144:147], v[196:199], v[220:223], v[144:147]\n"
"s_waitcnt lgkmcnt(7)\n"
"v_mfma_f32_16x16x32_bf16 v[160:163], v[192:195], v[224:227], v[160:163]\n"
"s_waitcnt lgkmcnt(6)\n"
"v_mfma_f32_16x16x32_bf16 v[160:163], v[196:199], v[228:231], v[160:163]\n"
"s_waitcnt lgkmcnt(5)\n"
"v_mfma_f32_16x16x32_bf16 v[176:179], v[192:195], v[232:235], v[176:179]\n"
"s_waitcnt lgkmcnt(4)\n"
"v_mfma_f32_16x16x32_bf16 v[176:179], v[196:199], v[236:239], v[176:179]\n"
"ds_read_b64_tr_b16 v[192:193], v245 offset:64\n"
"ds_read_b64_tr_b16 v[194:195], v245 offset:8768\n"
"ds_read_b64_tr_b16 v[196:197], v245 offset:17472\n"
"ds_read_b64_tr_b16 v[198:199], v245 offset:26176\n"
"s_waitcnt lgkmcnt(6)\n"
"v_mfma_f32_16x16x32_bf16 v[132:135], v[200:203], v[208:211], v[132:135]\n"
"s_waitcnt lgkmcnt(4)\n"
"v_mfma_f32_16x16x32_bf16 v[132:135], v[204:207], v[212:215], v[132:135]\n"
"v_mfma_f32_16x16x32_bf16 v[148:151], v[200:203], v[216:219], v[148:151]\n"
"v_mfma_f32_16x16x32_bf16 v[148:151], v[204:207], v[220:223], v[148:151]\n"
"v_mfma_f32_16x16x32_bf16 v[164:167], v[200:203], v[224:227], v[164:167]\n"
"v_mfma_f32_16x16x32_bf16 v[164:167], v[204:207], v[228:231], v[164:167]\n"
"v_mfma_f32_16x16x32_bf16 v[180:183], v[200:203], v[232:235], v[180:183]\n"
"v_mfma_f32_16x16x32_bf16 v[180:183], v[204:207], v[236:239], v[180:183]\n"
"ds_read_b64_tr_b16 v[200:201], v246 offset:64\n"
"ds_read_b64_tr_b16 v[202:203], v246 offset:8768\n"
"ds_read_b64_tr_b16 v[204:205], v246 offset:17472\n"
"ds_read_b64_tr_b16 v[206:207], v246 offset:26176\n"
"s_waitcnt lgkmcnt(6)\n"
"v_mfma_f32_16x16x32_bf16 v[136:139], v[192:195], v[208:211], v[136:139]\n"
"s_waitcnt lgkmcnt(4)\n"
"v_mfma_f32_16x16x32_bf16 v[136:139], v[196:199], v[212:215], v[136:139]\n"
"v_mfma_f32_16x16x32_bf16 v[152:155], v[192:195], v[216:219], v[152:155]\n"
"v_mfma_f32_16x16x32_bf16 v[152:155], v[196:199], v[220:223], v[152:155]\n"
"v_mfma_f32_16x16x32_bf16 v[168:171], v[192:195], v[224:227], v[168:171]\n"
"v_mfma_f32_16x16x32_bf16 v[168:171], v[196:199], v[228:231], v[168:171]\n"
"v_mfma_f32_16x16x32_bf16 v[184:187], v[192:195], v[232:235], v[184:187]\n"
"v_mfma_f32_16x16x32_bf16 v[184:187], v[196:199], v[236:239], v[184:187]\n"
"s_waitcnt lgkmcnt(2)\n"
"v_mfma_f32_16x16x32_bf16 v[140:143], v[200:203], v[208:211], v[140:143]\n"
"s_waitcnt lgkmcnt(0)\n"
"v_mfma_f32_16x16x32_bf16 v[140:143], v[204:207], v[212:215], v[140:143]\n"
"v_mfma_f32_16x16x32_bf16 v[156:159], v[200:203], v[216:219], v[156:159]\n"
"v_mfma_f32_16x16x32_bf16 v[156:159], v[204:207], v[220:223], v[156:159]\n"
"s_waitcnt lgkmcnt(0)\n"
"s_barrier\n"
"s_setprio 1\n"
"v_mfma_f32_16x16x32_bf16 v[172:175], v[200:203], v[224:227], v[172:175]\n"
"ds_read_b128 v[208:211], v244 offset:0\n"
"v_mfma_f32_16x16x32_bf16 v[172:175], v[204:207], v[228:231], v[172:175]\n"
"ds_read_b128 v[212:215], v244 offset:64\n"
"v_mfma_f32_16x16x32_bf16 v[188:191], v[200:203], v[232:235], v[188:191]\n"
"ds_read_b128 v[216:219], v244 offset:128\n"
"v_mfma_f32_16x16x32_bf16 v[188:191], v[204:207], v[236:239], v[188:191]\n"
"ds_read_b128 v[220:223], v244 offset:192\n"
"ds_read_b128 v[224:227], v244 offset:256\n"
"ds_read_b128 v[228:231], v244 offset:320\n"
"ds_read_b128 v[232:235], v244 offset:384\n"
"ds_read_b128 v[236:239], v244 offset:448\n"
"s_waitcnt lgkmcnt(7)\n"
"v_mfma_f32_16x16x32_bf16 v[192:195], v[208:211], v[0:3], 0\n"
"s_waitcnt lgkmcnt(6)\n"
"v_mfma_f32_16x16x32_bf16 v[192:195], v[212:215], v[4:7], v[192:195]\n"
"s_waitcnt lgkmcnt(5)\n"
"v_mfma_f32_16x16x32_bf16 v[192:195], v[216:219], v[8:11], v[192:195]\n"
"s_waitcnt lgkmcnt(4)\n"
"v_mfma_f32_16x16x32_bf16 v[192:195], v[220:223], v[12:15], v[192:195]\n"
"v_mfma_f32_16x16x32_bf16 v[196:199], v[208:211], v[32:35], 0\n"
"v_mfma_f32_16x16x32_bf16 v[196:199], v[212:215], v[36:39], v[196:199]\n"
"v_mfma_f32_16x16x32_bf16 v[196:199], v[216:219], v[40:43], v[196:199]\n"
"v_mfma_f32_16x16x32_bf16 v[196:199], v[220:223], v[44:47], v[196:199]\n"
"s_waitcnt lgkmcnt(3)\n"
"v_mfma_f32_16x16x32_bf16 v[192:195], v[224:227], v[16:19], v[192:195]\n"
"s_waitcnt lgkmcnt(2)\n"
"v_mfma_f32_16x16x32_bf16 v[192:195], v[228:231], v[20:23], v[192:195]\n"
"s_waitcnt lgkmcnt(1)\n"
"v_mfma_f32_16x16x32_bf16 v[192:195], v[232:235], v[24:27], v[192:195]\n"
"s_waitcnt lgkmcnt(0)\n"
"v_mfma_f32_16x16x32_bf16 v[192:195], v[236:239], v[28:31], v[192:195]\n"
"s_nop 1\n"
"v_mfma_f32_16x16x32_bf16 v[196:199], v[224:227], v[48:51], v[196:199]\n"
"s_nop 1\n"
"v_mfma_f32_16x16x32_bf16 v[196:199], v[228:231], v[52:55], v[196:199]\n"
"s_nop 1\n"
"v_mfma_f32_16x16x32_bf16 v[196:199], v[232:235], v[56:59], v[196:199]\n"
"v_sub_f32 v192, v192, v252\n"
"v_sub_f32 v193, v193, v252\n"
"v_mfma_f32_16x16x32_bf16 v[196:199], v[236:239], v[60:63], v[196:199]\n"
"v_sub_f32 v194, v194, v252\n"
"v_sub_f32 v195, v195, v252\n"
"v_mfma_f32_16x16x32_bf16 v[200:203], v[208:211], v[64:67], 0\n"
"v_exp_f32 v192, v192\n"
"v_mfma_f32_16x16x32_bf16 v[200:203], v[212:215], v[68:71], v[200:203]\n"
"v_exp_f32 v193, v193\n"
"v_mfma_f32_16x16x32_bf16 v[200:203], v[216:219], v[72:75], v[200:203]\n"
"v_exp_f32 v194, v194\n"
"v_mfma_f32_16x16x32_bf16 v[200:203], v[220:223], v[76:79], v[200:203]\n"
"v_exp_f32 v195, v195\n"
"v_mfma_f32_16x16x32_bf16 v[200:203], v[224:227], v[80:83], v[200:203]\n"
"v_add_f32 v240, v240, v192\n"
"v_cvt_pk_bf16_f32 v250, v192, v193\n"
"v_mfma_f32_16x16x32_bf16 v[200:203], v[228:231], v[84:87], v[200:203]\n"
"v_add_f32 v240, v240, v193\n"
"v_cvt_pk_bf16_f32 v251, v194, v195\n"
"v_mfma_f32_16x16x32_bf16 v[200:203], v[232:235], v[88:91], v[200:203]\n"
"v_add_f32 v240, v240, v194\n"
"ds_write_b64 v247, v[250:251] offset:0\n"
"v_mfma_f32_16x16x32_bf16 v[200:203], v[236:239], v[92:95], v[200:203]\n"
"v_add_f32 v240, v240, v195\n"
"v_sub_f32 v196, v196, v253\n"
"v_mfma_f32_16x16x32_bf16 v[204:207], v[208:211], v[96:99], 0\n"
"v_sub_f32 v197, v197, v253\n"
"v_sub_f32 v198, v198, v253\n"
"v_mfma_f32_16x16x32_bf16 v[204:207], v[212:215], v[100:103], v[204:207]\n"
"v_sub_f32 v199, v199, v253\n"
"v_exp_f32 v196, v196\n"
"v_mfma_f32_16x16x32_bf16 v[204:207], v[216:219], v[104:107], v[204:207]\n"
"v_exp_f32 v197, v197\n"
"v_mfma_f32_16x16x32_bf16 v[204:207], v[220:223], v[108:111], v[204:207]\n"
"v_exp_f32 v198, v198\n"
"v_mfma_f32_16x16x32_bf16 v[204:207], v[224:227], v[112:115], v[204:207]\n"
"v_exp_f32 v199, v199\n"
"v_mfma_f32_16x16x32_bf16 v[204:207], v[228:231], v[116:119], v[204:207]\n"
"v_add_f32 v241, v241, v196\n"
"v_cvt_pk_bf16_f32 v250, v196, v197\n"
"v_mfma_f32_16x16x32_bf16 v[204:207], v[232:235], v[120:123], v[204:207]\n"
"v_add_f32 v241, v241, v197\n"
"v_cvt_pk_bf16_f32 v251, v198, v199\n"
"v_mfma_f32_16x16x32_bf16 v[204:207], v[236:239], v[124:127], v[204:207]\n"
"v_add_f32 v241, v241, v198\n"
"ds_write_b64 v247, v[250:251] offset:2048\n"
"v_add_f32 v241, v241, v199\n"
"v_sub_f32 v200, v200, v254\n"
"v_sub_f32 v201, v201, v254\n"
"v_sub_f32 v202, v202, v254\n"
"v_sub_f32 v203, v203, v254\n"
"v_exp_f32 v200, v200\n"
"v_exp_f32 v201, v201\n"
"v_exp_f32 v202, v202\n"
"v_exp_f32 v203, v203\n"
"v_add_f32 v242, v242, v200\n"
"v_cvt_pk_bf16_f32 v250, v200, v201\n"
"v_add_f32 v242, v242, v201\n"
"v_cvt_pk_bf16_f32 v251, v202, v203\n"
"v_add_f32 v242, v242, v202\n"
"ds_write_b64 v247, v[250:251] offset:4096\n"
"v_add_f32 v242, v242, v203\n"
"v_sub_f32 v204, v204, v255\n"
"v_sub_f32 v205, v205, v255\n"
"v_sub_f32 v206, v206, v255\n"
"v_sub_f32 v207, v207, v255\n"
"v_exp_f32 v204, v204\n"
"v_exp_f32 v205, v205\n"
"v_exp_f32 v206, v206\n"
"v_exp_f32 v207, v207\n"
"v_add_f32 v243, v243, v204\n"
"v_cvt_pk_bf16_f32 v250, v204, v205\n"
"v_add_f32 v243, v243, v205\n"
"v_cvt_pk_bf16_f32 v251, v206, v207\n"
"v_add_f32 v243, v243, v206\n"
"ds_write_b64 v247, v[250:251] offset:6144\n"
"v_add_f32 v243, v243, v207\n"
"s_setprio 0\n"
"s_waitcnt vmcnt(0) lgkmcnt(0)\n"
"s_barrier\n"
"ds_read_b128 v[208:211], v248 offset:0\n"
"ds_read_b64_tr_b16 v[192:193], v245 offset:34816\n"
"ds_read_b64_tr_b16 v[194:195], v245 offset:43520\n"
"ds_read_b128 v[212:215], v248 offset:1024\n"
"ds_read_b64_tr_b16 v[196:197], v245 offset:52224\n"
"ds_read_b64_tr_b16 v[198:199], v245 offset:60928\n"
"ds_read_b128 v[216:219], v248 offset:2048\n"
"ds_read_b128 v[220:223], v248 offset:3072\n"
"ds_read_b128 v[224:227], v248 offset:4096\n"
"ds_read_b128 v[228:231], v248 offset:5120\n"
"ds_read_b128 v[232:235], v248 offset:6144\n"
"ds_read_b128 v[236:239], v248 offset:7168\n"
"s_add_u32 m0, s56, 69632\n"
"ds_read_b64_tr_b16 v[200:201], v246 offset:34816\n"
"buffer_load_dwordx4 v249, s[44:47], s64 offen nt lds\n"
"s_add_u32 m0, s57, 69632\n"
"ds_read_b64_tr_b16 v[202:203], v246 offset:43520\n"
"buffer_load_dwordx4 v249, s[44:47], s65 offen nt lds\n"
"s_add_u32 m0, s58, 69632\n"
"ds_read_b64_tr_b16 v[204:205], v246 offset:52224\n"
"buffer_load_dwordx4 v249, s[44:47], s66 offen nt lds\n"
"s_add_u32 m0, s59, 69632\n"
"ds_read_b64_tr_b16 v[206:207], v246 offset:60928\n"
"buffer_load_dwordx4 v249, s[44:47], s67 offen nt lds\n"
"s_add_u32 m0, s60, 69632\n"
"s_nop 0\n"
"buffer_load_dwordx4 v249, s[44:47], s68 offen nt lds\n"
"s_add_u32 m0, s61, 69632\n"
"s_nop 0\n"
"buffer_load_dwordx4 v249, s[44:47], s69 offen nt lds\n"
"v_add_u32 v249, 32768, v249\n"
"s_waitcnt lgkmcnt(13)\n"
"v_mfma_f32_16x16x32_bf16 v[128:131], v[192:195], v[208:211], v[128:131]\n"
"s_waitcnt lgkmcnt(10)\n"
"v_mfma_f32_16x16x32_bf16 v[128:131], v[196:199], v[212:215], v[128:131]\n"
"s_waitcnt lgkmcnt(9)\n"
"v_mfma_f32_16x16x32_bf16 v[144:147], v[192:195], v[216:219], v[144:147]\n"
"s_waitcnt lgkmcnt(8)\n"
"v_mfma_f32_16x16x32_bf16 v[144:147], v[196:199], v[220:223], v[144:147]\n"
"s_waitcnt lgkmcnt(7)\n"
"v_mfma_f32_16x16x32_bf16 v[160:163], v[192:195], v[224:227], v[160:163]\n"
"s_waitcnt lgkmcnt(6)\n"
"v_mfma_f32_16x16x32_bf16 v[160:163], v[196:199], v[228:231], v[160:163]\n"
"s_waitcnt lgkmcnt(5)\n"
"v_mfma_f32_16x16x32_bf16 v[176:179], v[192:195], v[232:235], v[176:179]\n"
"s_waitcnt lgkmcnt(4)\n"
"v_mfma_f32_16x16x32_bf16 v[176:179], v[196:199], v[236:239], v[176:179]\n"
"ds_read_b64_tr_b16 v[192:193], v245 offset:34880\n"
"ds_read_b64_tr_b16 v[194:195], v245 offset:43584\n"
"ds_read_b64_tr_b16 v[196:197], v245 offset:52288\n"
"ds_read_b64_tr_b16 v[198:199], v245 offset:60992\n"
"s_waitcnt lgkmcnt(6)\n"
"v_mfma_f32_16x16x32_bf16 v[132:135], v[200:203], v[208:211], v[132:135]\n"
"s_waitcnt lgkmcnt(4)\n"
"v_mfma_f32_16x16x32_bf16 v[132:135], v[204:207], v[212:215], v[132:135]\n"
"v_mfma_f32_16x16x32_bf16 v[148:151], v[200:203], v[216:219], v[148:151]\n"
"v_mfma_f32_16x16x32_bf16 v[148:151], v[204:207], v[220:223], v[148:151]\n"
"v_mfma_f32_16x16x32_bf16 v[164:167], v[200:203], v[224:227], v[164:167]\n"
"v_mfma_f32_16x16x32_bf16 v[164:167], v[204:207], v[228:231], v[164:167]\n"
"v_mfma_f32_16x16x32_bf16 v[180:183], v[200:203], v[232:235], v[180:183]\n"
"v_mfma_f32_16x16x32_bf16 v[180:183], v[204:207], v[236:239], v[180:183]\n"
"ds_read_b64_tr_b16 v[200:201], v246 offset:34880\n"
"ds_read_b64_tr_b16 v[202:203], v246 offset:43584\n"
"ds_read_b64_tr_b16 v[204:205], v246 offset:52288\n"
"ds_read_b64_tr_b16 v[206:207], v246 offset:60992\n"
"s_waitcnt lgkmcnt(6)\n"
"v_mfma_f32_16x16x32_bf16 v[136:139], v[192:195], v[208:211], v[136:139]\n"
"s_waitcnt lgkmcnt(4)\n"
"v_mfma_f32_16x16x32_bf16 v[136:139], v[196:199], v[212:215], v[136:139]\n"
"v_mfma_f32_16x16x32_bf16 v[152:155], v[192:195], v[216:219], v[152:155]\n"
"v_mfma_f32_16x16x32_bf16 v[152:155], v[196:199], v[220:223], v[152:155]\n"
"v_mfma_f32_16x16x32_bf16 v[168:171], v[192:195], v[224:227], v[168:171]\n"
"v_mfma_f32_16x16x32_bf16 v[168:171], v[196:199], v[228:231], v[168:171]\n"
"v_mfma_f32_16x16x32_bf16 v[184:187], v[192:195], v[232:235], v[184:187]\n"
"v_mfma_f32_16x16x32_bf16 v[184:187], v[196:199], v[236:239], v[184:187]\n"
"s_waitcnt lgkmcnt(2)\n"
"v_mfma_f32_16x16x32_bf16 v[140:143], v[200:203], v[208:211], v[140:143]\n"
"s_waitcnt lgkmcnt(0)\n"
"v_mfma_f32_16x16x32_bf16 v[140:143], v[204:207], v[212:215], v[140:143]\n"
"v_mfma_f32_16x16x32_bf16 v[156:159], v[200:203], v[216:219], v[156:159]\n"
"v_mfma_f32_16x16x32_bf16 v[156:159], v[204:207], v[220:223], v[156:159]\n"
"s_waitcnt lgkmcnt(0)\n"
"s_barrier\n"
"s_sub_u32 s83, s83, 1\n"
"s_cmp_lg_u32 s83, 0\n"
"s_cbranch_scc1 L_loopAs\n"
"s_setprio 1\n"
"v_mfma_f32_16x16x32_bf16 v[172:175], v[200:203], v[224:227], v[172:175]\n"
"ds_read_b128 v[208:211], v244 offset:34816\n"
"v_mfma_f32_16x16x32_bf16 v[172:175], v[204:207], v[228:231], v[172:175]\n"
"ds_read_b128 v[212:215], v244 offset:34880\n"
"v_mfma_f32_16x16x32_bf16 v[188:191], v[200:203], v[232:235], v[188:191]\n"
"ds_read_b128 v[216:219], v244 offset:34944\n"
"v_mfma_f32_16x16x32_bf16 v[188:191], v[204:207], v[236:239], v[188:191]\n"
"ds_read_b128 v[220:223], v244 offset:35008\n"
"ds_read_b128 v[224:227], v244 offset:35072\n"
"ds_read_b128 v[228:231], v244 offset:35136\n"
"ds_read_b128 v[232:235], v244 offset:35200\n"
"ds_read_b128 v[236:239], v244 offset:35264\n"
"s_waitcnt lgkmcnt(7)\n"
"v_mfma_f32_16x16x32_bf16 v[192:195], v[208:211], v[0:3], 0\n"
"s_waitcnt lgkmcnt(6)\n"
"v_mfma_f32_16x16x32_bf16 v[192:195], v[212:215], v[4:7], v[192:195]\n"
"s_waitcnt lgkmcnt(5)\n"
"v_mfma_f32_16x16x32_bf16 v[192:195], v[216:219], v[8:11], v[192:195]\n"
"s_waitcnt lgkmcnt(4)\n"
"v_mfma_f32_16x16x32_bf16 v[192:195], v[220:223], v[12:15], v[192:195]\n"
"v_mfma_f32_16x16x32_bf16 v[196:199], v[208:211], v[32:35], 0\n"
"v_mfma_f32_16x16x32_bf16 v[196:199], v[212:215], v[36:39], v[196:199]\n"
"v_mfma_f32_16x16x32_bf16 v[196:199], v[216:219], v[40:43], v[196:199]\n"
"v_mfma_f32_16x16x32_bf16 v[196:199], v[220:223], v[44:47], v[196:199]\n"
"s_waitcnt lgkmcnt(3)\n"
"v_mfma_f32_16x16x32_bf16 v[192:195], v[224:227], v[16:19], v[192:195]\n"
"s_waitcnt lgkmcnt(2)\n"
"v_mfma_f32_16x16x32_bf16 v[192:195], v[228:231], v[20:23], v[192:195]\n"
"s_waitcnt lgkmcnt(1)\n"
"v_mfma_f32_16x16x32_bf16 v[192:195], v[232:235], v[24:27], v[192:195]\n"
"s_waitcnt lgkmcnt(0)\n"
"v_mfma_f32_16x16x32_bf16 v[192:195], v[236:239], v[28:31], v[192:195]\n"
"s_nop 1\n"
"v_mfma_f32_16x16x32_bf16 v[196:199], v[224:227], v[48:51], v[196:199]\n"
"s_nop 1\n"
"v_mfma_f32_16x16x32_bf16 v[196:199], v[228:231], v[52:55], v[196:199]\n"
"s_nop 1\n"
"v_mfma_f32_16x16x32_bf16 v[196:199], v[232:235], v[56:59], v[196:199]\n"
"v_sub_f32 v192, v192, v252\n"
"v_sub_f32 v193, v193, v252\n"
"v_mfma_f32_16x16x32_bf16 v[196:199], v[236:239], v[60:63], v[196:199]\n"
"v_sub_f32 v194, v194, v252\n"
"v_sub_f32 v195, v195, v252\n"
"v_mfma_f32_16x16x32_bf16 v[200:203], v[208:211], v[64:67], 0\n"
"v_exp_f32 v192, v192\n"
"v_mfma_f32_16x16x32_bf16 v[200:203], v[212:215], v[68:71], v[200:203]\n"
"v_exp_f32 v193, v193\n"
"v_mfma_f32_16x16x32_bf16 v[200:203], v[216:219], v[72:75], v[200:203]\n"
"v_exp_f32 v194, v194\n"
"v_mfma_f32_16x16x32_bf16 v[200:203], v[220:223], v[76:79], v[200:203]\n"
"v_exp_f32 v195, v195\n"
"v_mfma_f32_16x16x32_bf16 v[200:203], v[224:227], v[80:83], v[200:203]\n"
"v_add_f32 v240, v240, v192\n"
"v_cvt_pk_bf16_f32 v250, v192, v193\n"
"v_mfma_f32_16x16x32_bf16 v[200:203], v[228:231], v[84:87], v[200:203]\n"
"v_add_f32 v240, v240, v193\n"
"v_cvt_pk_bf16_f32 v251, v194, v195\n"
"v_mfma_f32_16x16x32_bf16 v[200:203], v[232:235], v[88:91], v[200:203]\n"
"v_add_f32 v240, v240, v194\n"
"ds_write_b64 v247, v[250:251] offset:0\n"
"v_mfma_f32_16x16x32_bf16 v[200:203], v[236:239], v[92:95], v[200:203]\n"
"v_add_f32 v240, v240, v195\n"
"v_sub_f32 v196, v196, v253\n"
"v_mfma_f32_16x16x32_bf16 v[204:207], v[208:211], v[96:99], 0\n"
"v_sub_f32 v197, v197, v253\n"
"v_sub_f32 v198, v198, v253\n"
"v_mfma_f32_16x16x32_bf16 v[204:207], v[212:215], v[100:103], v[204:207]\n"
"v_sub_f32 v199, v199, v253\n"
"v_exp_f32 v196, v196\n"
"v_mfma_f32_16x16x32_bf16 v[204:207], v[216:219], v[104:107], v[204:207]\n"
"v_exp_f32 v197, v197\n"
"v_mfma_f32_16x16x32_bf16 v[204:207], v[220:223], v[108:111], v[204:207]\n"
"v_exp_f32 v198, v198\n"
"v_mfma_f32_16x16x32_bf16 v[204:207], v[224:227], v[112:115], v[204:207]\n"
"v_exp_f32 v199, v199\n"
"v_mfma_f32_16x16x32_bf16 v[204:207], v[228:231], v[116:119], v[204:207]\n"
"v_add_f32 v241, v241, v196\n"
"v_cvt_pk_bf16_f32 v250, v196, v197\n"
"v_mfma_f32_16x16x32_bf16 v[204:207], v[232:235], v[120:123], v[204:207]\n"
"v_add_f32 v241, v241, v197\n"
"v_cvt_pk_bf16_f32 v251, v198, v199\n"
"v_mfma_f32_16x16x32_bf16 v[204:207], v[236:239], v[124:127], v[204:207]\n"
"v_add_f32 v241, v241, v198\n"
"ds_write_b64 v247, v[250:251] offset:2048\n"
"v_add_f32 v241, v241, v199\n"
"v_sub_f32 v200, v200, v254\n"
"v_sub_f32 v201, v201, v254\n"
"v_sub_f32 v202, v202, v254\n"
"v_sub_f32 v203, v203, v254\n"
"v_exp_f32 v200, v200\n"
"v_exp_f32 v201, v201\n"
"v_exp_f32 v202, v202\n"
"v_exp_f32 v203, v203\n"
"v_add_f32 v242, v242, v200\n"
"v_cvt_pk_bf16_f32 v250, v200, v201\n"
"v_add_f32 v242, v242, v201\n"
"v_cvt_pk_bf16_f32 v251, v202, v203\n"
"v_add_f32 v242, v242, v202\n"
"ds_write_b64 v247, v[250:251] offset:4096\n"
"v_add_f32 v242, v242, v203\n"
"v_sub_f32 v204, v204, v255\n"
"v_sub_f32 v205, v205, v255\n"
"v_sub_f32 v206, v206, v255\n"
"v_sub_f32 v207, v207, v255\n"
"v_exp_f32 v204, v204\n"
"v_exp_f32 v205, v205\n"
"v_exp_f32 v206, v206\n"
"v_exp_f32 v207, v207\n"
"v_add_f32 v243, v243, v204\n"
"v_cvt_pk_bf16_f32 v250, v204, v205\n"
"v_add_f32 v243, v243, v205\n"
"v_cvt_pk_bf16_f32 v251, v206, v207\n"
"v_add_f32 v243, v243, v206\n"
"ds_write_b64 v247, v[250:251] offset:6144\n"
"v_add_f32 v243, v243, v207\n"
"s_setprio 0\n"
"s_waitcnt vmcnt(0) lgkmcnt(0)\n"
"s_barrier\n"
"ds_read_b128 v[208:211], v248 offset:0\n"
"ds_read_b64_tr_b16 v[192:193], v245 offset:0\n"
"ds_read_b64_tr_b16 v[194:195], v245 offset:8704\n"
"ds_read_b128 v[212:215], v248 offset:1024\n"
"ds_read_b64_tr_b16 v[196:197], v245 offset:17408\n"
"ds_read_b64_tr_b16 v[198:199], v245 offset:26112\n"
"ds_read_b128 v[216:219], v248 offset:2048\n"
"ds_read_b128 v[220:223], v248 offset:3072\n"
"ds_read_b128 v[224:227], v248 offset:4096\n"
"ds_read_b128 v[228:231], v248 offset:5120\n"
"ds_read_b128 v[232:235], v248 offset:6144\n"
"ds_read_b128 v[236:239], v248 offset:7168\n"
"s_add_u32 m0, s56, 104448\n"
"ds_read_b64_tr_b16 v[200:201], v246 offset:0\n"
"buffer_load_dwordx4 v249, s[44:47], s64 offen nt lds\n"
"s_add_u32 m0, s57, 104448\n"
"ds_read_b64_tr_b16 v[202:203], v246 offset:8704\n"
"buffer_load_dwordx4 v249, s[44:47], s65 offen nt lds\n"
"s_add_u32 m0, s58, 104448\n"
"ds_read_b64_tr_b16 v[204:205], v246 offset:17408\n"
"buffer_load_dwordx4 v249, s[44:47], s66 offen nt lds\n"
"s_add_u32 m0, s59, 104448\n"
"ds_read_b64_tr_b16 v[206:207], v246 offset:26112\n"
"buffer_load_dwordx4 v249, s[44:47], s67 offen nt lds\n"
"s_add_u32 m0, s60, 104448\n"
"s_nop 0\n"
"buffer_load_dwordx4 v249, s[44:47], s68 offen nt lds\n"
"s_add_u32 m0, s61, 104448\n"
"s_nop 0\n"
"buffer_load_dwordx4 v249, s[44:47], s69 offen nt lds\n"
"v_add_u32 v249, 32768, v249\n"
"s_waitcnt lgkmcnt(13)\n"
"v_mfma_f32_16x16x32_bf16 v[128:131], v[192:195], v[208:211], v[128:131]\n"
"s_waitcnt lgkmcnt(10)\n"
"v_mfma_f32_16x16x32_bf16 v[128:131], v[196:199], v[212:215], v[128:131]\n"
"s_waitcnt lgkmcnt(9)\n"
"v_mfma_f32_16x16x32_bf16 v[144:147], v[192:195], v[216:219], v[144:147]\n"
"s_waitcnt lgkmcnt(8)\n"
"v_mfma_f32_16x16x32_bf16 v[144:147], v[196:199], v[220:223], v[144:147]\n"
"s_waitcnt lgkmcnt(7)\n"
"v_mfma_f32_16x16x32_bf16 v[160:163], v[192:195], v[224:227], v[160:163]\n"
"s_waitcnt lgkmcnt(6)\n"
"v_mfma_f32_16x16x32_bf16 v[160:163], v[196:199], v[228:231], v[160:163]\n"
"s_waitcnt lgkmcnt(5)\n"
"v_mfma_f32_16x16x32_bf16 v[176:179], v[192:195], v[232:235], v[176:179]\n"
"s_waitcnt lgkmcnt(4)\n"
"v_mfma_f32_16x16x32_bf16 v[176:179], v[196:199], v[236:239], v[176:179]\n"
"ds_read_b64_tr_b16 v[192:193], v245 offset:64\n"
"ds_read_b64_tr_b16 v[194:195], v245 offset:8768\n"
"ds_read_b64_tr_b16 v[196:197], v245 offset:17472\n"
"ds_read_b64_tr_b16 v[198:199], v245 offset:26176\n"
"s_waitcnt lgkmcnt(6)\n"
"v_mfma_f32_16x16x32_bf16 v[132:135], v[200:203], v[208:211], v[132:135]\n"
"s_waitcnt lgkmcnt(4)\n"
"v_mfma_f32_16x16x32_bf16 v[132:135], v[204:207], v[212:215], v[132:135]\n"
"v_mfma_f32_16x16x32_bf16 v[148:151], v[200:203], v[216:219], v[148:151]\n"
"v_mfma_f32_16x16x32_bf16 v[148:151], v[204:207], v[220:223], v[148:151]\n"
"v_mfma_f32_16x16x32_bf16 v[164:167], v[200:203], v[224:227], v[164:167]\n"
"v_mfma_f32_16x16x32_bf16 v[164:167], v[204:207], v[228:231], v[164:167]\n"
"v_mfma_f32_16x16x32_bf16 v[180:183], v[200:203], v[232:235], v[180:183]\n"
"v_mfma_f32_16x16x32_bf16 v[180:183], v[204:207], v[236:239], v[180:183]\n"
"ds_read_b64_tr_b16 v[200:201], v246 offset:64\n"
"ds_read_b64_tr_b16 v[202:203], v246 offset:8768\n"
"ds_read_b64_tr_b16 v[204:205], v246 offset:17472\n"
"ds_read_b64_tr_b16 v[206:207], v246 offset:26176\n"
"s_waitcnt lgkmcnt(6)\n"
"v_mfma_f32_16x16x32_bf16 v[136:139], v[192:195], v[208:211], v[136:139]\n"
"s_waitcnt lgkmcnt(4)\n"
"v_mfma_f32_16x16x32_bf16 v[136:139], v[196:199], v[212:215], v[136:139]\n"
"v_mfma_f32_16x16x32_bf16 v[152:155], v[192:195], v[216:219], v[152:155]\n"
"v_mfma_f32_16x16x32_bf16 v[152:155], v[196:199], v[220:223], v[152:155]\n"
"v_mfma_f32_16x16x32_bf16 v[168:171], v[192:195], v[224:227], v[168:171]\n"
"v_mfma_f32_16x16x32_bf16 v[168:171], v[196:199], v[228:231], v[168:171]\n"
"v_mfma_f32_16x16x32_bf16 v[184:187], v[192:195], v[232:235], v[184:187]\n"
"v_mfma_f32_16x16x32_bf16 v[184:187], v[196:199], v[236:239], v[184:187]\n"
"s_waitcnt lgkmcnt(2)\n"
"v_mfma_f32_16x16x32_bf16 v[140:143], v[200:203], v[208:211], v[140:143]\n"
"s_waitcnt lgkmcnt(0)\n"
"v_mfma_f32_16x16x32_bf16 v[140:143], v[204:207], v[212:215], v[140:143]\n"
"v_mfma_f32_16x16x32_bf16 v[156:159], v[200:203], v[216:219], v[156:159]\n"
"v_mfma_f32_16x16x32_bf16 v[156:159], v[204:207], v[220:223], v[156:159]\n"
"s_waitcnt lgkmcnt(0)\n"
"s_barrier\n"
"s_setprio 1\n"
"v_mfma_f32_16x16x32_bf16 v[172:175], v[200:203], v[224:227], v[172:175]\n"
"ds_read_b128 v[208:211], v244 offset:0\n"
"v_mfma_f32_16x16x32_bf16 v[172:175], v[204:207], v[228:231], v[172:175]\n"
"ds_read_b128 v[212:215], v244 offset:64\n"
"v_mfma_f32_16x16x32_bf16 v[188:191], v[200:203], v[232:235], v[188:191]\n"
"ds_read_b128 v[216:219], v244 offset:128\n"
"v_mfma_f32_16x16x32_bf16 v[188:191], v[204:207], v[236:239], v[188:191]\n"
"ds_read_b128 v[220:223], v244 offset:192\n"
"ds_read_b128 v[224:227], v244 offset:256\n"
"ds_read_b128 v[228:231], v244 offset:320\n"
"ds_read_b128 v[232:235], v244 offset:384\n"
"ds_read_b128 v[236:239], v244 offset:448\n"
"s_waitcnt lgkmcnt(7)\n"
"v_mfma_f32_16x16x32_bf16 v[192:195], v[208:211], v[0:3], 0\n"
"s_waitcnt lgkmcnt(6)\n"
"v_mfma_f32_16x16x32_bf16 v[192:195], v[212:215], v[4:7], v[192:195]\n"
"s_waitcnt lgkmcnt(5)\n"
"v_mfma_f32_16x16x32_bf16 v[192:195], v[216:219], v[8:11], v[192:195]\n"
"s_waitcnt lgkmcnt(4)\n"
"v_mfma_f32_16x16x32_bf16 v[192:195], v[220:223], v[12:15], v[192:195]\n"
"v_mfma_f32_16x16x32_bf16 v[196:199], v[208:211], v[32:35], 0\n"
"v_mfma_f32_16x16x32_bf16 v[196:199], v[212:215], v[36:39], v[196:199]\n"
"v_mfma_f32_16x16x32_bf16 v[196:199], v[216:219], v[40:43], v[196:199]\n"
"v_mfma_f32_16x16x32_bf16 v[196:199], v[220:223], v[44:47], v[196:199]\n"
"s_waitcnt lgkmcnt(3)\n"
"v_mfma_f32_16x16x32_bf16 v[192:195], v[224:227], v[16:19], v[192:195]\n"
"s_waitcnt lgkmcnt(2)\n"
"v_mfma_f32_16x16x32_bf16 v[192:195], v[228:231], v[20:23], v[192:195]\n"
"s_waitcnt lgkmcnt(1)\n"
"v_mfma_f32_16x16x32_bf16 v[192:195], v[232:235], v[24:27], v[192:195]\n"
"s_waitcnt lgkmcnt(0)\n"
"v_mfma_f32_16x16x32_bf16 v[192:195], v[236:239], v[28:31], v[192:195]\n"
"s_nop 1\n"
"v_mfma_f32_16x16x32_bf16 v[196:199], v[224:227], v[48:51], v[196:199]\n"
"s_nop 1\n"
"v_mfma_f32_16x16x32_bf16 v[196:199], v[228:231], v[52:55], v[196:199]\n"
"s_nop 1\n"
"v_mfma_f32_16x16x32_bf16 v[196:199], v[232:235], v[56:59], v[196:199]\n"
"v_sub_f32 v192, v192, v252\n"
"v_sub_f32 v193, v193, v252\n"
"v_mfma_f32_16x16x32_bf16 v[196:199], v[236:239], v[60:63], v[196:199]\n"
"v_sub_f32 v194, v194, v252\n"
"v_sub_f32 v195, v195, v252\n"
"v_mfma_f32_16x16x32_bf16 v[200:203], v[208:211], v[64:67], 0\n"
"v_exp_f32 v192, v192\n"
"v_mfma_f32_16x16x32_bf16 v[200:203], v[212:215], v[68:71], v[200:203]\n"
"v_exp_f32 v193, v193\n"
"v_mfma_f32_16x16x32_bf16 v[200:203], v[216:219], v[72:75], v[200:203]\n"
"v_exp_f32 v194, v194\n"
"v_mfma_f32_16x16x32_bf16 v[200:203], v[220:223], v[76:79], v[200:203]\n"
"v_exp_f32 v195, v195\n"
"v_mfma_f32_16x16x32_bf16 v[200:203], v[224:227], v[80:83], v[200:203]\n"
"v_add_f32 v240, v240, v192\n"
"v_cvt_pk_bf16_f32 v250, v192, v193\n"
"v_mfma_f32_16x16x32_bf16 v[200:203], v[228:231], v[84:87], v[200:203]\n"
"v_add_f32 v240, v240, v193\n"
"v_cvt_pk_bf16_f32 v251, v194, v195\n"
"v_mfma_f32_16x16x32_bf16 v[200:203], v[232:235], v[88:91], v[200:203]\n"
"v_add_f32 v240, v240, v194\n"
"ds_write_b64 v247, v[250:251] offset:0\n"
"v_mfma_f32_16x16x32_bf16 v[200:203], v[236:239], v[92:95], v[200:203]\n"
"v_add_f32 v240, v240, v195\n"
"v_sub_f32 v196, v196, v253\n"
"v_mfma_f32_16x16x32_bf16 v[204:207], v[208:211], v[96:99], 0\n"
"v_sub_f32 v197, v197, v253\n"
"v_sub_f32 v198, v198, v253\n"
"v_mfma_f32_16x16x32_bf16 v[204:207], v[212:215], v[100:103], v[204:207]\n"
"v_sub_f32 v199, v199, v253\n"
"v_exp_f32 v196, v196\n"
"v_mfma_f32_16x16x32_bf16 v[204:207], v[216:219], v[104:107], v[204:207]\n"
"v_exp_f32 v197, v197\n"
"v_mfma_f32_16x16x32_bf16 v[204:207], v[220:223], v[108:111], v[204:207]\n"
"v_exp_f32 v198, v198\n"
"v_mfma_f32_16x16x32_bf16 v[204:207], v[224:227], v[112:115], v[204:207]\n"
"v_exp_f32 v199, v199\n"
"v_mfma_f32_16x16x32_bf16 v[204:207], v[228:231], v[116:119], v[204:207]\n"
"v_add_f32 v241, v241, v196\n"
"v_cvt_pk_bf16_f32 v250, v196, v197\n"
"v_mfma_f32_16x16x32_bf16 v[204:207], v[232:235], v[120:123], v[204:207]\n"
"v_add_f32 v241, v241, v197\n"
"v_cvt_pk_bf16_f32 v251, v198, v199\n"
"v_mfma_f32_16x16x32_bf16 v[204:207], v[236:239], v[124:127], v[204:207]\n"
"v_add_f32 v241, v241, v198\n"
"ds_write_b64 v247, v[250:251] offset:2048\n"
"v_add_f32 v241, v241, v199\n"
"v_sub_f32 v200, v200, v254\n"
"v_sub_f32 v201, v201, v254\n"
"v_sub_f32 v202, v202, v254\n"
"v_sub_f32 v203, v203, v254\n"
"v_exp_f32 v200, v200\n"
"v_exp_f32 v201, v201\n"
"v_exp_f32 v202, v202\n"
"v_exp_f32 v203, v203\n"
"v_add_f32 v242, v242, v200\n"
"v_cvt_pk_bf16_f32 v250, v200, v201\n"
"v_add_f32 v242, v242, v201\n"
"v_cvt_pk_bf16_f32 v251, v202, v203\n"
"v_add_f32 v242, v242, v202\n"
"ds_write_b64 v247, v[250:251] offset:4096\n"
"v_add_f32 v242, v242, v203\n"
"v_sub_f32 v204, v204, v255\n"
"v_sub_f32 v205, v205, v255\n"
"v_sub_f32 v206, v206, v255\n"
"v_sub_f32 v207, v207, v255\n"
"v_exp_f32 v204, v204\n"
"v_exp_f32 v205, v205\n"
"v_exp_f32 v206, v206\n"
"v_exp_f32 v207, v207\n"
"v_add_f32 v243, v243, v204\n"
"v_cvt_pk_bf16_f32 v250, v204, v205\n"
"v_add_f32 v243, v243, v205\n"
"v_cvt_pk_bf16_f32 v251, v206, v207\n"
"v_add_f32 v243, v243, v206\n"
"ds_write_b64 v247, v[250:251] offset:6144\n"
"v_add_f32 v243, v243, v207\n"
"v_mov_b32 v208, v240\n"
"s_nop 1\n"
"v_permlane16_swap_b32 v240, v208\n"
"v_add_f32 v240, v240, v208\n"
"v_mov_b32 v208, v240\n"
"s_nop 1\n"
"v_permlane32_swap_b32 v240, v208\n"
"v_add_f32 v240, v240, v208\n"
"v_mov_b32 v209, v241\n"
"s_nop 1\n"
"v_permlane16_swap_b32 v241, v209\n"
"v_add_f32 v241, v241, v209\n"
"v_mov_b32 v209, v241\n"
"s_nop 1\n"
"v_permlane32_swap_b32 v241, v209\n"
"v_add_f32 v241, v241, v209\n"
"v_mov_b32 v210, v242\n"
"s_nop 1\n"
"v_permlane16_swap_b32 v242, v210\n"
"v_add_f32 v242, v242, v210\n"
"v_mov_b32 v210, v242\n"
"s_nop 1\n"
"v_permlane32_swap_b32 v242, v210\n"
"v_add_f32 v242, v242, v210\n"
"v_mov_b32 v211, v243\n"
"s_nop 1\n"
"v_permlane16_swap_b32 v243, v211\n"
"v_add_f32 v243, v243, v211\n"
"v_mov_b32 v211, v243\n"
"s_nop 1\n"
"v_permlane32_swap_b32 v243, v211\n"
"v_add_f32 v243, v243, v211\n"
"v_mbcnt_lo_u32_b32 v216, -1, 0\n"
"v_mbcnt_hi_u32_b32 v216, -1, v216\n"
"v_and_b32 v216, 15, v216\n"
"v_lshlrev_b32 v216, 4, v216\n"
"s_lshl_b32 s84, s87, 2\n"
"s_add_u32 s84, s84, 161792\n"
"v_add_u32 v216, s84, v216\n"
"ds_write_b32 v216, v240 offset:0\n"
"ds_write_b32 v216, v241 offset:256\n"
"ds_write_b32 v216, v242 offset:512\n"
"ds_write_b32 v216, v243 offset:768\n"
"s_setprio 0\n"
"s_waitcnt vmcnt(0) lgkmcnt(0)\n"
"s_barrier\n"
"ds_read_b128 v[208:211], v248 offset:0\n"
"ds_read_b64_tr_b16 v[192:193], v245 offset:34816\n"
"ds_read_b64_tr_b16 v[194:195], v245 offset:43520\n"
"ds_read_b128 v[212:215], v248 offset:1024\n"
"ds_read_b64_tr_b16 v[196:197], v245 offset:52224\n"
"ds_read_b64_tr_b16 v[198:199], v245 offset:60928\n"
"ds_read_b128 v[216:219], v248 offset:2048\n"
"ds_read_b128 v[220:223], v248 offset:3072\n"
"ds_read_b128 v[224:227], v248 offset:4096\n"
"ds_read_b128 v[228:231], v248 offset:5120\n"
"ds_read_b128 v[232:235], v248 offset:6144\n"
"ds_read_b128 v[236:239], v248 offset:7168\n"
"v_mbcnt_lo_u32_b32 v24, -1, 0\n"
"v_mbcnt_hi_u32_b32 v24, -1, v24\n"
"v_lshrrev_b32 v22, 5, v24\n"
"v_and_b32 v24, 31, v24\n"
"v_lshlrev_b32 v23, 1, v22\n"
"v_xor_b32 v24, v24, v23\n"
"v_mul_u32_u24 v22, 896, v22\n"
"v_lshrrev_b32 v23, 3, v24\n"
"v_mul_u32_u24 v23, 224, v23\n"
"v_add_u32 v22, v22, v23\n"
"v_and_b32 v24, 7, v24\n"
"v_lshl_add_u32 v24, v24, 4, v22\n"
"v_add_u32 v24, 57344, v24\n"
"v_mbcnt_lo_u32_b32 v20, -1, 0\n"
"v_mbcnt_hi_u32_b32 v20, -1, v20\n"
"v_and_b32 v20, 15, v20\n"
"v_lshlrev_b32 v20, 4, v20\n"
"v_add_u32 v20, 161792, v20\n"
"ds_read_b128 v[0:3], v20 offset:0\n"
"ds_read_b128 v[4:7], v20 offset:256\n"
"ds_read_b128 v[8:11], v20 offset:512\n"
"ds_read_b128 v[12:15], v20 offset:768\n"
"s_add_u32 m0, s56, 69632\n"
"ds_read_b64_tr_b16 v[200:201], v246 offset:34816\n"
"buffer_load_dwordx4 v24, s[48:51], s36 offen lds\n"
"s_add_u32 m0, s57, 69632\n"
"ds_read_b64_tr_b16 v[202:203], v246 offset:43520\n"
"buffer_load_dwordx4 v24, s[48:51], s37 offen lds\n"
"s_add_u32 m0, s58, 69632\n"
"ds_read_b64_tr_b16 v[204:205], v246 offset:52224\n"
"buffer_load_dwordx4 v24, s[48:51], s38 offen lds\n"
"s_add_u32 m0, s59, 69632\n"
"ds_read_b64_tr_b16 v[206:207], v246 offset:60928\n"
"buffer_load_dwordx4 v24, s[48:51], s39 offen lds\n"
"s_add_u32 m0, s60, 69632\n"
"s_nop 0\n"
"buffer_load_dwordx4 v24, s[48:51], s40 offen lds\n"
"s_add_u32 m0, s61, 69632\n"
"s_nop 0\n"
"buffer_load_dwordx4 v24, s[48:51], s41 offen lds\n"
"s_add_u32 m0, s62, 69632\n"
"s_nop 0\n"
"buffer_load_dwordx4 v24, s[48:51], s42 offen lds\n"
"s_add_u32 m0, s63, 69632\n"
"s_nop 0\n"
"buffer_load_dwordx4 v24, s[48:51], s43 offen lds\n"
"s_waitcnt lgkmcnt(15)\n"
"v_mfma_f32_16x16x32_bf16 v[128:131], v[192:195], v[208:211], v[128:131]\n"
"s_waitcnt lgkmcnt(14)\n"
"v_mfma_f32_16x16x32_bf16 v[128:131], v[196:199], v[212:215], v[128:131]\n"
"s_waitcnt lgkmcnt(13)\n"
"v_mfma_f32_16x16x32_bf16 v[144:147], v[192:195], v[216:219], v[144:147]\n"
"s_waitcnt lgkmcnt(12)\n"
"v_mfma_f32_16x16x32_bf16 v[144:147], v[196:199], v[220:223], v[144:147]\n"
"s_waitcnt lgkmcnt(11)\n"
"v_mfma_f32_16x16x32_bf16 v[160:163], v[192:195], v[224:227], v[160:163]\n"
"s_waitcnt lgkmcnt(10)\n"
"v_mfma_f32_16x16x32_bf16 v[160:163], v[196:199], v[228:231], v[160:163]\n"
"s_waitcnt lgkmcnt(9)\n"
"v_mfma_f32_16x16x32_bf16 v[176:179], v[192:195], v[232:235], v[176:179]\n"
"s_waitcnt lgkmcnt(8)\n"
"v_mfma_f32_16x16x32_bf16 v[176:179], v[196:199], v[236:239], v[176:179]\n"
"ds_read_b64_tr_b16 v[192:193], v245 offset:34880\n"
"ds_read_b64_tr_b16 v[194:195], v245 offset:43584\n"
"ds_read_b64_tr_b16 v[196:197], v245 offset:52288\n"
"ds_read_b64_tr_b16 v[198:199], v245 offset:60992\n"
"s_waitcnt lgkmcnt(6)\n"
"v_mfma_f32_16x16x32_bf16 v[132:135], v[200:203], v[208:211], v[132:135]\n"
"s_waitcnt lgkmcnt(4)\n"
"v_mfma_f32_16x16x32_bf16 v[132:135], v[204:207], v[212:215], v[132:135]\n"
"v_mfma_f32_16x16x32_bf16 v[148:151], v[200:203], v[216:219], v[148:151]\n"
"v_mfma_f32_16x16x32_bf16 v[148:151], v[204:207], v[220:223], v[148:151]\n"
"v_mfma_f32_16x16x32_bf16 v[164:167], v[200:203], v[224:227], v[164:167]\n"
"v_mfma_f32_16x16x32_bf16 v[164:167], v[204:207], v[228:231], v[164:167]\n"
"v_mfma_f32_16x16x32_bf16 v[180:183], v[200:203], v[232:235], v[180:183]\n"
"v_mfma_f32_16x16x32_bf16 v[180:183], v[204:207], v[236:239], v[180:183]\n"
"ds_read_b64_tr_b16 v[200:201], v246 offset:34880\n"
"ds_read_b64_tr_b16 v[202:203], v246 offset:43584\n"
"ds_read_b64_tr_b16 v[204:205], v246 offset:52288\n"
"ds_read_b64_tr_b16 v[206:207], v246 offset:60992\n"
"s_waitcnt lgkmcnt(6)\n"
"v_mfma_f32_16x16x32_bf16 v[136:139], v[192:195], v[208:211], v[136:139]\n"
"s_waitcnt lgkmcnt(4)\n"
"v_mfma_f32_16x16x32_bf16 v[136:139], v[196:199], v[212:215], v[136:139]\n"
"v_mfma_f32_16x16x32_bf16 v[152:155], v[192:195], v[216:219], v[152:155]\n"
"v_mfma_f32_16x16x32_bf16 v[152:155], v[196:199], v[220:223], v[152:155]\n"
"v_mfma_f32_16x16x32_bf16 v[168:171], v[192:195], v[224:227], v[168:171]\n"
"v_mfma_f32_16x16x32_bf16 v[168:171], v[196:199], v[228:231], v[168:171]\n"
"v_mfma_f32_16x16x32_bf16 v[184:187], v[192:195], v[232:235], v[184:187]\n"
"v_mfma_f32_16x16x32_bf16 v[184:187], v[196:199], v[236:239], v[184:187]\n"
"s_waitcnt lgkmcnt(2)\n"
"v_mfma_f32_16x16x32_bf16 v[140:143], v[200:203], v[208:211], v[140:143]\n"
"s_waitcnt lgkmcnt(0)\n"
"v_mfma_f32_16x16x32_bf16 v[140:143], v[204:207], v[212:215], v[140:143]\n"
"v_mfma_f32_16x16x32_bf16 v[156:159], v[200:203], v[216:219], v[156:159]\n"
"v_mfma_f32_16x16x32_bf16 v[156:159], v[204:207], v[220:223], v[156:159]\n"
"v_mfma_f32_16x16x32_bf16 v[172:175], v[200:203], v[224:227], v[172:175]\n"
"v_mfma_f32_16x16x32_bf16 v[172:175], v[204:207], v[228:231], v[172:175]\n"
"v_mfma_f32_16x16x32_bf16 v[188:191], v[200:203], v[232:235], v[188:191]\n"
"v_mfma_f32_16x16x32_bf16 v[188:191], v[204:207], v[236:239], v[188:191]\n"
"s_barrier\n"
"s_branch L_postA\n"
"L_roleB:\n"
"s_mov_b64 s[44:45], %2\n"
"s_and_b32 s45, s45, 0xffff\n"
"s_mov_b32 s46, 1048576\n"
"s_mov_b32 s47, 0x20000\n"
"s_mov_b64 s[52:53], %3\n"
"s_and_b32 s53, s53, 0xffff\n"
"s_mov_b32 s54, 1048576\n"
"s_mov_b32 s55, 0x20000\n"
"v_add_u32 v96, 32768, v249\n"
"ds_read_b128 v[208:211], v104 offset:8192\n"
"ds_read_b128 v[212:215], v104 offset:9216\n"
"ds_read_b128 v[216:219], v104 offset:10240\n"
"ds_read_b128 v[220:223], v104 offset:11264\n"
"ds_read_b128 v[224:227], v104 offset:12288\n"
"ds_read_b128 v[228:231], v104 offset:13312\n"
"ds_read_b64_tr_b16 v[192:193], v102 offset:0\n"
"ds_read_b64_tr_b16 v[194:195], v102 offset:8704\n"
"ds_read_b64_tr_b16 v[196:197], v102 offset:17408\n"
"ds_read_b64_tr_b16 v[198:199], v102 offset:26112\n"
"ds_read_b64_tr_b16 v[200:201], v103 offset:0\n"
"ds_read_b64_tr_b16 v[202:203], v103 offset:8704\n"
"ds_read_b64_tr_b16 v[204:205], v103 offset:17408\n"
"ds_read_b64_tr_b16 v[206:207], v103 offset:26112\n"
"s_waitcnt lgkmcnt(6)\n"
"v_mfma_f32_16x16x32_bf16 v[128:131], v[192:195], v[208:211], v[128:131]\n"
"s_add_u32 m0, s72, 69632\n"
"s_nop 0\n"
"buffer_load_dwordx4 v100, s[40:43], s76 offen lds\n"
"s_waitcnt lgkmcnt(4)\n"
"v_mfma_f32_16x16x32_bf16 v[128:131], v[196:199], v[212:215], v[128:131]\n"
"v_mfma_f32_16x16x32_bf16 v[144:147], v[192:195], v[216:219], v[144:147]\n"
"v_mfma_f32_16x16x32_bf16 v[144:147], v[196:199], v[220:223], v[144:147]\n"
"s_add_u32 m0, s73, 69632\n"
"s_nop 0\n"
"buffer_load_dwordx4 v100, s[40:43], s77 offen lds\n"
"v_mfma_f32_16x16x32_bf16 v[160:163], v[192:195], v[224:227], v[160:163]\n"
"v_mfma_f32_16x16x32_bf16 v[160:163], v[196:199], v[228:231], v[160:163]\n"
"ds_read_b64_tr_b16 v[192:193], v102 offset:64\n"
"ds_read_b64_tr_b16 v[194:195], v102 offset:8768\n"
"ds_read_b64_tr_b16 v[196:197], v102 offset:17472\n"
"ds_read_b64_tr_b16 v[198:199], v102 offset:26176\n"
"s_waitcnt lgkmcnt(6)\n"
"v_mfma_f32_16x16x32_bf16 v[132:135], v[200:203], v[208:211], v[132:135]\n"
"s_add_u32 m0, s74, 69632\n"
"s_nop 0\n"
"buffer_load_dwordx4 v100, s[40:43], s78 offen lds\n"
"s_waitcnt lgkmcnt(4)\n"
"v_mfma_f32_16x16x32_bf16 v[132:135], v[204:207], v[212:215], v[132:135]\n"
"v_mfma_f32_16x16x32_bf16 v[148:151], v[200:203], v[216:219], v[148:151]\n"
"v_mfma_f32_16x16x32_bf16 v[148:151], v[204:207], v[220:223], v[148:151]\n"
"s_add_u32 m0, s75, 69632\n"
"s_nop 0\n"
"buffer_load_dwordx4 v100, s[40:43], s79 offen lds\n"
"s_add_u32 s90, s90, 1\n"
"s_cmp_eq_u32 s90, 7\n"
"s_cselect_b32 s90, 0, s90\n"
"s_lshl_b32 s84, s90, 19\n"
"v_add_u32 v100, s84, v106\n"
"v_mfma_f32_16x16x32_bf16 v[164:167], v[200:203], v[224:227], v[164:167]\n"
"v_mfma_f32_16x16x32_bf16 v[164:167], v[204:207], v[228:231], v[164:167]\n"
"ds_read_b64_tr_b16 v[200:201], v103 offset:64\n"
"ds_read_b64_tr_b16 v[202:203], v103 offset:8768\n"
"ds_read_b64_tr_b16 v[204:205], v103 offset:17472\n"
"ds_read_b64_tr_b16 v[206:207], v103 offset:26176\n"
"s_waitcnt lgkmcnt(6)\n"
"v_mfma_f32_16x16x32_bf16 v[136:139], v[192:195], v[208:211], v[136:139]\n"
"ds_read_b128 v[0:3], v104 offset:22528\n"
"s_add_u32 m0, s82, 43008\n"
"s_nop 0\n"
"buffer_load_dwordx4 v101, s[36:39], s80 offen lds\n"
"s_waitcnt lgkmcnt(5)\n"
"v_mfma_f32_16x16x32_bf16 v[136:139], v[196:199], v[212:215], v[136:139]\n"
"ds_read_b128 v[4:7], v104 offset:23552\n"
"v_mfma_f32_16x16x32_bf16 v[152:155], v[192:195], v[216:219], v[152:155]\n"
"ds_read_b128 v[8:11], v104 offset:24576\n"
"v_mfma_f32_16x16x32_bf16 v[152:155], v[196:199], v[220:223], v[152:155]\n"
"ds_read_b128 v[12:15], v104 offset:25600\n"
"s_add_u32 m0, s82, 44032\n"
"s_nop 0\n"
"buffer_load_dwordx4 v101, s[36:39], s81 offen lds\n"
"s_add_u32 s89, s89, 1\n"
"s_cmp_eq_u32 s89, 7\n"
"s_cselect_b32 s89, 0, s89\n"
"s_lshl_b32 s85, s89, 7\n"
"v_add_u32 v101, s85, v107\n"
"v_mfma_f32_16x16x32_bf16 v[168:171], v[192:195], v[224:227], v[168:171]\n"
"ds_read_b128 v[16:19], v104 offset:26624\n"
"v_mfma_f32_16x16x32_bf16 v[168:171], v[196:199], v[228:231], v[168:171]\n"
"ds_read_b128 v[20:23], v104 offset:27648\n"
"s_waitcnt lgkmcnt(8)\n"
"v_mfma_f32_16x16x32_bf16 v[140:143], v[200:203], v[208:211], v[140:143]\n"
"s_waitcnt lgkmcnt(6)\n"
"v_mfma_f32_16x16x32_bf16 v[140:143], v[204:207], v[212:215], v[140:143]\n"
"v_mfma_f32_16x16x32_bf16 v[156:159], v[200:203], v[216:219], v[156:159]\n"
"v_mfma_f32_16x16x32_bf16 v[156:159], v[204:207], v[220:223], v[156:159]\n"
"s_waitcnt lgkmcnt(0)\n"
"v_readfirstlane_b32 s88, v108\n"
"s_nop 1\n"
"s_add_u32 s88, s88, 1\n"
"s_waitcnt vmcnt(6)\n"
"s_barrier\n"
"v_mfma_f32_16x16x32_bf16 v[172:175], v[200:203], v[224:227], v[172:175]\n"
"ds_read_b64_tr_b16 v[192:193], v102 offset:34816\n"
"v_mfma_f32_16x16x32_bf16 v[172:175], v[204:207], v[228:231], v[172:175]\n"
"ds_read_b64_tr_b16 v[194:195], v102 offset:43520\n"
"ds_read_b64_tr_b16 v[196:197], v102 offset:52224\n"
"ds_read_b64_tr_b16 v[198:199], v102 offset:60928\n"
"ds_read_b64_tr_b16 v[200:201], v103 offset:34816\n"
"ds_read_b64_tr_b16 v[202:203], v103 offset:43520\n"
"ds_read_b64_tr_b16 v[204:205], v103 offset:52224\n"
"ds_read_b64_tr_b16 v[206:207], v103 offset:60928\n"
"s_waitcnt lgkmcnt(6)\n"
"v_mfma_f32_16x16x32_bf16 v[128:131], v[192:195], v[0:3], v[128:131]\n"
"s_add_u32 m0, s72, 0\n"
"s_nop 0\n"
"buffer_load_dwordx4 v100, s[40:43], s76 offen lds\n"
"s_waitcnt lgkmcnt(4)\n"
"v_mfma_f32_16x16x32_bf16 v[128:131], v[196:199], v[4:7], v[128:131]\n"
"v_mfma_f32_16x16x32_bf16 v[144:147], v[192:195], v[8:11], v[144:147]\n"
"v_mfma_f32_16x16x32_bf16 v[144:147], v[196:199], v[12:15], v[144:147]\n"
"s_add_u32 m0, s73, 0\n"
"s_nop 0\n"
"buffer_load_dwordx4 v100, s[40:43], s77 offen lds\n"
"v_mfma_f32_16x16x32_bf16 v[160:163], v[192:195], v[16:19], v[160:163]\n"
"v_mfma_f32_16x16x32_bf16 v[160:163], v[196:199], v[20:23], v[160:163]\n"
"ds_read_b64_tr_b16 v[192:193], v102 offset:34880\n"
"ds_read_b64_tr_b16 v[194:195], v102 offset:43584\n"
"ds_read_b64_tr_b16 v[196:197], v102 offset:52288\n"
"ds_read_b64_tr_b16 v[198:199], v102 offset:60992\n"
"s_waitcnt lgkmcnt(6)\n"
"v_mfma_f32_16x16x32_bf16 v[132:135], v[200:203], v[0:3], v[132:135]\n"
"s_add_u32 m0, s74, 0\n"
"s_nop 0\n"
"buffer_load_dwordx4 v100, s[40:43], s78 offen lds\n"
"s_waitcnt lgkmcnt(4)\n"
"v_mfma_f32_16x16x32_bf16 v[132:135], v[204:207], v[4:7], v[132:135]\n"
"v_mfma_f32_16x16x32_bf16 v[148:151], v[200:203], v[8:11], v[148:151]\n"
"v_mfma_f32_16x16x32_bf16 v[148:151], v[204:207], v[12:15], v[148:151]\n"
"s_add_u32 m0, s75, 0\n"
"s_nop 0\n"
"buffer_load_dwordx4 v100, s[40:43], s79 offen lds\n"
"s_add_u32 s90, s90, 1\n"
"s_cmp_eq_u32 s90, 7\n"
"s_cselect_b32 s90, 0, s90\n"
"s_lshl_b32 s84, s90, 19\n"
"v_add_u32 v100, s84, v106\n"
"v_mfma_f32_16x16x32_bf16 v[164:167], v[200:203], v[16:19], v[164:167]\n"
"v_mfma_f32_16x16x32_bf16 v[164:167], v[204:207], v[20:23], v[164:167]\n"
"ds_read_b64_tr_b16 v[200:201], v103 offset:34880\n"
"ds_read_b64_tr_b16 v[202:203], v103 offset:43584\n"
"ds_read_b64_tr_b16 v[204:205], v103 offset:52288\n"
"ds_read_b64_tr_b16 v[206:207], v103 offset:60992\n"
"s_waitcnt lgkmcnt(6)\n"
"v_mfma_f32_16x16x32_bf16 v[136:139], v[192:195], v[0:3], v[136:139]\n"
"ds_read_b128 v[208:211], v104 offset:36864\n"
"s_add_u32 m0, s82, 0\n"
"s_nop 0\n"
"buffer_load_dwordx4 v101, s[36:39], s80 offen lds\n"
"s_waitcnt lgkmcnt(5)\n"
"v_mfma_f32_16x16x32_bf16 v[136:139], v[196:199], v[4:7], v[136:139]\n"
"ds_read_b128 v[212:215], v104 offset:37888\n"
"v_mfma_f32_16x16x32_bf16 v[152:155], v[192:195], v[8:11], v[152:155]\n"
"ds_read_b128 v[216:219], v104 offset:38912\n"
"v_mfma_f32_16x16x32_bf16 v[152:155], v[196:199], v[12:15], v[152:155]\n"
"ds_read_b128 v[220:223], v104 offset:39936\n"
"s_add_u32 m0, s82, 1024\n"
"s_nop 0\n"
"buffer_load_dwordx4 v101, s[36:39], s81 offen lds\n"
"s_add_u32 s89, s89, 1\n"
"s_cmp_eq_u32 s89, 7\n"
"s_cselect_b32 s89, 0, s89\n"
"s_lshl_b32 s85, s89, 7\n"
"v_add_u32 v101, s85, v107\n"
"v_mfma_f32_16x16x32_bf16 v[168:171], v[192:195], v[16:19], v[168:171]\n"
"ds_read_b128 v[224:227], v104 offset:40960\n"
"v_mfma_f32_16x16x32_bf16 v[168:171], v[196:199], v[20:23], v[168:171]\n"
"ds_read_b128 v[228:231], v104 offset:41984\n"
"s_waitcnt lgkmcnt(8)\n"
"v_mfma_f32_16x16x32_bf16 v[140:143], v[200:203], v[0:3], v[140:143]\n"
"s_waitcnt lgkmcnt(6)\n"
"v_mfma_f32_16x16x32_bf16 v[140:143], v[204:207], v[4:7], v[140:143]\n"
"v_mfma_f32_16x16x32_bf16 v[156:159], v[200:203], v[8:11], v[156:159]\n"
"v_mfma_f32_16x16x32_bf16 v[156:159], v[204:207], v[12:15], v[156:159]\n"
"s_waitcnt lgkmcnt(0)\n"
"s_waitcnt vmcnt(6)\n"
"s_barrier\n"
"v_mfma_f32_16x16x32_bf16 v[172:175], v[200:203], v[16:19], v[172:175]\n"
"ds_read_b64_tr_b16 v[192:193], v245 offset:0\n"
"v_mfma_f32_16x16x32_bf16 v[172:175], v[204:207], v[20:23], v[172:175]\n"
"ds_read_b64_tr_b16 v[194:195], v245 offset:8704\n"
"ds_read_b64_tr_b16 v[196:197], v245 offset:17408\n"
"ds_read_b64_tr_b16 v[198:199], v245 offset:26112\n"
"ds_read_b64_tr_b16 v[200:201], v246 offset:0\n"
"ds_read_b64_tr_b16 v[202:203], v246 offset:8704\n"
"ds_read_b64_tr_b16 v[204:205], v246 offset:17408\n"
"ds_read_b64_tr_b16 v[206:207], v246 offset:26112\n"
"s_waitcnt lgkmcnt(6)\n"
"v_mfma_f32_16x16x32_bf16 v[128:131], v[192:195], v[208:211], v[128:131]\n"
"s_add_u32 m0, s72, 34816\n"
"s_nop 0\n"
"buffer_load_dwordx4 v100, s[40:43], s76 offen lds\n"
"s_waitcnt lgkmcnt(4)\n"
"v_mfma_f32_16x16x32_bf16 v[128:131], v[196:199], v[212:215], v[128:131]\n"
"v_mfma_f32_16x16x32_bf16 v[144:147], v[192:195], v[216:219], v[144:147]\n"
"v_mfma_f32_16x16x32_bf16 v[144:147], v[196:199], v[220:223], v[144:147]\n"
"s_add_u32 m0, s73, 34816\n"
"s_nop 0\n"
"buffer_load_dwordx4 v100, s[40:43], s77 offen lds\n"
"v_mfma_f32_16x16x32_bf16 v[160:163], v[192:195], v[224:227], v[160:163]\n"
"v_mfma_f32_16x16x32_bf16 v[160:163], v[196:199], v[228:231], v[160:163]\n"
"ds_read_b64_tr_b16 v[192:193], v245 offset:64\n"
"ds_read_b64_tr_b16 v[194:195], v245 offset:8768\n"
"ds_read_b64_tr_b16 v[196:197], v245 offset:17472\n"
"ds_read_b64_tr_b16 v[198:199], v245 offset:26176\n"
"s_waitcnt lgkmcnt(6)\n"
"v_mfma_f32_16x16x32_bf16 v[132:135], v[200:203], v[208:211], v[132:135]\n"
"s_add_u32 m0, s74, 34816\n"
"s_nop 0\n"
"buffer_load_dwordx4 v100, s[40:43], s78 offen lds\n"
"s_waitcnt lgkmcnt(4)\n"
"v_mfma_f32_16x16x32_bf16 v[132:135], v[204:207], v[212:215], v[132:135]\n"
"v_mfma_f32_16x16x32_bf16 v[148:151], v[200:203], v[216:219], v[148:151]\n"
"v_mfma_f32_16x16x32_bf16 v[148:151], v[204:207], v[220:223], v[148:151]\n"
"s_add_u32 m0, s75, 34816\n"
"s_nop 0\n"
"buffer_load_dwordx4 v100, s[40:43], s79 offen lds\n"
"s_add_u32 s90, s90, 1\n"
"s_cmp_eq_u32 s90, 7\n"
"s_cselect_b32 s90, 0, s90\n"
"s_lshl_b32 s84, s90, 19\n"
"v_add_u32 v100, s84, v106\n"
"v_mfma_f32_16x16x32_bf16 v[164:167], v[200:203], v[224:227], v[164:167]\n"
"v_mfma_f32_16x16x32_bf16 v[164:167], v[204:207], v[228:231], v[164:167]\n"
"ds_read_b64_tr_b16 v[200:201], v246 offset:64\n"
"ds_read_b64_tr_b16 v[202:203], v246 offset:8768\n"
"ds_read_b64_tr_b16 v[204:205], v246 offset:17472\n"
"ds_read_b64_tr_b16 v[206:207], v246 offset:26176\n"
"s_waitcnt lgkmcnt(6)\n"
"v_mfma_f32_16x16x32_bf16 v[136:139], v[192:195], v[208:211], v[136:139]\n"
"ds_read_b128 v[0:3], v104 offset:51200\n"
"s_add_u32 m0, s82, 14336\n"
"s_nop 0\n"
"buffer_load_dwordx4 v101, s[36:39], s80 offen lds\n"
"s_waitcnt lgkmcnt(5)\n"
"v_mfma_f32_16x16x32_bf16 v[136:139], v[196:199], v[212:215], v[136:139]\n"
"ds_read_b128 v[4:7], v104 offset:52224\n"
"v_mfma_f32_16x16x32_bf16 v[152:155], v[192:195], v[216:219], v[152:155]\n"
"ds_read_b128 v[8:11], v104 offset:53248\n"
"v_mfma_f32_16x16x32_bf16 v[152:155], v[196:199], v[220:223], v[152:155]\n"
"ds_read_b128 v[12:15], v104 offset:54272\n"
"s_add_u32 m0, s82, 15360\n"
"s_nop 0\n"
"buffer_load_dwordx4 v101, s[36:39], s81 offen lds\n"
"s_add_u32 s89, s89, 1\n"
"s_cmp_eq_u32 s89, 7\n"
"s_cselect_b32 s89, 0, s89\n"
"s_lshl_b32 s85, s89, 7\n"
"v_add_u32 v101, s85, v107\n"
"v_mfma_f32_16x16x32_bf16 v[168:171], v[192:195], v[224:227], v[168:171]\n"
"ds_read_b128 v[16:19], v104 offset:55296\n"
"v_mfma_f32_16x16x32_bf16 v[168:171], v[196:199], v[228:231], v[168:171]\n"
"ds_read_b128 v[20:23], v104 offset:56320\n"
"s_waitcnt lgkmcnt(8)\n"
"v_mfma_f32_16x16x32_bf16 v[140:143], v[200:203], v[208:211], v[140:143]\n"
"s_waitcnt lgkmcnt(6)\n"
"v_mfma_f32_16x16x32_bf16 v[140:143], v[204:207], v[212:215], v[140:143]\n"
"v_mfma_f32_16x16x32_bf16 v[156:159], v[200:203], v[216:219], v[156:159]\n"
"v_mfma_f32_16x16x32_bf16 v[156:159], v[204:207], v[220:223], v[156:159]\n"
"s_waitcnt lgkmcnt(0)\n"
"s_waitcnt vmcnt(6)\n"
"s_barrier\n"
"v_mfma_f32_16x16x32_bf16 v[172:175], v[200:203], v[224:227], v[172:175]\n"
"ds_read_b64_tr_b16 v[192:193], v102 offset:0\n"
"v_mfma_f32_16x16x32_bf16 v[172:175], v[204:207], v[228:231], v[172:175]\n"
"ds_read_b64_tr_b16 v[194:195], v102 offset:8704\n"
"ds_read_b64_tr_b16 v[196:197], v102 offset:17408\n"
"ds_read_b64_tr_b16 v[198:199], v102 offset:26112\n"
"ds_read_b64_tr_b16 v[200:201], v103 offset:0\n"
"ds_read_b64_tr_b16 v[202:203], v103 offset:8704\n"
"ds_read_b64_tr_b16 v[204:205], v103 offset:17408\n"
"ds_read_b64_tr_b16 v[206:207], v103 offset:26112\n"
"s_waitcnt lgkmcnt(6)\n"
"v_mfma_f32_16x16x32_bf16 v[128:131], v[192:195], v[0:3], v[128:131]\n"
"s_add_u32 m0, s72, 69632\n"
"s_nop 0\n"
"buffer_load_dwordx4 v100, s[40:43], s76 offen lds\n"
"s_waitcnt lgkmcnt(4)\n"
"v_mfma_f32_16x16x32_bf16 v[128:131], v[196:199], v[4:7], v[128:131]\n"
"v_mfma_f32_16x16x32_bf16 v[144:147], v[192:195], v[8:11], v[144:147]\n"
"v_mfma_f32_16x16x32_bf16 v[144:147], v[196:199], v[12:15], v[144:147]\n"
"s_add_u32 m0, s73, 69632\n"
"s_nop 0\n"
"buffer_load_dwordx4 v100, s[40:43], s77 offen lds\n"
"v_mfma_f32_16x16x32_bf16 v[160:163], v[192:195], v[16:19], v[160:163]\n"
"v_mfma_f32_16x16x32_bf16 v[160:163], v[196:199], v[20:23], v[160:163]\n"
"ds_read_b64_tr_b16 v[192:193], v102 offset:64\n"
"ds_read_b64_tr_b16 v[194:195], v102 offset:8768\n"
"ds_read_b64_tr_b16 v[196:197], v102 offset:17472\n"
"ds_read_b64_tr_b16 v[198:199], v102 offset:26176\n"
"s_waitcnt lgkmcnt(6)\n"
"v_mfma_f32_16x16x32_bf16 v[132:135], v[200:203], v[0:3], v[132:135]\n"
"s_add_u32 m0, s74, 69632\n"
"s_nop 0\n"
"buffer_load_dwordx4 v100, s[40:43], s78 offen lds\n"
"s_waitcnt lgkmcnt(4)\n"
"v_mfma_f32_16x16x32_bf16 v[132:135], v[204:207], v[4:7], v[132:135]\n"
"v_mfma_f32_16x16x32_bf16 v[148:151], v[200:203], v[8:11], v[148:151]\n"
"v_mfma_f32_16x16x32_bf16 v[148:151], v[204:207], v[12:15], v[148:151]\n"
"s_add_u32 m0, s75, 69632\n"
"s_nop 0\n"
"buffer_load_dwordx4 v100, s[40:43], s79 offen lds\n"
"s_add_u32 s90, s90, 1\n"
"s_cmp_eq_u32 s90, 7\n"
"s_cselect_b32 s90, 0, s90\n"
"s_lshl_b32 s84, s90, 19\n"
"v_add_u32 v100, s84, v106\n"
"v_mfma_f32_16x16x32_bf16 v[164:167], v[200:203], v[16:19], v[164:167]\n"
"v_mfma_f32_16x16x32_bf16 v[164:167], v[204:207], v[20:23], v[164:167]\n"
"ds_read_b64_tr_b16 v[200:201], v103 offset:64\n"
"ds_read_b64_tr_b16 v[202:203], v103 offset:8768\n"
"ds_read_b64_tr_b16 v[204:205], v103 offset:17472\n"
"ds_read_b64_tr_b16 v[206:207], v103 offset:26176\n"
"s_waitcnt lgkmcnt(6)\n"
"v_mfma_f32_16x16x32_bf16 v[136:139], v[192:195], v[0:3], v[136:139]\n"
"ds_read_b128 v[208:211], v104 offset:8192\n"
"s_add_u32 m0, s82, 28672\n"
"s_nop 0\n"
"buffer_load_dwordx4 v101, s[36:39], s80 offen lds\n"
"s_waitcnt lgkmcnt(5)\n"
"v_mfma_f32_16x16x32_bf16 v[136:139], v[196:199], v[4:7], v[136:139]\n"
"ds_read_b128 v[212:215], v104 offset:9216\n"
"v_mfma_f32_16x16x32_bf16 v[152:155], v[192:195], v[8:11], v[152:155]\n"
"ds_read_b128 v[216:219], v104 offset:10240\n"
"v_mfma_f32_16x16x32_bf16 v[152:155], v[196:199], v[12:15], v[152:155]\n"
"ds_read_b128 v[220:223], v104 offset:11264\n"
"s_add_u32 m0, s82, 29696\n"
"s_nop 0\n"
"buffer_load_dwordx4 v101, s[36:39], s81 offen lds\n"
"s_add_u32 s89, s89, 1\n"
"s_cmp_eq_u32 s89, 7\n"
"s_cselect_b32 s89, 0, s89\n"
"s_lshl_b32 s85, s89, 7\n"
"v_add_u32 v101, s85, v107\n"
"v_mfma_f32_16x16x32_bf16 v[168:171], v[192:195], v[16:19], v[168:171]\n"
"ds_read_b128 v[224:227], v104 offset:12288\n"
"v_mfma_f32_16x16x32_bf16 v[168:171], v[196:199], v[20:23], v[168:171]\n"
"ds_read_b128 v[228:231], v104 offset:13312\n"
"s_waitcnt lgkmcnt(8)\n"
"v_mfma_f32_16x16x32_bf16 v[140:143], v[200:203], v[0:3], v[140:143]\n"
"s_waitcnt lgkmcnt(6)\n"
"v_mfma_f32_16x16x32_bf16 v[140:143], v[204:207], v[4:7], v[140:143]\n"
"v_mfma_f32_16x16x32_bf16 v[156:159], v[200:203], v[8:11], v[156:159]\n"
"v_mfma_f32_16x16x32_bf16 v[156:159], v[204:207], v[12:15], v[156:159]\n"
"s_waitcnt lgkmcnt(0)\n"
"s_waitcnt vmcnt(6)\n"
"s_barrier\n"
"v_mfma_f32_16x16x32_bf16 v[172:175], v[200:203], v[16:19], v[172:175]\n"
"ds_read_b64_tr_b16 v[192:193], v102 offset:34816\n"
"v_mfma_f32_16x16x32_bf16 v[172:175], v[204:207], v[20:23], v[172:175]\n"
"ds_read_b64_tr_b16 v[194:195], v102 offset:43520\n"
"ds_read_b64_tr_b16 v[196:197], v102 offset:52224\n"
"ds_read_b64_tr_b16 v[198:199], v102 offset:60928\n"
"ds_read_b64_tr_b16 v[200:201], v103 offset:34816\n"
"ds_read_b64_tr_b16 v[202:203], v103 offset:43520\n"
"ds_read_b64_tr_b16 v[204:205], v103 offset:52224\n"
"ds_read_b64_tr_b16 v[206:207], v103 offset:60928\n"
"s_waitcnt lgkmcnt(6)\n"
"v_mfma_f32_16x16x32_bf16 v[128:131], v[192:195], v[208:211], v[128:131]\n"
"s_add_u32 m0, s72, 0\n"
"s_nop 0\n"
"buffer_load_dwordx4 v100, s[40:43], s76 offen lds\n"
"s_waitcnt lgkmcnt(4)\n"
"v_mfma_f32_16x16x32_bf16 v[128:131], v[196:199], v[212:215], v[128:131]\n"
"v_mfma_f32_16x16x32_bf16 v[144:147], v[192:195], v[216:219], v[144:147]\n"
"v_mfma_f32_16x16x32_bf16 v[144:147], v[196:199], v[220:223], v[144:147]\n"
"s_add_u32 m0, s73, 0\n"
"s_nop 0\n"
"buffer_load_dwordx4 v100, s[40:43], s77 offen lds\n"
"v_mfma_f32_16x16x32_bf16 v[160:163], v[192:195], v[224:227], v[160:163]\n"
"v_mfma_f32_16x16x32_bf16 v[160:163], v[196:199], v[228:231], v[160:163]\n"
"ds_read_b64_tr_b16 v[192:193], v102 offset:34880\n"
"ds_read_b64_tr_b16 v[194:195], v102 offset:43584\n"
"ds_read_b64_tr_b16 v[196:197], v102 offset:52288\n"
"ds_read_b64_tr_b16 v[198:199], v102 offset:60992\n"
"s_waitcnt lgkmcnt(6)\n"
"v_mfma_f32_16x16x32_bf16 v[132:135], v[200:203], v[208:211], v[132:135]\n"
"s_add_u32 m0, s74, 0\n"
"s_nop 0\n"
"buffer_load_dwordx4 v100, s[40:43], s78 offen lds\n"
"s_waitcnt lgkmcnt(4)\n"
"v_mfma_f32_16x16x32_bf16 v[132:135], v[204:207], v[212:215], v[132:135]\n"
"v_mfma_f32_16x16x32_bf16 v[148:151], v[200:203], v[216:219], v[148:151]\n"
"v_mfma_f32_16x16x32_bf16 v[148:151], v[204:207], v[220:223], v[148:151]\n"
"s_add_u32 m0, s75, 0\n"
"s_nop 0\n"
"buffer_load_dwordx4 v100, s[40:43], s79 offen lds\n"
"s_add_u32 s90, s90, 1\n"
"s_cmp_eq_u32 s90, 7\n"
"s_cselect_b32 s90, 0, s90\n"
"s_lshl_b32 s84, s90, 19\n"
"v_add_u32 v100, s84, v106\n"
"v_mfma_f32_16x16x32_bf16 v[164:167], v[200:203], v[224:227], v[164:167]\n"
"v_mfma_f32_16x16x32_bf16 v[164:167], v[204:207], v[228:231], v[164:167]\n"
"ds_read_b64_tr_b16 v[200:201], v103 offset:34880\n"
"ds_read_b64_tr_b16 v[202:203], v103 offset:43584\n"
"ds_read_b64_tr_b16 v[204:205], v103 offset:52288\n"
"ds_read_b64_tr_b16 v[206:207], v103 offset:60992\n"
"s_waitcnt lgkmcnt(6)\n"
"v_mfma_f32_16x16x32_bf16 v[136:139], v[192:195], v[208:211], v[136:139]\n"
"ds_read_b128 v[0:3], v104 offset:22528\n"
"s_waitcnt lgkmcnt(5)\n"
"v_mfma_f32_16x16x32_bf16 v[136:139], v[196:199], v[212:215], v[136:139]\n"
"ds_read_b128 v[4:7], v104 offset:23552\n"
"v_mfma_f32_16x16x32_bf16 v[152:155], v[192:195], v[216:219], v[152:155]\n"
"ds_read_b128 v[8:11], v104 offset:24576\n"
"v_mfma_f32_16x16x32_bf16 v[152:155], v[196:199], v[220:223], v[152:155]\n"
"ds_read_b128 v[12:15], v104 offset:25600\n"
"v_mfma_f32_16x16x32_bf16 v[168:171], v[192:195], v[224:227], v[168:171]\n"
"ds_read_b128 v[16:19], v104 offset:26624\n"
"v_mfma_f32_16x16x32_bf16 v[168:171], v[196:199], v[228:231], v[168:171]\n"
"ds_read_b128 v[20:23], v104 offset:27648\n"
"s_waitcnt lgkmcnt(8)\n"
"v_mfma_f32_16x16x32_bf16 v[140:143], v[200:203], v[208:211], v[140:143]\n"
"s_waitcnt lgkmcnt(6)\n"
"v_mfma_f32_16x16x32_bf16 v[140:143], v[204:207], v[212:215], v[140:143]\n"
"v_mfma_f32_16x16x32_bf16 v[156:159], v[200:203], v[216:219], v[156:159]\n"
"v_mfma_f32_16x16x32_bf16 v[156:159], v[204:207], v[220:223], v[156:159]\n"
"s_waitcnt lgkmcnt(0)\n"
"s_waitcnt vmcnt(4)\n"
"s_barrier\n"
"v_mfma_f32_16x16x32_bf16 v[172:175], v[200:203], v[224:227], v[172:175]\n"
"ds_read_b64_tr_b16 v[192:193], v245 offset:0\n"
"v_mfma_f32_16x16x32_bf16 v[172:175], v[204:207], v[228:231], v[172:175]\n"
"ds_read_b64_tr_b16 v[194:195], v245 offset:8704\n"
"ds_read_b64_tr_b16 v[196:197], v245 offset:17408\n"
"ds_read_b64_tr_b16 v[198:199], v245 offset:26112\n"
"ds_read_b64_tr_b16 v[200:201], v246 offset:0\n"
"ds_read_b64_tr_b16 v[202:203], v246 offset:8704\n"
"ds_read_b64_tr_b16 v[204:205], v246 offset:17408\n"
"ds_read_b64_tr_b16 v[206:207], v246 offset:26112\n"
"s_waitcnt lgkmcnt(6)\n"
"v_mfma_f32_16x16x32_bf16 v[128:131], v[192:195], v[0:3], v[128:131]\n"
"s_add_u32 m0, s56, 34816\n"
"s_nop 0\n"
"buffer_load_dwordx4 v249, s[44:47], s64 offen nt lds\n"
"s_waitcnt lgkmcnt(4)\n"
"v_mfma_f32_16x16x32_bf16 v[128:131], v[196:199], v[4:7], v[128:131]\n"
"v_mfma_f32_16x16x32_bf16 v[144:147], v[192:195], v[8:11], v[144:147]\n"
"s_add_u32 m0, s57, 34816\n"
"s_nop 0\n"
"buffer_load_dwordx4 v249, s[44:47], s65 offen nt lds\n"
"v_mfma_f32_16x16x32_bf16 v[144:147], v[196:199], v[12:15], v[144:147]\n"
"v_mfma_f32_16x16x32_bf16 v[160:163], v[192:195], v[16:19], v[160:163]\n"
"s_add_u32 m0, s58, 34816\n"
"s_nop 0\n"
"buffer_load_dwordx4 v249, s[44:47], s66 offen nt lds\n"
"v_mfma_f32_16x16x32_bf16 v[160:163], v[196:199], v[20:23], v[160:163]\n"
"ds_read_b64_tr_b16 v[192:193], v245 offset:64\n"
"ds_read_b64_tr_b16 v[194:195], v245 offset:8768\n"
"ds_read_b64_tr_b16 v[196:197], v245 offset:17472\n"
"ds_read_b64_tr_b16 v[198:199], v245 offset:26176\n"
"s_waitcnt lgkmcnt(6)\n"
"v_mfma_f32_16x16x32_bf16 v[132:135], v[200:203], v[0:3], v[132:135]\n"
"s_add_u32 m0, s59, 34816\n"
"s_nop 0\n"
"buffer_load_dwordx4 v249, s[44:47], s67 offen nt lds\n"
"s_waitcnt lgkmcnt(4)\n"
"v_mfma_f32_16x16x32_bf16 v[132:135], v[204:207], v[4:7], v[132:135]\n"
"v_mfma_f32_16x16x32_bf16 v[148:151], v[200:203], v[8:11], v[148:151]\n"
"s_add_u32 m0, s60, 34816\n"
"s_nop 0\n"
"buffer_load_dwordx4 v249, s[44:47], s68 offen nt lds\n"
"v_mfma_f32_16x16x32_bf16 v[148:151], v[204:207], v[12:15], v[148:151]\n"
"v_mfma_f32_16x16x32_bf16 v[164:167], v[200:203], v[16:19], v[164:167]\n"
"s_add_u32 m0, s61, 34816\n"
"s_nop 0\n"
"buffer_load_dwordx4 v249, s[44:47], s69 offen nt lds\n"
"v_mfma_f32_16x16x32_bf16 v[164:167], v[204:207], v[20:23], v[164:167]\n"
"ds_read_b64_tr_b16 v[200:201], v246 offset:64\n"
"ds_read_b64_tr_b16 v[202:203], v246 offset:8768\n"
"ds_read_b64_tr_b16 v[204:205], v246 offset:17472\n"
"ds_read_b64_tr_b16 v[206:207], v246 offset:26176\n"
"s_waitcnt lgkmcnt(6)\n"
"v_mfma_f32_16x16x32_bf16 v[136:139], v[192:195], v[0:3], v[136:139]\n"
"ds_read_b128 v[208:211], v104 offset:36864\n"
"s_add_u32 m0, s62, 34816\n"
"s_nop 0\n"
"buffer_load_dwordx4 v249, s[44:47], s70 offen nt lds\n"
"s_waitcnt lgkmcnt(5)\n"
"v_mfma_f32_16x16x32_bf16 v[136:139], v[196:199], v[4:7], v[136:139]\n"
"ds_read_b128 v[212:215], v104 offset:37888\n"
"v_mfma_f32_16x16x32_bf16 v[152:155], v[192:195], v[8:11], v[152:155]\n"
"ds_read_b128 v[216:219], v104 offset:38912\n"
"s_add_u32 m0, s63, 34816\n"
"s_nop 0\n"
"buffer_load_dwordx4 v249, s[44:47], s71 offen nt lds\n"
"v_add_u32 v249, 32768, v249\n"
"v_mfma_f32_16x16x32_bf16 v[152:155], v[196:199], v[12:15], v[152:155]\n"
"ds_read_b128 v[220:223], v104 offset:39936\n"
"v_mfma_f32_16x16x32_bf16 v[168:171], v[192:195], v[16:19], v[168:171]\n"
"ds_read_b128 v[224:227], v104 offset:40960\n"
"v_mfma_f32_16x16x32_bf16 v[168:171], v[196:199], v[20:23], v[168:171]\n"
"ds_read_b128 v[228:231], v104 offset:41984\n"
"s_waitcnt lgkmcnt(8)\n"
"v_mfma_f32_16x16x32_bf16 v[140:143], v[200:203], v[0:3], v[140:143]\n"
"s_waitcnt lgkmcnt(6)\n"
"v_mfma_f32_16x16x32_bf16 v[140:143], v[204:207], v[4:7], v[140:143]\n"
"v_mfma_f32_16x16x32_bf16 v[156:159], v[200:203], v[8:11], v[156:159]\n"
"v_mfma_f32_16x16x32_bf16 v[156:159], v[204:207], v[12:15], v[156:159]\n"
"s_waitcnt lgkmcnt(0)\n"
"s_waitcnt vmcnt(8)\n"
"s_barrier\n"
"s_mov_b32 s84, 0x3db8aa3b\n"
"s_mov_b32 s85, 0x3db8aa3b\n"
"v_mfma_f32_16x16x32_bf16 v[172:175], v[200:203], v[16:19], v[172:175]\n"
"ds_read_b64_tr_b16 v[192:193], v102 offset:0\n"
"v_mfma_f32_16x16x32_bf16 v[172:175], v[204:207], v[20:23], v[172:175]\n"
"ds_read_b64_tr_b16 v[194:195], v102 offset:8704\n"
"ds_read_b64_tr_b16 v[196:197], v102 offset:17408\n"
"ds_read_b64_tr_b16 v[198:199], v102 offset:26112\n"
"ds_read_b64_tr_b16 v[200:201], v103 offset:0\n"
"ds_read_b64_tr_b16 v[202:203], v103 offset:8704\n"
"ds_read_b64_tr_b16 v[204:205], v103 offset:17408\n"
"ds_read_b64_tr_b16 v[206:207], v103 offset:26112\n"
"s_waitcnt lgkmcnt(6)\n"
"v_mfma_f32_16x16x32_bf16 v[128:131], v[192:195], v[208:211], v[128:131]\n"
"s_waitcnt lgkmcnt(4)\n"
"v_mfma_f32_16x16x32_bf16 v[128:131], v[196:199], v[212:215], v[128:131]\n"
"v_mfma_f32_16x16x32_bf16 v[144:147], v[192:195], v[216:219], v[144:147]\n"
"v_mfma_f32_16x16x32_bf16 v[144:147], v[196:199], v[220:223], v[144:147]\n"
"v_mfma_f32_16x16x32_bf16 v[160:163], v[192:195], v[224:227], v[160:163]\n"
"v_mfma_f32_16x16x32_bf16 v[160:163], v[196:199], v[228:231], v[160:163]\n"
"ds_read_b64_tr_b16 v[192:193], v102 offset:64\n"
"ds_read_b64_tr_b16 v[194:195], v102 offset:8768\n"
"ds_read_b64_tr_b16 v[196:197], v102 offset:17472\n"
"ds_read_b64_tr_b16 v[198:199], v102 offset:26176\n"
"s_waitcnt lgkmcnt(6)\n"
"v_mfma_f32_16x16x32_bf16 v[132:135], v[200:203], v[208:211], v[132:135]\n"
"s_waitcnt lgkmcnt(4)\n"
"v_mfma_f32_16x16x32_bf16 v[132:135], v[204:207], v[212:215], v[132:135]\n"
"v_pk_mul_f32 v[128:129], v[128:129], s[84:85]\n"
"v_pk_mul_f32 v[130:131], v[130:131], s[84:85]\n"
"v_cvt_pk_bf16_f32 v32, v128, v129\n"
"v_cvt_pk_bf16_f32 v33, v130, v131\n"
"ds_write_b64 v105, v[32:33] offset:32768\n"
"v_mfma_f32_16x16x32_bf16 v[148:151], v[200:203], v[216:219], v[148:151]\n"
"v_pk_mul_f32 v[144:145], v[144:145], s[84:85]\n"
"v_pk_mul_f32 v[146:147], v[146:147], s[84:85]\n"
"v_cvt_pk_bf16_f32 v34, v144, v145\n"
"v_cvt_pk_bf16_f32 v35, v146, v147\n"
"ds_write_b64 v105, v[34:35] offset:40960\n"
"v_mfma_f32_16x16x32_bf16 v[148:151], v[204:207], v[220:223], v[148:151]\n"
"v_pk_mul_f32 v[160:161], v[160:161], s[84:85]\n"
"v_pk_mul_f32 v[162:163], v[162:163], s[84:85]\n"
"v_cvt_pk_bf16_f32 v36, v160, v161\n"
"v_cvt_pk_bf16_f32 v37, v162, v163\n"
"ds_write_b64 v105, v[36:37] offset:49152\n"
"v_mfma_f32_16x16x32_bf16 v[164:167], v[200:203], v[224:227], v[164:167]\n"
"v_mfma_f32_16x16x32_bf16 v[164:167], v[204:207], v[228:231], v[164:167]\n"
"ds_read_b64_tr_b16 v[200:201], v103 offset:64\n"
"ds_read_b64_tr_b16 v[202:203], v103 offset:8768\n"
"ds_read_b64_tr_b16 v[204:205], v103 offset:17472\n"
"ds_read_b64_tr_b16 v[206:207], v103 offset:26176\n"
"s_waitcnt lgkmcnt(9)\n"
"v_mfma_f32_16x16x32_bf16 v[136:139], v[192:195], v[208:211], v[136:139]\n"
"s_waitcnt lgkmcnt(7)\n"
"v_mfma_f32_16x16x32_bf16 v[136:139], v[196:199], v[212:215], v[136:139]\n"
"v_pk_mul_f32 v[132:133], v[132:133], s[84:85]\n"
"v_pk_mul_f32 v[134:135], v[134:135], s[84:85]\n"
"v_cvt_pk_bf16_f32 v38, v132, v133\n"
"v_cvt_pk_bf16_f32 v39, v134, v135\n"
"ds_write_b64 v105, v[38:39] offset:33280\n"
"v_mfma_f32_16x16x32_bf16 v[152:155], v[192:195], v[216:219], v[152:155]\n"
"v_pk_mul_f32 v[148:149], v[148:149], s[84:85]\n"
"v_pk_mul_f32 v[150:151], v[150:151], s[84:85]\n"
"v_cvt_pk_bf16_f32 v40, v148, v149\n"
"v_cvt_pk_bf16_f32 v41, v150, v151\n"
"ds_write_b64 v105, v[40:41] offset:41472\n"
"v_mfma_f32_16x16x32_bf16 v[152:155], v[196:199], v[220:223], v[152:155]\n"
"v_pk_mul_f32 v[164:165], v[164:165], s[84:85]\n"
"v_pk_mul_f32 v[166:167], v[166:167], s[84:85]\n"
"v_cvt_pk_bf16_f32 v42, v164, v165\n"
"v_cvt_pk_bf16_f32 v43, v166, v167\n"
"ds_write_b64 v105, v[42:43] offset:49664\n"
"v_mfma_f32_16x16x32_bf16 v[168:171], v[192:195], v[224:227], v[168:171]\n"
"v_mfma_f32_16x16x32_bf16 v[168:171], v[196:199], v[228:231], v[168:171]\n"
"s_waitcnt lgkmcnt(5)\n"
"v_mfma_f32_16x16x32_bf16 v[140:143], v[200:203], v[208:211], v[140:143]\n"
"s_waitcnt lgkmcnt(3)\n"
"v_mfma_f32_16x16x32_bf16 v[140:143], v[204:207], v[212:215], v[140:143]\n"
"v_pk_mul_f32 v[136:137], v[136:137], s[84:85]\n"
"v_pk_mul_f32 v[138:139], v[138:139], s[84:85]\n"
"v_cvt_pk_bf16_f32 v44, v136, v137\n"
"v_cvt_pk_bf16_f32 v45, v138, v139\n"
"ds_write_b64 v105, v[44:45] offset:33792\n"
"v_mfma_f32_16x16x32_bf16 v[156:159], v[200:203], v[216:219], v[156:159]\n"
"v_pk_mul_f32 v[152:153], v[152:153], s[84:85]\n"
"v_pk_mul_f32 v[154:155], v[154:155], s[84:85]\n"
"v_cvt_pk_bf16_f32 v46, v152, v153\n"
"v_cvt_pk_bf16_f32 v47, v154, v155\n"
"ds_write_b64 v105, v[46:47] offset:41984\n"
"v_mfma_f32_16x16x32_bf16 v[156:159], v[204:207], v[220:223], v[156:159]\n"
"v_pk_mul_f32 v[168:169], v[168:169], s[84:85]\n"
"v_pk_mul_f32 v[170:171], v[170:171], s[84:85]\n"
"v_cvt_pk_bf16_f32 v48, v168, v169\n"
"v_cvt_pk_bf16_f32 v49, v170, v171\n"
"ds_write_b64 v105, v[48:49] offset:50176\n"
"v_mfma_f32_16x16x32_bf16 v[172:175], v[200:203], v[224:227], v[172:175]\n"
"v_mfma_f32_16x16x32_bf16 v[172:175], v[204:207], v[228:231], v[172:175]\n"
"v_pk_mul_f32 v[140:141], v[140:141], s[84:85]\n"
"v_pk_mul_f32 v[142:143], v[142:143], s[84:85]\n"
"v_cvt_pk_bf16_f32 v50, v140, v141\n"
"v_cvt_pk_bf16_f32 v51, v142, v143\n"
"ds_write_b64 v105, v[50:51] offset:34304\n"
"v_pk_mul_f32 v[156:157], v[156:157], s[84:85]\n"
"v_pk_mul_f32 v[158:159], v[158:159], s[84:85]\n"
"v_cvt_pk_bf16_f32 v52, v156, v157\n"
"v_cvt_pk_bf16_f32 v53, v158, v159\n"
"ds_write_b64 v105, v[52:53] offset:42496\n"
"v_pk_mul_f32 v[172:173], v[172:173], s[84:85]\n"
"v_pk_mul_f32 v[174:175], v[174:175], s[84:85]\n"
"v_cvt_pk_bf16_f32 v54, v172, v173\n"
"v_cvt_pk_bf16_f32 v55, v174, v175\n"
"ds_write_b64 v105, v[54:55] offset:50688\n"
"s_waitcnt lgkmcnt(0)\n"
"v_mov_b32 v250, v109\n"
"s_waitcnt lgkmcnt(0)\n"
"s_barrier\n"
"s_add_u32 m0, s56, 0\n"
"s_nop 0\n"
"buffer_load_dwordx4 v249, s[44:47], s64 offen nt lds\n"
"s_add_u32 m0, s57, 0\n"
"s_nop 0\n"
"buffer_load_dwordx4 v249, s[44:47], s65 offen nt lds\n"
"s_add_u32 m0, s58, 0\n"
"s_nop 0\n"
"buffer_load_dwordx4 v249, s[44:47], s66 offen nt lds\n"
"s_add_u32 m0, s59, 0\n"
"s_nop 0\n"
"buffer_load_dwordx4 v249, s[44:47], s67 offen nt lds\n"
"s_add_u32 m0, s60, 0\n"
"s_nop 0\n"
"buffer_load_dwordx4 v249, s[44:47], s68 offen nt lds\n"
"s_add_u32 m0, s61, 0\n"
"s_nop 0\n"
"buffer_load_dwordx4 v249, s[44:47], s69 offen nt lds\n"
"s_add_u32 m0, s62, 0\n"
"s_nop 0\n"
"buffer_load_dwordx4 v249, s[44:47], s70 offen nt lds\n"
"s_add_u32 m0, s63, 0\n"
"s_nop 0\n"
"buffer_load_dwordx4 v249, s[44:47], s71 offen nt lds\n"
"v_add_u32 v249, 32768, v249\n"
"ds_read_b128 v[0:3], v250 offset:32768\n"
"ds_read_b128 v[4:7], v250 offset:33792\n"
"ds_read_b128 v[8:11], v250 offset:34816\n"
"ds_read_b128 v[12:15], v250 offset:35840\n"
"ds_read_b128 v[16:19], v250 offset:36864\n"
"ds_read_b128 v[20:23], v250 offset:37888\n"
"ds_read_b128 v[24:27], v250 offset:38912\n"
"ds_read_b128 v[28:31], v250 offset:39936\n"
"ds_read_b128 v[32:35], v250 offset:40960\n"
"ds_read_b128 v[36:39], v250 offset:41984\n"
"ds_read_b128 v[40:43], v250 offset:43008\n"
"ds_read_b128 v[44:47], v250 offset:44032\n"
"ds_read_b128 v[48:51], v250 offset:45056\n"
"ds_read_b128 v[52:55], v250 offset:46080\n"
"ds_read_b128 v[56:59], v250 offset:47104\n"
"ds_read_b128 v[60:63], v250 offset:48128\n"
"ds_read_b128 v[64:67], v250 offset:49152\n"
"ds_read_b128 v[68:71], v250 offset:50176\n"
"ds_read_b128 v[72:75], v250 offset:51200\n"
"ds_read_b128 v[76:79], v250 offset:52224\n"
"ds_read_b128 v[80:83], v250 offset:53248\n"
"ds_read_b128 v[84:87], v250 offset:54272\n"
"ds_read_b128 v[88:91], v250 offset:55296\n"
"ds_read_b128 v[92:95], v250 offset:56320\n"
"v_mov_b64 v[128:129], 0\n"
"v_mov_b64 v[130:131], 0\n"
"v_mov_b64 v[132:133], 0\n"
"v_mov_b64 v[134:135], 0\n"
"v_mov_b64 v[136:137], 0\n"
"v_mov_b64 v[138:139], 0\n"
"v_mov_b64 v[140:141], 0\n"
"v_mov_b64 v[142:143], 0\n"
"v_mov_b64 v[144:145], 0\n"
"v_mov_b64 v[146:147], 0\n"
"v_mov_b64 v[148:149], 0\n"
"v_mov_b64 v[150:151], 0\n"
"v_mov_b64 v[152:153], 0\n"
"v_mov_b64 v[154:155], 0\n"
"v_mov_b64 v[156:157], 0\n"
"v_mov_b64 v[158:159], 0\n"
"v_mov_b64 v[160:161], 0\n"
"v_mov_b64 v[162:163], 0\n"
"v_mov_b64 v[164:165], 0\n"
"v_mov_b64 v[166:167], 0\n"
"v_mov_b64 v[168:169], 0\n"
"v_mov_b64 v[170:171], 0\n"
"v_mov_b64 v[172:173], 0\n"
"v_mov_b64 v[174:175], 0\n"
"v_mov_b32 v240, 0\n"
"v_mov_b32 v241, 0\n"
"v_mov_b32 v242, 0\n"
"v_mov_b64 v[200:201], 0\n"
"v_mov_b64 v[202:203], 0\n"
"v_mov_b64 v[204:205], 0\n"
"v_mov_b64 v[206:207], 0\n"
"v_mov_b64 v[224:225], 0\n"
"v_mov_b64 v[226:227], 0\n"
"v_mov_b64 v[228:229], 0\n"
"v_mov_b64 v[230:231], 0\n"
"v_mov_b64 v[232:233], 0\n"
"v_mov_b64 v[234:235], 0\n"
"v_mov_b64 v[236:237], 0\n"
"v_mov_b64 v[238:239], 0\n"
"s_bfe_u32 s84, %8, 0x40004\n"
"s_mul_i32 s84, s84, 0\n"
"s_xor_b32 s85, s84, 0\n"
"s_lshl_b32 s85, s85, 2\n"
"s_add_u32 s85, s85, s87\n"
"s_mul_i32 s36, s85, 1792\n"
"s_xor_b32 s85, s84, 1\n"
"s_lshl_b32 s85, s85, 2\n"
"s_add_u32 s85, s85, s87\n"
"s_mul_i32 s37, s85, 1792\n"
"s_xor_b32 s85, s84, 2\n"
"s_lshl_b32 s85, s85, 2\n"
"s_add_u32 s85, s85, s87\n"
"s_mul_i32 s38, s85, 1792\n"
"s_xor_b32 s85, s84, 3\n"
"s_lshl_b32 s85, s85, 2\n"
"s_add_u32 s85, s85, s87\n"
"s_mul_i32 s39, s85, 1792\n"
"s_xor_b32 s85, s84, 4\n"
"s_lshl_b32 s85, s85, 2\n"
"s_add_u32 s85, s85, s87\n"
"s_mul_i32 s40, s85, 1792\n"
"s_xor_b32 s85, s84, 5\n"
"s_lshl_b32 s85, s85, 2\n"
"s_add_u32 s85, s85, s87\n"
"s_mul_i32 s41, s85, 1792\n"
"s_xor_b32 s85, s84, 6\n"
"s_lshl_b32 s85, s85, 2\n"
"s_add_u32 s85, s85, s87\n"
"s_mul_i32 s42, s85, 1792\n"
"s_xor_b32 s85, s84, 7\n"
"s_lshl_b32 s85, s85, 2\n"
"s_add_u32 s85, s85, s87\n"
"s_mul_i32 s43, s85, 1792\n"
"s_waitcnt vmcnt(8) lgkmcnt(0)\n"
"s_barrier\n"
"s_mov_b32 s90, 0x3f803f80\n"
"s_cmp_lg_u32 s91, 0\n"
"s_cbranch_scc1 L_safeB\n"
"s_barrier\n"
"s_mov_b32 s83, 15\n"
"L_loopB:\n"
"s_setprio 1\n"
"v_mfma_f32_16x16x32_bf16 v[172:175], v[200:203], v[224:227], v[172:175]\n"
"ds_read_b128 v[208:211], v244 offset:34816\n"
"v_mfma_f32_16x16x32_bf16 v[172:175], v[204:207], v[228:231], v[172:175]\n"
"ds_read_b128 v[212:215], v244 offset:34880\n"
"ds_read_b128 v[216:219], v244 offset:34944\n"
"ds_read_b128 v[220:223], v244 offset:35008\n"
"ds_read_b128 v[224:227], v244 offset:35072\n"
"ds_read_b128 v[228:231], v244 offset:35136\n"
"ds_read_b128 v[232:235], v244 offset:35200\n"
"ds_read_b128 v[236:239], v244 offset:35264\n"
"s_waitcnt lgkmcnt(7)\n"
"v_mfma_f32_16x16x32_bf16 v[192:195], v[208:211], v[0:3], 0\n"
"s_waitcnt lgkmcnt(6)\n"
"v_mfma_f32_16x16x32_bf16 v[192:195], v[212:215], v[4:7], v[192:195]\n"
"s_waitcnt lgkmcnt(5)\n"
"v_mfma_f32_16x16x32_bf16 v[192:195], v[216:219], v[8:11], v[192:195]\n"
"s_waitcnt lgkmcnt(4)\n"
"v_mfma_f32_16x16x32_bf16 v[192:195], v[220:223], v[12:15], v[192:195]\n"
"v_mfma_f32_16x16x32_bf16 v[196:199], v[208:211], v[32:35], 0\n"
"v_mfma_f32_16x16x32_bf16 v[196:199], v[212:215], v[36:39], v[196:199]\n"
"v_mfma_f32_16x16x32_bf16 v[196:199], v[216:219], v[40:43], v[196:199]\n"
"v_mfma_f32_16x16x32_bf16 v[196:199], v[220:223], v[44:47], v[196:199]\n"
"s_waitcnt lgkmcnt(3)\n"
"v_mfma_f32_16x16x32_bf16 v[192:195], v[224:227], v[16:19], v[192:195]\n"
"s_waitcnt lgkmcnt(2)\n"
"v_mfma_f32_16x16x32_bf16 v[192:195], v[228:231], v[20:23], v[192:195]\n"
"s_waitcnt lgkmcnt(1)\n"
"v_mfma_f32_16x16x32_bf16 v[192:195], v[232:235], v[24:27], v[192:195]\n"
"s_waitcnt lgkmcnt(0)\n"
"v_mfma_f32_16x16x32_bf16 v[192:195], v[236:239], v[28:31], v[192:195]\n"
"s_nop 1\n"
"v_mfma_f32_16x16x32_bf16 v[196:199], v[224:227], v[48:51], v[196:199]\n"
"s_nop 1\n"
"v_mfma_f32_16x16x32_bf16 v[196:199], v[228:231], v[52:55], v[196:199]\n"
"s_nop 1\n"
"v_mfma_f32_16x16x32_bf16 v[196:199], v[232:235], v[56:59], v[196:199]\n"
"v_exp_f32 v192, v192\n"
"v_mfma_f32_16x16x32_bf16 v[196:199], v[236:239], v[60:63], v[196:199]\n"
"v_exp_f32 v193, v193\n"
"v_mfma_f32_16x16x32_bf16 v[200:203], v[208:211], v[64:67], 0\n"
"v_exp_f32 v194, v194\n"
"v_mfma_f32_16x16x32_bf16 v[200:203], v[212:215], v[68:71], v[200:203]\n"
"v_exp_f32 v195, v195\n"
"v_mfma_f32_16x16x32_bf16 v[200:203], v[216:219], v[72:75], v[200:203]\n"
"v_add_f32 v240, v240, v192\n"
"v_cvt_pk_bf16_f32 v250, v192, v193\n"
"v_mfma_f32_16x16x32_bf16 v[200:203], v[220:223], v[76:79], v[200:203]\n"
"v_add_f32 v240, v240, v193\n"
"v_cvt_pk_bf16_f32 v251, v194, v195\n"
"v_mfma_f32_16x16x32_bf16 v[200:203], v[224:227], v[80:83], v[200:203]\n"
"v_add_f32 v240, v240, v194\n"
"ds_write_b64 v247, v[250:251] offset:8192\n"
"v_mfma_f32_16x16x32_bf16 v[200:203], v[228:231], v[84:87], v[200:203]\n"
"v_add_f32 v240, v240, v195\n"
"v_exp_f32 v196, v196\n"
"v_mfma_f32_16x16x32_bf16 v[200:203], v[232:235], v[88:91], v[200:203]\n"
"v_exp_f32 v197, v197\n"
"v_mfma_f32_16x16x32_bf16 v[200:203], v[236:239], v[92:95], v[200:203]\n"
"v_exp_f32 v198, v198\n"
"v_exp_f32 v199, v199\n"
"v_add_f32 v241, v241, v196\n"
"v_cvt_pk_bf16_f32 v252, v196, v197\n"
"v_add_f32 v241, v241, v197\n"
"v_cvt_pk_bf16_f32 v253, v198, v199\n"
"v_add_f32 v241, v241, v198\n"
"ds_write_b64 v247, v[252:253] offset:10240\n"
"v_add_f32 v241, v241, v199\n"
"v_exp_f32 v200, v200\n"
"v_exp_f32 v201, v201\n"
"v_exp_f32 v202, v202\n"
"v_exp_f32 v203, v203\n"
"v_add_f32 v242, v242, v200\n"
"v_cvt_pk_bf16_f32 v254, v200, v201\n"
"v_add_f32 v242, v242, v201\n"
"v_cvt_pk_bf16_f32 v255, v202, v203\n"
"v_add_f32 v242, v242, v202\n"
"ds_write_b64 v247, v[254:255] offset:12288\n"
"v_add_f32 v242, v242, v203\n"
"s_add_u32 m0, s62, 104448\n"
"s_nop 0\n"
"buffer_load_dwordx4 v96, s[52:55], s70 offen nt lds\n"
"s_add_u32 m0, s63, 104448\n"
"s_nop 0\n"
"buffer_load_dwordx4 v96, s[52:55], s71 offen nt lds\n"
"v_add_u32 v96, 32768, v96\n"
"s_setprio 0\n"
"s_waitcnt vmcnt(2) lgkmcnt(0)\n"
"s_barrier\n"
"ds_read_b128 v[208:211], v248 offset:8192\n"
"ds_read_b64_tr_b16 v[192:193], v245 offset:0\n"
"ds_read_b64_tr_b16 v[194:195], v245 offset:8704\n"
"ds_read_b128 v[212:215], v248 offset:9216\n"
"ds_read_b64_tr_b16 v[196:197], v245 offset:17408\n"
"ds_read_b64_tr_b16 v[198:199], v245 offset:26112\n"
"ds_read_b128 v[216:219], v248 offset:10240\n"
"ds_read_b128 v[220:223], v248 offset:11264\n"
"ds_read_b128 v[224:227], v248 offset:12288\n"
"ds_read_b128 v[228:231], v248 offset:13312\n"
"s_add_u32 m0, s56, 34816\n"
"ds_read_b64_tr_b16 v[200:201], v246 offset:0\n"
"buffer_load_dwordx4 v249, s[44:47], s64 offen nt lds\n"
"s_add_u32 m0, s57, 34816\n"
"ds_read_b64_tr_b16 v[202:203], v246 offset:8704\n"
"buffer_load_dwordx4 v249, s[44:47], s65 offen nt lds\n"
"s_add_u32 m0, s58, 34816\n"
"ds_read_b64_tr_b16 v[204:205], v246 offset:17408\n"
"buffer_load_dwordx4 v249, s[44:47], s66 offen nt lds\n"
"s_add_u32 m0, s59, 34816\n"
"ds_read_b64_tr_b16 v[206:207], v246 offset:26112\n"
"buffer_load_dwordx4 v249, s[44:47], s67 offen nt lds\n"
"s_add_u32 m0, s60, 34816\n"
"s_nop 0\n"
"buffer_load_dwordx4 v249, s[44:47], s68 offen nt lds\n"
"s_add_u32 m0, s61, 34816\n"
"s_nop 0\n"
"buffer_load_dwordx4 v249, s[44:47], s69 offen nt lds\n"
"s_add_u32 m0, s62, 34816\n"
"s_nop 0\n"
"buffer_load_dwordx4 v249, s[44:47], s70 offen nt lds\n"
"s_add_u32 m0, s63, 34816\n"
"s_nop 0\n"
"buffer_load_dwordx4 v249, s[44:47], s71 offen nt lds\n"
"v_add_u32 v249, 32768, v249\n"
"s_waitcnt lgkmcnt(11)\n"
"v_mfma_f32_16x16x32_bf16 v[128:131], v[192:195], v[208:211], v[128:131]\n"
"s_waitcnt lgkmcnt(8)\n"
"v_mfma_f32_16x16x32_bf16 v[128:131], v[196:199], v[212:215], v[128:131]\n"
"s_waitcnt lgkmcnt(7)\n"
"v_mfma_f32_16x16x32_bf16 v[144:147], v[192:195], v[216:219], v[144:147]\n"
"s_waitcnt lgkmcnt(6)\n"
"v_mfma_f32_16x16x32_bf16 v[144:147], v[196:199], v[220:223], v[144:147]\n"
"s_waitcnt lgkmcnt(5)\n"
"v_mfma_f32_16x16x32_bf16 v[160:163], v[192:195], v[224:227], v[160:163]\n"
"s_waitcnt lgkmcnt(4)\n"
"v_mfma_f32_16x16x32_bf16 v[160:163], v[196:199], v[228:231], v[160:163]\n"
"ds_read_b64_tr_b16 v[192:193], v245 offset:64\n"
"ds_read_b64_tr_b16 v[194:195], v245 offset:8768\n"
"ds_read_b64_tr_b16 v[196:197], v245 offset:17472\n"
"ds_read_b64_tr_b16 v[198:199], v245 offset:26176\n"
"s_waitcnt lgkmcnt(6)\n"
"v_mfma_f32_16x16x32_bf16 v[132:135], v[200:203], v[208:211], v[132:135]\n"
"s_waitcnt lgkmcnt(4)\n"
"v_mfma_f32_16x16x32_bf16 v[132:135], v[204:207], v[212:215], v[132:135]\n"
"v_mfma_f32_16x16x32_bf16 v[148:151], v[200:203], v[216:219], v[148:151]\n"
"v_mfma_f32_16x16x32_bf16 v[148:151], v[204:207], v[220:223], v[148:151]\n"
"v_mfma_f32_16x16x32_bf16 v[164:167], v[200:203], v[224:227], v[164:167]\n"
"v_mfma_f32_16x16x32_bf16 v[164:167], v[204:207], v[228:231], v[164:167]\n"
"ds_read_b64_tr_b16 v[200:201], v246 offset:64\n"
"ds_read_b64_tr_b16 v[202:203], v246 offset:8768\n"
"ds_read_b64_tr_b16 v[204:205], v246 offset:17472\n"
"ds_read_b64_tr_b16 v[206:207], v246 offset:26176\n"
"s_waitcnt lgkmcnt(6)\n"
"v_mfma_f32_16x16x32_bf16 v[136:139], v[192:195], v[208:211], v[136:139]\n"
"s_waitcnt lgkmcnt(4)\n"
"v_mfma_f32_16x16x32_bf16 v[136:139], v[196:199], v[212:215], v[136:139]\n"
"v_mfma_f32_16x16x32_bf16 v[152:155], v[192:195], v[216:219], v[152:155]\n"
"v_mfma_f32_16x16x32_bf16 v[152:155], v[196:199], v[220:223], v[152:155]\n"
"v_mfma_f32_16x16x32_bf16 v[168:171], v[192:195], v[224:227], v[168:171]\n"
"v_mfma_f32_16x16x32_bf16 v[168:171], v[196:199], v[228:231], v[168:171]\n"
"s_waitcnt lgkmcnt(2)\n"
"v_mfma_f32_16x16x32_bf16 v[140:143], v[200:203], v[208:211], v[140:143]\n"
"s_waitcnt lgkmcnt(0)\n"
"v_mfma_f32_16x16x32_bf16 v[140:143], v[204:207], v[212:215], v[140:143]\n"
"v_mfma_f32_16x16x32_bf16 v[156:159], v[200:203], v[216:219], v[156:159]\n"
"v_mfma_f32_16x16x32_bf16 v[156:159], v[204:207], v[220:223], v[156:159]\n"
"s_waitcnt lgkmcnt(0)\n"
"s_waitcnt vmcnt(8)\n"
"s_barrier\n"
"s_setprio 1\n"
"v_mfma_f32_16x16x32_bf16 v[172:175], v[200:203], v[224:227], v[172:175]\n"
"ds_read_b128 v[208:211], v244 offset:0\n"
"v_mfma_f32_16x16x32_bf16 v[172:175], v[204:207], v[228:231], v[172:175]\n"
"ds_read_b128 v[212:215], v244 offset:64\n"
"ds_read_b128 v[216:219], v244 offset:128\n"
"ds_read_b128 v[220:223], v244 offset:192\n"
"ds_read_b128 v[224:227], v244 offset:256\n"
"ds_read_b128 v[228:231], v244 offset:320\n"
"ds_read_b128 v[232:235], v244 offset:384\n"
"ds_read_b128 v[236:239], v244 offset:448\n"
"s_waitcnt lgkmcnt(7)\n"
"v_mfma_f32_16x16x32_bf16 v[192:195], v[208:211], v[0:3], 0\n"
"s_waitcnt lgkmcnt(6)\n"
"v_mfma_f32_16x16x32_bf16 v[192:195], v[212:215], v[4:7], v[192:195]\n"
"s_waitcnt lgkmcnt(5)\n"
"v_mfma_f32_16x16x32_bf16 v[192:195], v[216:219], v[8:11], v[192:195]\n"
"s_waitcnt lgkmcnt(4)\n"
"v_mfma_f32_16x16x32_bf16 v[192:195], v[220:223], v[12:15], v[192:195]\n"
"v_mfma_f32_16x16x32_bf16 v[196:199], v[208:211], v[32:35], 0\n"
"v_mfma_f32_16x16x32_bf16 v[196:199], v[212:215], v[36:39], v[196:199]\n"
"v_mfma_f32_16x16x32_bf16 v[196:199], v[216:219], v[40:43], v[196:199]\n"
"v_mfma_f32_16x16x32_bf16 v[196:199], v[220:223], v[44:47], v[196:199]\n"
"s_waitcnt lgkmcnt(3)\n"
"v_mfma_f32_16x16x32_bf16 v[192:195], v[224:227], v[16:19], v[192:195]\n"
"s_waitcnt lgkmcnt(2)\n"
"v_mfma_f32_16x16x32_bf16 v[192:195], v[228:231], v[20:23], v[192:195]\n"
"s_waitcnt lgkmcnt(1)\n"
"v_mfma_f32_16x16x32_bf16 v[192:195], v[232:235], v[24:27], v[192:195]\n"
"s_waitcnt lgkmcnt(0)\n"
"v_mfma_f32_16x16x32_bf16 v[192:195], v[236:239], v[28:31], v[192:195]\n"
"s_nop 1\n"
"v_mfma_f32_16x16x32_bf16 v[196:199], v[224:227], v[48:51], v[196:199]\n"
"s_nop 1\n"
"v_mfma_f32_16x16x32_bf16 v[196:199], v[228:231], v[52:55], v[196:199]\n"
"s_nop 1\n"
"v_mfma_f32_16x16x32_bf16 v[196:199], v[232:235], v[56:59], v[196:199]\n"
"v_exp_f32 v192, v192\n"
"v_mfma_f32_16x16x32_bf16 v[196:199], v[236:239], v[60:63], v[196:199]\n"
"v_exp_f32 v193, v193\n"
"v_mfma_f32_16x16x32_bf16 v[200:203], v[208:211], v[64:67], 0\n"
"v_exp_f32 v194, v194\n"
"v_mfma_f32_16x16x32_bf16 v[200:203], v[212:215], v[68:71], v[200:203]\n"
"v_exp_f32 v195, v195\n"
"v_mfma_f32_16x16x32_bf16 v[200:203], v[216:219], v[72:75], v[200:203]\n"
"v_add_f32 v240, v240, v192\n"
"v_cvt_pk_bf16_f32 v250, v192, v193\n"
"v_mfma_f32_16x16x32_bf16 v[200:203], v[220:223], v[76:79], v[200:203]\n"
"v_add_f32 v240, v240, v193\n"
"v_cvt_pk_bf16_f32 v251, v194, v195\n"
"v_mfma_f32_16x16x32_bf16 v[200:203], v[224:227], v[80:83], v[200:203]\n"
"v_add_f32 v240, v240, v194\n"
"ds_write_b64 v247, v[250:251] offset:8192\n"
"v_mfma_f32_16x16x32_bf16 v[200:203], v[228:231], v[84:87], v[200:203]\n"
"v_add_f32 v240, v240, v195\n"
"v_exp_f32 v196, v196\n"
"v_mfma_f32_16x16x32_bf16 v[200:203], v[232:235], v[88:91], v[200:203]\n"
"v_exp_f32 v197, v197\n"
"v_mfma_f32_16x16x32_bf16 v[200:203], v[236:239], v[92:95], v[200:203]\n"
"v_exp_f32 v198, v198\n"
"v_exp_f32 v199, v199\n"
"v_add_f32 v241, v241, v196\n"
"v_cvt_pk_bf16_f32 v252, v196, v197\n"
"v_add_f32 v241, v241, v197\n"
"v_cvt_pk_bf16_f32 v253, v198, v199\n"
"v_add_f32 v241, v241, v198\n"
"ds_write_b64 v247, v[252:253] offset:10240\n"
"v_add_f32 v241, v241, v199\n"
"v_exp_f32 v200, v200\n"
"v_exp_f32 v201, v201\n"
"v_exp_f32 v202, v202\n"
"v_exp_f32 v203, v203\n"
"v_add_f32 v242, v242, v200\n"
"v_cvt_pk_bf16_f32 v254, v200, v201\n"
"v_add_f32 v242, v242, v201\n"
"v_cvt_pk_bf16_f32 v255, v202, v203\n"
"v_add_f32 v242, v242, v202\n"
"ds_write_b64 v247, v[254:255] offset:12288\n"
"v_add_f32 v242, v242, v203\n"
"s_add_u32 m0, s62, 69632\n"
"s_nop 0\n"
"buffer_load_dwordx4 v96, s[52:55], s70 offen nt lds\n"
"s_add_u32 m0, s63, 69632\n"
"s_nop 0\n"
"buffer_load_dwordx4 v96, s[52:55], s71 offen nt lds\n"
"v_add_u32 v96, 32768, v96\n"
"s_setprio 0\n"
"s_waitcnt vmcnt(2) lgkmcnt(0)\n"
"s_barrier\n"
"ds_read_b128 v[208:211], v248 offset:8192\n"
"ds_read_b64_tr_b16 v[192:193], v245 offset:34816\n"
"ds_read_b64_tr_b16 v[194:195], v245 offset:43520\n"
"ds_read_b128 v[212:215], v248 offset:9216\n"
"ds_read_b64_tr_b16 v[196:197], v245 offset:52224\n"
"ds_read_b64_tr_b16 v[198:199], v245 offset:60928\n"
"ds_read_b128 v[216:219], v248 offset:10240\n"
"ds_read_b128 v[220:223], v248 offset:11264\n"
"ds_read_b128 v[224:227], v248 offset:12288\n"
"ds_read_b128 v[228:231], v248 offset:13312\n"
"s_add_u32 m0, s56, 0\n"
"ds_read_b64_tr_b16 v[200:201], v246 offset:34816\n"
"buffer_load_dwordx4 v249, s[44:47], s64 offen nt lds\n"
"s_add_u32 m0, s57, 0\n"
"ds_read_b64_tr_b16 v[202:203], v246 offset:43520\n"
"buffer_load_dwordx4 v249, s[44:47], s65 offen nt lds\n"
"s_add_u32 m0, s58, 0\n"
"ds_read_b64_tr_b16 v[204:205], v246 offset:52224\n"
"buffer_load_dwordx4 v249, s[44:47], s66 offen nt lds\n"
"s_add_u32 m0, s59, 0\n"
"ds_read_b64_tr_b16 v[206:207], v246 offset:60928\n"
"buffer_load_dwordx4 v249, s[44:47], s67 offen nt lds\n"
"s_add_u32 m0, s60, 0\n"
"s_nop 0\n"
"buffer_load_dwordx4 v249, s[44:47], s68 offen nt lds\n"
"s_add_u32 m0, s61, 0\n"
"s_nop 0\n"
"buffer_load_dwordx4 v249, s[44:47], s69 offen nt lds\n"
"s_add_u32 m0, s62, 0\n"
"s_nop 0\n"
"buffer_load_dwordx4 v249, s[44:47], s70 offen nt lds\n"
"s_add_u32 m0, s63, 0\n"
"s_nop 0\n"
"buffer_load_dwordx4 v249, s[44:47], s71 offen nt lds\n"
"v_add_u32 v249, 32768, v249\n"
"s_waitcnt lgkmcnt(11)\n"
"v_mfma_f32_16x16x32_bf16 v[128:131], v[192:195], v[208:211], v[128:131]\n"
"s_waitcnt lgkmcnt(8)\n"
"v_mfma_f32_16x16x32_bf16 v[128:131], v[196:199], v[212:215], v[128:131]\n"
"s_waitcnt lgkmcnt(7)\n"
"v_mfma_f32_16x16x32_bf16 v[144:147], v[192:195], v[216:219], v[144:147]\n"
"s_waitcnt lgkmcnt(6)\n"
"v_mfma_f32_16x16x32_bf16 v[144:147], v[196:199], v[220:223], v[144:147]\n"
"s_waitcnt lgkmcnt(5)\n"
"v_mfma_f32_16x16x32_bf16 v[160:163], v[192:195], v[224:227], v[160:163]\n"
"s_waitcnt lgkmcnt(4)\n"
"v_mfma_f32_16x16x32_bf16 v[160:163], v[196:199], v[228:231], v[160:163]\n"
"ds_read_b64_tr_b16 v[192:193], v245 offset:34880\n"
"ds_read_b64_tr_b16 v[194:195], v245 offset:43584\n"
"ds_read_b64_tr_b16 v[196:197], v245 offset:52288\n"
"ds_read_b64_tr_b16 v[198:199], v245 offset:60992\n"
"s_waitcnt lgkmcnt(6)\n"
"v_mfma_f32_16x16x32_bf16 v[132:135], v[200:203], v[208:211], v[132:135]\n"
"s_waitcnt lgkmcnt(4)\n"
"v_mfma_f32_16x16x32_bf16 v[132:135], v[204:207], v[212:215], v[132:135]\n"
"v_mfma_f32_16x16x32_bf16 v[148:151], v[200:203], v[216:219], v[148:151]\n"
"v_mfma_f32_16x16x32_bf16 v[148:151], v[204:207], v[220:223], v[148:151]\n"
"v_mfma_f32_16x16x32_bf16 v[164:167], v[200:203], v[224:227], v[164:167]\n"
"v_mfma_f32_16x16x32_bf16 v[164:167], v[204:207], v[228:231], v[164:167]\n"
"ds_read_b64_tr_b16 v[200:201], v246 offset:34880\n"
"ds_read_b64_tr_b16 v[202:203], v246 offset:43584\n"
"ds_read_b64_tr_b16 v[204:205], v246 offset:52288\n"
"ds_read_b64_tr_b16 v[206:207], v246 offset:60992\n"
"s_waitcnt lgkmcnt(6)\n"
"v_mfma_f32_16x16x32_bf16 v[136:139], v[192:195], v[208:211], v[136:139]\n"
"s_waitcnt lgkmcnt(4)\n"
"v_mfma_f32_16x16x32_bf16 v[136:139], v[196:199], v[212:215], v[136:139]\n"
"v_mfma_f32_16x16x32_bf16 v[152:155], v[192:195], v[216:219], v[152:155]\n"
"v_mfma_f32_16x16x32_bf16 v[152:155], v[196:199], v[220:223], v[152:155]\n"
"v_mfma_f32_16x16x32_bf16 v[168:171], v[192:195], v[224:227], v[168:171]\n"
"v_mfma_f32_16x16x32_bf16 v[168:171], v[196:199], v[228:231], v[168:171]\n"
"s_waitcnt lgkmcnt(2)\n"
"v_mfma_f32_16x16x32_bf16 v[140:143], v[200:203], v[208:211], v[140:143]\n"
"s_waitcnt lgkmcnt(0)\n"
"v_mfma_f32_16x16x32_bf16 v[140:143], v[204:207], v[212:215], v[140:143]\n"
"v_mfma_f32_16x16x32_bf16 v[156:159], v[200:203], v[216:219], v[156:159]\n"
"v_mfma_f32_16x16x32_bf16 v[156:159], v[204:207], v[220:223], v[156:159]\n"
"s_waitcnt lgkmcnt(0)\n"
"s_waitcnt vmcnt(8)\n"
"s_barrier\n"
"s_sub_u32 s83, s83, 1\n"
"s_cmp_lg_u32 s83, 0\n"
"s_cbranch_scc1 L_loopB\n"
"s_setprio 1\n"
"v_mfma_f32_16x16x32_bf16 v[172:175], v[200:203], v[224:227], v[172:175]\n"
"ds_read_b128 v[208:211], v244 offset:34816\n"
"v_mfma_f32_16x16x32_bf16 v[172:175], v[204:207], v[228:231], v[172:175]\n"
"ds_read_b128 v[212:215], v244 offset:34880\n"
"ds_read_b128 v[216:219], v244 offset:34944\n"
"ds_read_b128 v[220:223], v244 offset:35008\n"
"ds_read_b128 v[224:227], v244 offset:35072\n"
"ds_read_b128 v[228:231], v244 offset:35136\n"
"ds_read_b128 v[232:235], v244 offset:35200\n"
"ds_read_b128 v[236:239], v244 offset:35264\n"
"s_waitcnt lgkmcnt(7)\n"
"v_mfma_f32_16x16x32_bf16 v[192:195], v[208:211], v[0:3], 0\n"
"s_waitcnt lgkmcnt(6)\n"
"v_mfma_f32_16x16x32_bf16 v[192:195], v[212:215], v[4:7], v[192:195]\n"
"s_waitcnt lgkmcnt(5)\n"
"v_mfma_f32_16x16x32_bf16 v[192:195], v[216:219], v[8:11], v[192:195]\n"
"s_waitcnt lgkmcnt(4)\n"
"v_mfma_f32_16x16x32_bf16 v[192:195], v[220:223], v[12:15], v[192:195]\n"
"v_mfma_f32_16x16x32_bf16 v[196:199], v[208:211], v[32:35], 0\n"
"v_mfma_f32_16x16x32_bf16 v[196:199], v[212:215], v[36:39], v[196:199]\n"
"v_mfma_f32_16x16x32_bf16 v[196:199], v[216:219], v[40:43], v[196:199]\n"
"v_mfma_f32_16x16x32_bf16 v[196:199], v[220:223], v[44:47], v[196:199]\n"
"s_waitcnt lgkmcnt(3)\n"
"v_mfma_f32_16x16x32_bf16 v[192:195], v[224:227], v[16:19], v[192:195]\n"
"s_waitcnt lgkmcnt(2)\n"
"v_mfma_f32_16x16x32_bf16 v[192:195], v[228:231], v[20:23], v[192:195]\n"
"s_waitcnt lgkmcnt(1)\n"
"v_mfma_f32_16x16x32_bf16 v[192:195], v[232:235], v[24:27], v[192:195]\n"
"s_waitcnt lgkmcnt(0)\n"
"v_mfma_f32_16x16x32_bf16 v[192:195], v[236:239], v[28:31], v[192:195]\n"
"s_nop 1\n"
"v_mfma_f32_16x16x32_bf16 v[196:199], v[224:227], v[48:51], v[196:199]\n"
"s_nop 1\n"
"v_mfma_f32_16x16x32_bf16 v[196:199], v[228:231], v[52:55], v[196:199]\n"
"s_nop 1\n"
"v_mfma_f32_16x16x32_bf16 v[196:199], v[232:235], v[56:59], v[196:199]\n"
"v_exp_f32 v192, v192\n"
"v_mfma_f32_16x16x32_bf16 v[196:199], v[236:239], v[60:63], v[196:199]\n"
"v_exp_f32 v193, v193\n"
"v_mfma_f32_16x16x32_bf16 v[200:203], v[208:211], v[64:67], 0\n"
"v_exp_f32 v194, v194\n"
"v_mfma_f32_16x16x32_bf16 v[200:203], v[212:215], v[68:71], v[200:203]\n"
"v_exp_f32 v195, v195\n"
"v_mfma_f32_16x16x32_bf16 v[200:203], v[216:219], v[72:75], v[200:203]\n"
"v_add_f32 v240, v240, v192\n"
"v_cvt_pk_bf16_f32 v250, v192, v193\n"
"v_mfma_f32_16x16x32_bf16 v[200:203], v[220:223], v[76:79], v[200:203]\n"
"v_add_f32 v240, v240, v193\n"
"v_cvt_pk_bf16_f32 v251, v194, v195\n"
"v_mfma_f32_16x16x32_bf16 v[200:203], v[224:227], v[80:83], v[200:203]\n"
"v_add_f32 v240, v240, v194\n"
"ds_write_b64 v247, v[250:251] offset:8192\n"
"v_mfma_f32_16x16x32_bf16 v[200:203], v[228:231], v[84:87], v[200:203]\n"
"v_add_f32 v240, v240, v195\n"
"v_exp_f32 v196, v196\n"
"v_mfma_f32_16x16x32_bf16 v[200:203], v[232:235], v[88:91], v[200:203]\n"
"v_exp_f32 v197, v197\n"
"v_mfma_f32_16x16x32_bf16 v[200:203], v[236:239], v[92:95], v[200:203]\n"
"v_exp_f32 v198, v198\n"
"v_exp_f32 v199, v199\n"
"v_add_f32 v241, v241, v196\n"
"v_cvt_pk_bf16_f32 v252, v196, v197\n"
"v_add_f32 v241, v241, v197\n"
"v_cvt_pk_bf16_f32 v253, v198, v199\n"
"v_add_f32 v241, v241, v198\n"
"ds_write_b64 v247, v[252:253] offset:10240\n"
"v_add_f32 v241, v241, v199\n"
"v_exp_f32 v200, v200\n"
"v_exp_f32 v201, v201\n"
"v_exp_f32 v202, v202\n"
"v_exp_f32 v203, v203\n"
"v_add_f32 v242, v242, v200\n"
"v_cvt_pk_bf16_f32 v254, v200, v201\n"
"v_add_f32 v242, v242, v201\n"
"v_cvt_pk_bf16_f32 v255, v202, v203\n"
"v_add_f32 v242, v242, v202\n"
"ds_write_b64 v247, v[254:255] offset:12288\n"
"v_add_f32 v242, v242, v203\n"
"s_add_u32 m0, s62, 104448\n"
"s_nop 0\n"
"buffer_load_dwordx4 v96, s[52:55], s70 offen nt lds\n"
"s_add_u32 m0, s63, 104448\n"
"s_nop 0\n"
"buffer_load_dwordx4 v96, s[52:55], s71 offen nt lds\n"
"v_add_u32 v96, 32768, v96\n"
"s_setprio 0\n"
"s_waitcnt vmcnt(2) lgkmcnt(0)\n"
"s_barrier\n"
"ds_read_b128 v[208:211], v248 offset:8192\n"
"ds_read_b64_tr_b16 v[192:193], v245 offset:0\n"
"ds_read_b64_tr_b16 v[194:195], v245 offset:8704\n"
"ds_read_b128 v[212:215], v248 offset:9216\n"
"ds_read_b64_tr_b16 v[196:197], v245 offset:17408\n"
"ds_read_b64_tr_b16 v[198:199], v245 offset:26112\n"
"ds_read_b128 v[216:219], v248 offset:10240\n"
"ds_read_b128 v[220:223], v248 offset:11264\n"
"ds_read_b128 v[224:227], v248 offset:12288\n"
"ds_read_b128 v[228:231], v248 offset:13312\n"
"v_mbcnt_lo_u32_b32 v100, -1, 0\n"
"v_mbcnt_hi_u32_b32 v100, -1, v100\n"
"v_lshrrev_b32 v98, 5, v100\n"
"v_and_b32 v100, 31, v100\n"
"v_lshlrev_b32 v99, 1, v98\n"
"v_xor_b32 v100, v100, v99\n"
"v_mul_u32_u24 v98, 896, v98\n"
"v_lshrrev_b32 v99, 3, v100\n"
"v_mul_u32_u24 v99, 224, v99\n"
"v_add_u32 v98, v98, v99\n"
"v_and_b32 v100, 7, v100\n"
"v_lshl_add_u32 v100, v100, 4, v98\n"
"s_add_u32 m0, s56, 34816\n"
"ds_read_b64_tr_b16 v[200:201], v246 offset:0\n"
"buffer_load_dwordx4 v100, s[48:51], s36 offen lds\n"
"s_add_u32 m0, s57, 34816\n"
"ds_read_b64_tr_b16 v[202:203], v246 offset:8704\n"
"buffer_load_dwordx4 v100, s[48:51], s37 offen lds\n"
"s_add_u32 m0, s58, 34816\n"
"ds_read_b64_tr_b16 v[204:205], v246 offset:17408\n"
"buffer_load_dwordx4 v100, s[48:51], s38 offen lds\n"
"s_add_u32 m0, s59, 34816\n"
"ds_read_b64_tr_b16 v[206:207], v246 offset:26112\n"
"buffer_load_dwordx4 v100, s[48:51], s39 offen lds\n"
"s_add_u32 m0, s60, 34816\n"
"s_nop 0\n"
"buffer_load_dwordx4 v100, s[48:51], s40 offen lds\n"
"s_add_u32 m0, s61, 34816\n"
"s_nop 0\n"
"buffer_load_dwordx4 v100, s[48:51], s41 offen lds\n"
"s_add_u32 m0, s62, 34816\n"
"s_nop 0\n"
"buffer_load_dwordx4 v100, s[48:51], s42 offen lds\n"
"s_add_u32 m0, s63, 34816\n"
"s_nop 0\n"
"buffer_load_dwordx4 v100, s[48:51], s43 offen lds\n"
"s_waitcnt lgkmcnt(11)\n"
"v_mfma_f32_16x16x32_bf16 v[128:131], v[192:195], v[208:211], v[128:131]\n"
"s_waitcnt lgkmcnt(8)\n"
"v_mfma_f32_16x16x32_bf16 v[128:131], v[196:199], v[212:215], v[128:131]\n"
"s_waitcnt lgkmcnt(7)\n"
"v_mfma_f32_16x16x32_bf16 v[144:147], v[192:195], v[216:219], v[144:147]\n"
"s_waitcnt lgkmcnt(6)\n"
"v_mfma_f32_16x16x32_bf16 v[144:147], v[196:199], v[220:223], v[144:147]\n"
"s_waitcnt lgkmcnt(5)\n"
"v_mfma_f32_16x16x32_bf16 v[160:163], v[192:195], v[224:227], v[160:163]\n"
"s_waitcnt lgkmcnt(4)\n"
"v_mfma_f32_16x16x32_bf16 v[160:163], v[196:199], v[228:231], v[160:163]\n"
"ds_read_b64_tr_b16 v[192:193], v245 offset:64\n"
"ds_read_b64_tr_b16 v[194:195], v245 offset:8768\n"
"ds_read_b64_tr_b16 v[196:197], v245 offset:17472\n"
"ds_read_b64_tr_b16 v[198:199], v245 offset:26176\n"
"s_waitcnt lgkmcnt(6)\n"
"v_mfma_f32_16x16x32_bf16 v[132:135], v[200:203], v[208:211], v[132:135]\n"
"s_waitcnt lgkmcnt(4)\n"
"v_mfma_f32_16x16x32_bf16 v[132:135], v[204:207], v[212:215], v[132:135]\n"
"v_mfma_f32_16x16x32_bf16 v[148:151], v[200:203], v[216:219], v[148:151]\n"
"v_mfma_f32_16x16x32_bf16 v[148:151], v[204:207], v[220:223], v[148:151]\n"
"v_mfma_f32_16x16x32_bf16 v[164:167], v[200:203], v[224:227], v[164:167]\n"
"v_mfma_f32_16x16x32_bf16 v[164:167], v[204:207], v[228:231], v[164:167]\n"
"ds_read_b64_tr_b16 v[200:201], v246 offset:64\n"
"ds_read_b64_tr_b16 v[202:203], v246 offset:8768\n"
"ds_read_b64_tr_b16 v[204:205], v246 offset:17472\n"
"ds_read_b64_tr_b16 v[206:207], v246 offset:26176\n"
"s_waitcnt lgkmcnt(6)\n"
"v_mfma_f32_16x16x32_bf16 v[136:139], v[192:195], v[208:211], v[136:139]\n"
"s_waitcnt lgkmcnt(4)\n"
"v_mfma_f32_16x16x32_bf16 v[136:139], v[196:199], v[212:215], v[136:139]\n"
"v_mfma_f32_16x16x32_bf16 v[152:155], v[192:195], v[216:219], v[152:155]\n"
"v_mfma_f32_16x16x32_bf16 v[152:155], v[196:199], v[220:223], v[152:155]\n"
"v_mfma_f32_16x16x32_bf16 v[168:171], v[192:195], v[224:227], v[168:171]\n"
"v_mfma_f32_16x16x32_bf16 v[168:171], v[196:199], v[228:231], v[168:171]\n"
"s_waitcnt lgkmcnt(2)\n"
"v_mfma_f32_16x16x32_bf16 v[140:143], v[200:203], v[208:211], v[140:143]\n"
"s_waitcnt lgkmcnt(0)\n"
"v_mfma_f32_16x16x32_bf16 v[140:143], v[204:207], v[212:215], v[140:143]\n"
"v_mfma_f32_16x16x32_bf16 v[156:159], v[200:203], v[216:219], v[156:159]\n"
"v_mfma_f32_16x16x32_bf16 v[156:159], v[204:207], v[220:223], v[156:159]\n"
"s_waitcnt lgkmcnt(0)\n"
"s_waitcnt vmcnt(8)\n"
"s_barrier\n"
"s_setprio 1\n"
"v_mfma_f32_16x16x32_bf16 v[172:175], v[200:203], v[224:227], v[172:175]\n"
"ds_read_b128 v[208:211], v244 offset:0\n"
"v_mfma_f32_16x16x32_bf16 v[172:175], v[204:207], v[228:231], v[172:175]\n"
"ds_read_b128 v[212:215], v244 offset:64\n"
"ds_read_b128 v[216:219], v244 offset:128\n"
"ds_read_b128 v[220:223], v244 offset:192\n"
"ds_read_b128 v[224:227], v244 offset:256\n"
"ds_read_b128 v[228:231], v244 offset:320\n"
"ds_read_b128 v[232:235], v244 offset:384\n"
"ds_read_b128 v[236:239], v244 offset:448\n"
"s_waitcnt lgkmcnt(7)\n"
"v_mfma_f32_16x16x32_bf16 v[192:195], v[208:211], v[0:3], 0\n"
"s_waitcnt lgkmcnt(6)\n"
"v_mfma_f32_16x16x32_bf16 v[192:195], v[212:215], v[4:7], v[192:195]\n"
"s_waitcnt lgkmcnt(5)\n"
"v_mfma_f32_16x16x32_bf16 v[192:195], v[216:219], v[8:11], v[192:195]\n"
"s_waitcnt lgkmcnt(4)\n"
"v_mfma_f32_16x16x32_bf16 v[192:195], v[220:223], v[12:15], v[192:195]\n"
"v_mfma_f32_16x16x32_bf16 v[196:199], v[208:211], v[32:35], 0\n"
"v_mfma_f32_16x16x32_bf16 v[196:199], v[212:215], v[36:39], v[196:199]\n"
"v_mfma_f32_16x16x32_bf16 v[196:199], v[216:219], v[40:43], v[196:199]\n"
"v_mfma_f32_16x16x32_bf16 v[196:199], v[220:223], v[44:47], v[196:199]\n"
"s_waitcnt lgkmcnt(3)\n"
"v_mfma_f32_16x16x32_bf16 v[192:195], v[224:227], v[16:19], v[192:195]\n"
"s_waitcnt lgkmcnt(2)\n"
"v_mfma_f32_16x16x32_bf16 v[192:195], v[228:231], v[20:23], v[192:195]\n"
"s_waitcnt lgkmcnt(1)\n"
"v_mfma_f32_16x16x32_bf16 v[192:195], v[232:235], v[24:27], v[192:195]\n"
"s_waitcnt lgkmcnt(0)\n"
"v_mfma_f32_16x16x32_bf16 v[192:195], v[236:239], v[28:31], v[192:195]\n"
"s_nop 1\n"
"v_mfma_f32_16x16x32_bf16 v[196:199], v[224:227], v[48:51], v[196:199]\n"
"s_nop 1\n"
"v_mfma_f32_16x16x32_bf16 v[196:199], v[228:231], v[52:55], v[196:199]\n"
"s_nop 1\n"
"v_mfma_f32_16x16x32_bf16 v[196:199], v[232:235], v[56:59], v[196:199]\n"
"v_exp_f32 v192, v192\n"
"v_mfma_f32_16x16x32_bf16 v[196:199], v[236:239], v[60:63], v[196:199]\n"
"v_exp_f32 v193, v193\n"
"v_mfma_f32_16x16x32_bf16 v[200:203], v[208:211], v[64:67], 0\n"
"v_exp_f32 v194, v194\n"
"v_mfma_f32_16x16x32_bf16 v[200:203], v[212:215], v[68:71], v[200:203]\n"
"v_exp_f32 v195, v195\n"
"v_mfma_f32_16x16x32_bf16 v[200:203], v[216:219], v[72:75], v[200:203]\n"
"v_add_f32 v240, v240, v192\n"
"v_cvt_pk_bf16_f32 v250, v192, v193\n"
"v_mfma_f32_16x16x32_bf16 v[200:203], v[220:223], v[76:79], v[200:203]\n"
"v_add_f32 v240, v240, v193\n"
"v_cvt_pk_bf16_f32 v251, v194, v195\n"
"v_mfma_f32_16x16x32_bf16 v[200:203], v[224:227], v[80:83], v[200:203]\n"
"v_add_f32 v240, v240, v194\n"
"ds_write_b64 v247, v[250:251] offset:8192\n"
"v_mfma_f32_16x16x32_bf16 v[200:203], v[228:231], v[84:87], v[200:203]\n"
"v_add_f32 v240, v240, v195\n"
"v_exp_f32 v196, v196\n"
"v_mfma_f32_16x16x32_bf16 v[200:203], v[232:235], v[88:91], v[200:203]\n"
"v_exp_f32 v197, v197\n"
"v_mfma_f32_16x16x32_bf16 v[200:203], v[236:239], v[92:95], v[200:203]\n"
"v_exp_f32 v198, v198\n"
"v_exp_f32 v199, v199\n"
"v_add_f32 v241, v241, v196\n"
"v_cvt_pk_bf16_f32 v252, v196, v197\n"
"v_add_f32 v241, v241, v197\n"
"v_cvt_pk_bf16_f32 v253, v198, v199\n"
"v_add_f32 v241, v241, v198\n"
"ds_write_b64 v247, v[252:253] offset:10240\n"
"v_add_f32 v241, v241, v199\n"
"v_exp_f32 v200, v200\n"
"v_exp_f32 v201, v201\n"
"v_exp_f32 v202, v202\n"
"v_exp_f32 v203, v203\n"
"v_add_f32 v242, v242, v200\n"
"v_cvt_pk_bf16_f32 v254, v200, v201\n"
"v_add_f32 v242, v242, v201\n"
"v_cvt_pk_bf16_f32 v255, v202, v203\n"
"v_add_f32 v242, v242, v202\n"
"ds_write_b64 v247, v[254:255] offset:12288\n"
"v_add_f32 v242, v242, v203\n"
"v_mov_b32 v208, v240\n"
"s_nop 1\n"
"v_permlane16_swap_b32 v240, v208\n"
"v_add_f32 v240, v240, v208\n"
"v_mov_b32 v208, v240\n"
"s_nop 1\n"
"v_permlane32_swap_b32 v240, v208\n"
"v_add_f32 v240, v240, v208\n"
"v_mov_b32 v209, v241\n"
"s_nop 1\n"
"v_permlane16_swap_b32 v241, v209\n"
"v_add_f32 v241, v241, v209\n"
"v_mov_b32 v209, v241\n"
"s_nop 1\n"
"v_permlane32_swap_b32 v241, v209\n"
"v_add_f32 v241, v241, v209\n"
"v_mov_b32 v210, v242\n"
"s_nop 1\n"
"v_permlane16_swap_b32 v242, v210\n"
"v_add_f32 v242, v242, v210\n"
"v_mov_b32 v210, v242\n"
"s_nop 1\n"
"v_permlane32_swap_b32 v242, v210\n"
"v_add_f32 v242, v242, v210\n"
"v_mbcnt_lo_u32_b32 v216, -1, 0\n"
"v_mbcnt_hi_u32_b32 v216, -1, v216\n"
"v_and_b32 v216, 15, v216\n"
"v_lshlrev_b32 v216, 4, v216\n"
"s_lshl_b32 s84, s87, 2\n"
"s_add_u32 s84, s84, 161792\n"
"v_add_u32 v216, s84, v216\n"
"ds_write_b32 v216, v240 offset:1024\n"
"ds_write_b32 v216, v241 offset:1280\n"
"ds_write_b32 v216, v242 offset:1536\n"
"s_setprio 0\n"
"s_waitcnt lgkmcnt(0)\n"
"s_barrier\n"
"ds_read_b128 v[208:211], v248 offset:8192\n"
"ds_read_b64_tr_b16 v[192:193], v245 offset:34816\n"
"ds_read_b64_tr_b16 v[194:195], v245 offset:43520\n"
"ds_read_b128 v[212:215], v248 offset:9216\n"
"ds_read_b64_tr_b16 v[196:197], v245 offset:52224\n"
"ds_read_b64_tr_b16 v[198:199], v245 offset:60928\n"
"ds_read_b128 v[216:219], v248 offset:10240\n"
"ds_read_b128 v[220:223], v248 offset:11264\n"
"ds_read_b128 v[224:227], v248 offset:12288\n"
"ds_read_b128 v[228:231], v248 offset:13312\n"
"v_mbcnt_lo_u32_b32 v24, -1, 0\n"
"v_mbcnt_hi_u32_b32 v24, -1, v24\n"
"v_lshrrev_b32 v22, 5, v24\n"
"v_and_b32 v24, 31, v24\n"
"v_lshlrev_b32 v23, 1, v22\n"
"v_xor_b32 v24, v24, v23\n"
"v_mul_u32_u24 v22, 896, v22\n"
"v_lshrrev_b32 v23, 3, v24\n"
"v_mul_u32_u24 v23, 224, v23\n"
"v_add_u32 v22, v22, v23\n"
"v_and_b32 v24, 7, v24\n"
"v_lshl_add_u32 v24, v24, 4, v22\n"
"v_add_u32 v24, 114688, v24\n"
"v_mbcnt_lo_u32_b32 v20, -1, 0\n"
"v_mbcnt_hi_u32_b32 v20, -1, v20\n"
"v_and_b32 v20, 15, v20\n"
"v_lshlrev_b32 v20, 4, v20\n"
"v_add_u32 v20, 161792, v20\n"
"ds_read_b128 v[0:3], v20 offset:1024\n"
"ds_read_b128 v[4:7], v20 offset:1280\n"
"ds_read_b128 v[8:11], v20 offset:1536\n"
"s_add_u32 m0, s56, 0\n"
"ds_read_b64_tr_b16 v[200:201], v246 offset:34816\n"
"buffer_load_dwordx4 v24, s[48:51], s36 offen lds\n"
"s_add_u32 m0, s57, 0\n"
"ds_read_b64_tr_b16 v[202:203], v246 offset:43520\n"
"buffer_load_dwordx4 v24, s[48:51], s37 offen lds\n"
"s_add_u32 m0, s58, 0\n"
"ds_read_b64_tr_b16 v[204:205], v246 offset:52224\n"
"buffer_load_dwordx4 v24, s[48:51], s38 offen lds\n"
"s_add_u32 m0, s59, 0\n"
"ds_read_b64_tr_b16 v[206:207], v246 offset:60928\n"
"buffer_load_dwordx4 v24, s[48:51], s39 offen lds\n"
"s_add_u32 m0, s60, 0\n"
"s_nop 0\n"
"buffer_load_dwordx4 v24, s[48:51], s40 offen lds\n"
"s_add_u32 m0, s61, 0\n"
"s_nop 0\n"
"buffer_load_dwordx4 v24, s[48:51], s41 offen lds\n"
"s_add_u32 m0, s62, 0\n"
"s_nop 0\n"
"buffer_load_dwordx4 v24, s[48:51], s42 offen lds\n"
"s_add_u32 m0, s63, 0\n"
"s_nop 0\n"
"buffer_load_dwordx4 v24, s[48:51], s43 offen lds\n"
"s_waitcnt lgkmcnt(14)\n"
"v_mfma_f32_16x16x32_bf16 v[128:131], v[192:195], v[208:211], v[128:131]\n"
"s_waitcnt lgkmcnt(11)\n"
"v_mfma_f32_16x16x32_bf16 v[128:131], v[196:199], v[212:215], v[128:131]\n"
"s_waitcnt lgkmcnt(10)\n"
"v_mfma_f32_16x16x32_bf16 v[144:147], v[192:195], v[216:219], v[144:147]\n"
"s_waitcnt lgkmcnt(9)\n"
"v_mfma_f32_16x16x32_bf16 v[144:147], v[196:199], v[220:223], v[144:147]\n"
"s_waitcnt lgkmcnt(8)\n"
"v_mfma_f32_16x16x32_bf16 v[160:163], v[192:195], v[224:227], v[160:163]\n"
"s_waitcnt lgkmcnt(7)\n"
"v_mfma_f32_16x16x32_bf16 v[160:163], v[196:199], v[228:231], v[160:163]\n"
"ds_read_b64_tr_b16 v[192:193], v245 offset:34880\n"
"ds_read_b64_tr_b16 v[194:195], v245 offset:43584\n"
"ds_read_b64_tr_b16 v[196:197], v245 offset:52288\n"
"ds_read_b64_tr_b16 v[198:199], v245 offset:60992\n"
"s_waitcnt lgkmcnt(6)\n"
"v_mfma_f32_16x16x32_bf16 v[132:135], v[200:203], v[208:211], v[132:135]\n"
"s_waitcnt lgkmcnt(4)\n"
"v_mfma_f32_16x16x32_bf16 v[132:135], v[204:207], v[212:215], v[132:135]\n"
"v_mfma_f32_16x16x32_bf16 v[148:151], v[200:203], v[216:219], v[148:151]\n"
"v_mfma_f32_16x16x32_bf16 v[148:151], v[204:207], v[220:223], v[148:151]\n"
"v_mfma_f32_16x16x32_bf16 v[164:167], v[200:203], v[224:227], v[164:167]\n"
"v_mfma_f32_16x16x32_bf16 v[164:167], v[204:207], v[228:231], v[164:167]\n"
"ds_read_b64_tr_b16 v[200:201], v246 offset:34880\n"
"ds_read_b64_tr_b16 v[202:203], v246 offset:43584\n"
"ds_read_b64_tr_b16 v[204:205], v246 offset:52288\n"
"ds_read_b64_tr_b16 v[206:207], v246 offset:60992\n"
"s_waitcnt lgkmcnt(6)\n"
"v_mfma_f32_16x16x32_bf16 v[136:139], v[192:195], v[208:211], v[136:139]\n"
"s_waitcnt lgkmcnt(4)\n"
"v_mfma_f32_16x16x32_bf16 v[136:139], v[196:199], v[212:215], v[136:139]\n"
"v_mfma_f32_16x16x32_bf16 v[152:155], v[192:195], v[216:219], v[152:155]\n"
"v_mfma_f32_16x16x32_bf16 v[152:155], v[196:199], v[220:223], v[152:155]\n"
"v_mfma_f32_16x16x32_bf16 v[168:171], v[192:195], v[224:227], v[168:171]\n"
"v_mfma_f32_16x16x32_bf16 v[168:171], v[196:199], v[228:231], v[168:171]\n"
"s_waitcnt lgkmcnt(2)\n"
"v_mfma_f32_16x16x32_bf16 v[140:143], v[200:203], v[208:211], v[140:143]\n"
"s_waitcnt lgkmcnt(0)\n"
"v_mfma_f32_16x16x32_bf16 v[140:143], v[204:207], v[212:215], v[140:143]\n"
"v_mfma_f32_16x16x32_bf16 v[156:159], v[200:203], v[216:219], v[156:159]\n"
"v_mfma_f32_16x16x32_bf16 v[156:159], v[204:207], v[220:223], v[156:159]\n"
"v_mfma_f32_16x16x32_bf16 v[172:175], v[200:203], v[224:227], v[172:175]\n"
"v_mfma_f32_16x16x32_bf16 v[172:175], v[204:207], v[228:231], v[172:175]\n"
"L_postB:\n"
"v_add_f32 v0, v0, v1\n"
"v_add_f32 v2, v2, v3\n"
"v_add_f32 v0, v0, v2\n"
"v_add_f32 v4, v4, v5\n"
"v_add_f32 v6, v6, v7\n"
"v_add_f32 v4, v4, v6\n"
"v_add_f32 v8, v8, v9\n"
"v_add_f32 v10, v10, v11\n"
"v_add_f32 v8, v8, v10\n"
"s_mov_b64 s[84:85], 0\n"
"v_cmp_nle_f32 vcc, 0x18000000, v0\n"
"s_nop 1\n"
"s_or_b64 s[84:85], s[84:85], vcc\n"
"v_cmp_nge_f32 vcc, 0x5f800000, v0\n"
"s_nop 1\n"
"s_or_b64 s[84:85], s[84:85], vcc\n"
"v_cmp_nle_f32 vcc, 0x18000000, v4\n"
"s_nop 1\n"
"s_or_b64 s[84:85], s[84:85], vcc\n"
"v_cmp_nge_f32 vcc, 0x5f800000, v4\n"
"s_nop 1\n"
"s_or_b64 s[84:85], s[84:85], vcc\n"
"v_cmp_nle_f32 vcc, 0x18000000, v8\n"
"s_nop 1\n"
"s_or_b64 s[84:85], s[84:85], vcc\n"
"v_cmp_nge_f32 vcc, 0x5f800000, v8\n"
"s_nop 1\n"
"s_or_b64 s[84:85], s[84:85], vcc\n"
"v_rcp_f32 v0, v0\n"
"v_rcp_f32 v4, v4\n"
"v_rcp_f32 v8, v8\n"
"v_mov_b64 v[32:33], 0\n"
"v_mov_b64 v[34:35], 0\n"
"v_mov_b32 v1, v0\n"
"v_mov_b32 v5, v4\n"
"v_mov_b32 v9, v8\n"
"v_pk_fma_f32 v[32:33], v[128:129], v[34:35], v[32:33]\n"
"v_pk_mul_f32 v[128:129], v[128:129], v[0:1]\n"
"v_pk_fma_f32 v[32:33], v[130:131], v[34:35], v[32:33]\n"
"v_pk_mul_f32 v[130:131], v[130:131], v[0:1]\n"
"v_pk_fma_f32 v[32:33], v[132:133], v[34:35], v[32:33]\n"
"v_pk_mul_f32 v[132:133], v[132:133], v[0:1]\n"
"v_pk_fma_f32 v[32:33], v[134:135], v[34:35], v[32:33]\n"
"v_pk_mul_f32 v[134:135], v[134:135], v[0:1]\n"
"v_pk_fma_f32 v[32:33], v[136:137], v[34:35], v[32:33]\n"
"v_pk_mul_f32 v[136:137], v[136:137], v[0:1]\n"
"v_pk_fma_f32 v[32:33], v[138:139], v[34:35], v[32:33]\n"
"v_pk_mul_f32 v[138:139], v[138:139], v[0:1]\n"
"v_pk_fma_f32 v[32:33], v[140:141], v[34:35], v[32:33]\n"
"v_pk_mul_f32 v[140:141], v[140:141], v[0:1]\n"
"v_pk_fma_f32 v[32:33], v[142:143], v[34:35], v[32:33]\n"
"v_pk_mul_f32 v[142:143], v[142:143], v[0:1]\n"
"v_pk_fma_f32 v[32:33], v[144:145], v[34:35], v[32:33]\n"
"v_pk_mul_f32 v[144:145], v[144:145], v[4:5]\n"
"v_pk_fma_f32 v[32:33], v[146:147], v[34:35], v[32:33]\n"
"v_pk_mul_f32 v[146:147], v[146:147], v[4:5]\n"
"v_pk_fma_f32 v[32:33], v[148:149], v[34:35], v[32:33]\n"
"v_pk_mul_f32 v[148:149], v[148:149], v[4:5]\n"
"v_pk_fma_f32 v[32:33], v[150:151], v[34:35], v[32:33]\n"
"v_pk_mul_f32 v[150:151], v[150:151], v[4:5]\n"
"v_pk_fma_f32 v[32:33], v[152:153], v[34:35], v[32:33]\n"
"v_pk_mul_f32 v[152:153], v[152:153], v[4:5]\n"
"v_pk_fma_f32 v[32:33], v[154:155], v[34:35], v[32:33]\n"
"v_pk_mul_f32 v[154:155], v[154:155], v[4:5]\n"
"v_pk_fma_f32 v[32:33], v[156:157], v[34:35], v[32:33]\n"
"v_pk_mul_f32 v[156:157], v[156:157], v[4:5]\n"
"v_pk_fma_f32 v[32:33], v[158:159], v[34:35], v[32:33]\n"
"v_pk_mul_f32 v[158:159], v[158:159], v[4:5]\n"
"v_pk_fma_f32 v[32:33], v[160:161], v[34:35], v[32:33]\n"
"v_pk_mul_f32 v[160:161], v[160:161], v[8:9]\n"
"v_pk_fma_f32 v[32:33], v[162:163], v[34:35], v[32:33]\n"
"v_pk_mul_f32 v[162:163], v[162:163], v[8:9]\n"
"v_pk_fma_f32 v[32:33], v[164:165], v[34:35], v[32:33]\n"
"v_pk_mul_f32 v[164:165], v[164:165], v[8:9]\n"
"v_pk_fma_f32 v[32:33], v[166:167], v[34:35], v[32:33]\n"
"v_pk_mul_f32 v[166:167], v[166:167], v[8:9]\n"
"v_pk_fma_f32 v[32:33], v[168:169], v[34:35], v[32:33]\n"
"v_pk_mul_f32 v[168:169], v[168:169], v[8:9]\n"
"v_pk_fma_f32 v[32:33], v[170:171], v[34:35], v[32:33]\n"
"v_pk_mul_f32 v[170:171], v[170:171], v[8:9]\n"
"v_pk_fma_f32 v[32:33], v[172:173], v[34:35], v[32:33]\n"
"v_pk_mul_f32 v[172:173], v[172:173], v[8:9]\n"
"v_pk_fma_f32 v[32:33], v[174:175], v[34:35], v[32:33]\n"
"v_pk_mul_f32 v[174:175], v[174:175], v[8:9]\n"
"v_add_f32 v32, v32, v33\n"
"v_cmp_u_f32 vcc, v32, v32\n"
"s_nop 1\n"
"s_or_b64 s[84:85], s[84:85], vcc\n"
"v_cvt_pk_bf16_f32 v192, v128, v129\n"
"v_cvt_pk_bf16_f32 v193, v130, v131\n"
"v_cvt_pk_bf16_f32 v194, v132, v133\n"
"v_cvt_pk_bf16_f32 v195, v134, v135\n"
"v_cvt_pk_bf16_f32 v196, v136, v137\n"
"v_cvt_pk_bf16_f32 v197, v138, v139\n"
"v_cvt_pk_bf16_f32 v198, v140, v141\n"
"v_cvt_pk_bf16_f32 v199, v142, v143\n"
"v_cvt_pk_bf16_f32 v200, v144, v145\n"
"v_cvt_pk_bf16_f32 v201, v146, v147\n"
"v_cvt_pk_bf16_f32 v202, v148, v149\n"
"v_cvt_pk_bf16_f32 v203, v150, v151\n"
"v_cvt_pk_bf16_f32 v204, v152, v153\n"
"v_cvt_pk_bf16_f32 v205, v154, v155\n"
"v_cvt_pk_bf16_f32 v206, v156, v157\n"
"v_cvt_pk_bf16_f32 v207, v158, v159\n"
"v_cvt_pk_bf16_f32 v208, v160, v161\n"
"v_cvt_pk_bf16_f32 v209, v162, v163\n"
"v_cvt_pk_bf16_f32 v210, v164, v165\n"
"v_cvt_pk_bf16_f32 v211, v166, v167\n"
"v_cvt_pk_bf16_f32 v212, v168, v169\n"
"v_cvt_pk_bf16_f32 v213, v170, v171\n"
"v_cvt_pk_bf16_f32 v214, v172, v173\n"
"v_cvt_pk_bf16_f32 v215, v174, v175\n"
"v_mbcnt_lo_u32_b32 v36, -1, 0\n"
"v_mbcnt_hi_u32_b32 v36, -1, v36\n"
"v_lshlrev_b32 v248, 4, v36\n"
"v_add_u32 v240, 104448, v248\n"
"s_lshl_b32 s89, s87, 11\n"
"v_add_u32 v241, s89, v240\n"
"v_subrev_u32 v242, 69632, v245\n"
"v_subrev_u32 v243, 69632, v246\n"
"v_mbcnt_lo_u32_b32 v244, -1, 0\n"
"v_mbcnt_hi_u32_b32 v244, -1, v244\n"
"v_lshrrev_b32 v40, 5, v244\n"
"v_and_b32 v244, 31, v244\n"
"v_lshlrev_b32 v41, 1, v40\n"
"v_xor_b32 v244, v244, v41\n"
"v_mul_u32_u24 v40, 896, v40\n"
"v_lshrrev_b32 v41, 3, v244\n"
"v_mul_u32_u24 v41, 224, v41\n"
"v_add_u32 v40, v40, v41\n"
"v_and_b32 v244, 7, v244\n"
"v_lshl_add_u32 v244, v244, 4, v40\n"
"v_add_u32 v244, 172032, v244\n"
"v_mbcnt_lo_u32_b32 v247, -1, 0\n"
"v_mbcnt_hi_u32_b32 v247, -1, v247\n"
"v_lshrrev_b32 v40, 5, v247\n"
"v_and_b32 v247, 31, v247\n"
"v_lshlrev_b32 v41, 1, v40\n"
"v_xor_b32 v247, v247, v41\n"
"v_mul_u32_u24 v40, 896, v40\n"
"v_lshrrev_b32 v41, 3, v247\n"
"v_mul_u32_u24 v41, 224, v41\n"
"v_add_u32 v40, v40, v41\n"
"v_and_b32 v247, 7, v247\n"
"v_min_u32 v247, 5, v247\n"
"v_lshl_add_u32 v247, v247, 4, v40\n"
"v_add_u32 v247, 128, v247\n"
"v_and_b32 v251, 15, v36\n"
"v_lshlrev_b32 v251, 8, v251\n"
"v_mov_b32 v249, 163584\n"
"v_mov_b32 v42, 1\n"
"s_barrier\n"
"s_cmp_eq_u64 s[84:85], 0\n"
"s_cbranch_scc1 L_okB\n"
"ds_write_b32 v249, v42\n"
"L_okB:\n"
"ds_write_b128 v241, v[192:195] offset:32768\n"
"ds_write_b128 v241, v[196:199] offset:33792\n"
"ds_write_b128 v241, v[200:203] offset:40960\n"
"ds_write_b128 v241, v[204:207] offset:41984\n"
"ds_write_b128 v241, v[208:211] offset:49152\n"
"ds_write_b128 v241, v[212:215] offset:50176\n"
"v_mov_b64 v[128:129], 0\n"
"v_mov_b64 v[130:131], 0\n"
"v_mov_b64 v[132:133], 0\n"
"v_mov_b64 v[134:135], 0\n"
"v_mov_b64 v[136:137], 0\n"
"v_mov_b64 v[138:139], 0\n"
"v_mov_b64 v[140:141], 0\n"
"v_mov_b64 v[142:143], 0\n"
"v_mov_b64 v[144:145], 0\n"
"v_mov_b64 v[146:147], 0\n"
"v_mov_b64 v[148:149], 0\n"
"v_mov_b64 v[150:151], 0\n"
"v_mov_b64 v[152:153], 0\n"
"v_mov_b64 v[154:155], 0\n"
"v_mov_b64 v[156:157], 0\n"
"v_mov_b64 v[158:159], 0\n"
"v_mov_b64 v[160:161], 0\n"
"v_mov_b64 v[162:163], 0\n"
"v_mov_b64 v[164:165], 0\n"
"v_mov_b64 v[166:167], 0\n"
"v_mov_b64 v[168:169], 0\n"
"v_mov_b64 v[170:171], 0\n"
"v_mov_b64 v[172:173], 0\n"
"v_mov_b64 v[174:175], 0\n"
"v_mov_b64 v[0:1], 0\n"
"v_mov_b64 v[2:3], 0\n"
"v_mov_b64 v[4:5], 0\n"
"v_mov_b64 v[6:7], 0\n"
"v_mov_b64 v[8:9], 0\n"
"v_mov_b64 v[10:11], 0\n"
"v_mov_b64 v[12:13], 0\n"
"v_mov_b64 v[14:15], 0\n"
"v_mov_b64 v[16:17], 0\n"
"v_mov_b64 v[18:19], 0\n"
"v_mov_b64 v[20:21], 0\n"
"v_mov_b64 v[22:23], 0\n"
"v_mov_b64 v[24:25], 0\n"
"v_mov_b64 v[26:27], 0\n"
"v_mov_b64 v[28:29], 0\n"
"v_mov_b64 v[30:31], 0\n"
"v_mov_b64 v[32:33], 0\n"
"v_mov_b64 v[34:35], 0\n"
"s_mov_b64 s[52:53], %5\n"
"s_and_b32 s53, s53, 0xffff\n"
"s_mov_b32 s54, 1638400\n"
"s_mov_b32 s55, 0x20000\n"
"s_mov_b64 s[72:73], %6\n"
"s_and_b32 s73, s73, 0xffff\n"
"s_mov_b32 s74, 8448\n"
"s_mov_b32 s75, 0x20000\n"
"s_waitcnt vmcnt(0) lgkmcnt(0)\n"
"s_barrier\n"
"ds_read_b32 v250, v249\n"
"s_waitcnt lgkmcnt(0)\n"
"v_readfirstlane_b32 s84, v250\n"
"s_xor_b32 s85, s91, 1\n"
"s_nop 0\n"
"s_and_b32 s84, s84, s85\n"
"s_cmp_lg_u32 s84, 0\n"
"s_cbranch_scc1 L_fbB\n"
"ds_read_b128 v[208:211], v240 offset:32768\n"
"ds_read_b128 v[212:215], v240 offset:33792\n"
"ds_read_b128 v[216:219], v240 offset:40960\n"
"ds_read_b128 v[220:223], v240 offset:41984\n"
"ds_read_b128 v[224:227], v240 offset:49152\n"
"ds_read_b128 v[228:231], v240 offset:50176\n"
"ds_read_b64_tr_b16 v[192:193], v242 offset:34816\n"
"ds_read_b64_tr_b16 v[194:195], v242 offset:43520\n"
"ds_read_b64_tr_b16 v[196:197], v242 offset:52224\n"
"ds_read_b64_tr_b16 v[198:199], v242 offset:60928\n"
"ds_read_b64_tr_b16 v[200:201], v243 offset:34816\n"
"ds_read_b64_tr_b16 v[202:203], v243 offset:43520\n"
"ds_read_b64_tr_b16 v[204:205], v243 offset:52224\n"
"ds_read_b64_tr_b16 v[206:207], v243 offset:60928\n"
"s_waitcnt lgkmcnt(6)\n"
"v_mfma_f32_16x16x32_bf16 v[128:131], v[192:195], v[208:211], v[128:131]\n"
"s_waitcnt lgkmcnt(4)\n"
"v_mfma_f32_16x16x32_bf16 v[128:131], v[196:199], v[212:215], v[128:131]\n"
"v_mfma_f32_16x16x32_bf16 v[144:147], v[192:195], v[216:219], v[144:147]\n"
"v_mfma_f32_16x16x32_bf16 v[144:147], v[196:199], v[220:223], v[144:147]\n"
"v_mfma_f32_16x16x32_bf16 v[160:163], v[192:195], v[224:227], v[160:163]\n"
"v_mfma_f32_16x16x32_bf16 v[160:163], v[196:199], v[228:231], v[160:163]\n"
"ds_read_b64_tr_b16 v[192:193], v242 offset:34880\n"
"ds_read_b64_tr_b16 v[194:195], v242 offset:43584\n"
"ds_read_b64_tr_b16 v[196:197], v242 offset:52288\n"
"ds_read_b64_tr_b16 v[198:199], v242 offset:60992\n"
"s_waitcnt lgkmcnt(6)\n"
"v_mfma_f32_16x16x32_bf16 v[132:135], v[200:203], v[208:211], v[132:135]\n"
"s_waitcnt lgkmcnt(4)\n"
"v_mfma_f32_16x16x32_bf16 v[132:135], v[204:207], v[212:215], v[132:135]\n"
"v_mfma_f32_16x16x32_bf16 v[148:151], v[200:203], v[216:219], v[148:151]\n"
"v_mfma_f32_16x16x32_bf16 v[148:151], v[204:207], v[220:223], v[148:151]\n"
"v_mfma_f32_16x16x32_bf16 v[164:167], v[200:203], v[224:227], v[164:167]\n"
"v_mfma_f32_16x16x32_bf16 v[164:167], v[204:207], v[228:231], v[164:167]\n"
"ds_read_b64_tr_b16 v[200:201], v243 offset:34880\n"
"ds_read_b64_tr_b16 v[202:203], v243 offset:43584\n"
"ds_read_b64_tr_b16 v[204:205], v243 offset:52288\n"
"ds_read_b64_tr_b16 v[206:207], v243 offset:60992\n"
"s_waitcnt lgkmcnt(6)\n"
"v_mfma_f32_16x16x32_bf16 v[136:139], v[192:195], v[208:211], v[136:139]\n"
"ds_read_b128 v[64:67], v240 offset:34816\n"
"s_waitcnt lgkmcnt(5)\n"
"v_mfma_f32_16x16x32_bf16 v[136:139], v[196:199], v[212:215], v[136:139]\n"
"ds_read_b128 v[68:71], v240 offset:35840\n"
"v_mfma_f32_16x16x32_bf16 v[152:155], v[192:195], v[216:219], v[152:155]\n"
"ds_read_b128 v[72:75], v240 offset:43008\n"
"v_mfma_f32_16x16x32_bf16 v[152:155], v[196:199], v[220:223], v[152:155]\n"
"ds_read_b128 v[76:79], v240 offset:44032\n"
"v_mfma_f32_16x16x32_bf16 v[168:171], v[192:195], v[224:227], v[168:171]\n"
"ds_read_b128 v[80:83], v240 offset:51200\n"
"v_mfma_f32_16x16x32_bf16 v[168:171], v[196:199], v[228:231], v[168:171]\n"
"ds_read_b128 v[84:87], v240 offset:52224\n"
"s_waitcnt lgkmcnt(8)\n"
"v_mfma_f32_16x16x32_bf16 v[140:143], v[200:203], v[208:211], v[140:143]\n"
"s_waitcnt lgkmcnt(6)\n"
"v_mfma_f32_16x16x32_bf16 v[140:143], v[204:207], v[212:215], v[140:143]\n"
"v_mfma_f32_16x16x32_bf16 v[156:159], v[200:203], v[216:219], v[156:159]\n"
"v_mfma_f32_16x16x32_bf16 v[156:159], v[204:207], v[220:223], v[156:159]\n"
"s_waitcnt lgkmcnt(0)\n"
"s_waitcnt vmcnt(0) lgkmcnt(0)\n"
"s_barrier\n"
"v_mfma_f32_16x16x32_bf16 v[172:175], v[200:203], v[224:227], v[172:175]\n"
"ds_read_b64_tr_b16 v[192:193], v245 offset:0\n"
"v_mfma_f32_16x16x32_bf16 v[172:175], v[204:207], v[228:231], v[172:175]\n"
"ds_read_b64_tr_b16 v[194:195], v245 offset:8704\n"
"ds_read_b64_tr_b16 v[196:197], v245 offset:17408\n"
"ds_read_b64_tr_b16 v[198:199], v245 offset:26112\n"
"ds_read_b64_tr_b16 v[200:201], v246 offset:0\n"
"ds_read_b64_tr_b16 v[202:203], v246 offset:8704\n"
"ds_read_b64_tr_b16 v[204:205], v246 offset:17408\n"
"ds_read_b64_tr_b16 v[206:207], v246 offset:26112\n"
"s_waitcnt lgkmcnt(6)\n"
"v_mfma_f32_16x16x32_bf16 v[128:131], v[192:195], v[64:67], v[128:131]\n"
"s_waitcnt lgkmcnt(4)\n"
"v_mfma_f32_16x16x32_bf16 v[128:131], v[196:199], v[68:71], v[128:131]\n"
"s_add_u32 m0, s60, 34816\n"
"s_nop 0\n"
"buffer_load_dwordx4 v244, s[48:51], s40 offen lds\n"
"v_mfma_f32_16x16x32_bf16 v[144:147], v[192:195], v[72:75], v[144:147]\n"
"v_mfma_f32_16x16x32_bf16 v[144:147], v[196:199], v[76:79], v[144:147]\n"
"v_mfma_f32_16x16x32_bf16 v[160:163], v[192:195], v[80:83], v[160:163]\n"
"s_add_u32 m0, s61, 34816\n"
"s_nop 0\n"
"buffer_load_dwordx4 v244, s[48:51], s41 offen lds\n"
"v_mfma_f32_16x16x32_bf16 v[160:163], v[196:199], v[84:87], v[160:163]\n"
"ds_read_b64_tr_b16 v[192:193], v245 offset:64\n"
"ds_read_b64_tr_b16 v[194:195], v245 offset:8768\n"
"ds_read_b64_tr_b16 v[196:197], v245 offset:17472\n"
"ds_read_b64_tr_b16 v[198:199], v245 offset:26176\n"
"s_waitcnt lgkmcnt(6)\n"
"v_mfma_f32_16x16x32_bf16 v[132:135], v[200:203], v[64:67], v[132:135]\n"
"s_waitcnt lgkmcnt(4)\n"
"v_mfma_f32_16x16x32_bf16 v[132:135], v[204:207], v[68:71], v[132:135]\n"
"s_add_u32 m0, s62, 34816\n"
"s_nop 0\n"
"buffer_load_dwordx4 v244, s[48:51], s42 offen lds\n"
"v_mfma_f32_16x16x32_bf16 v[148:151], v[200:203], v[72:75], v[148:151]\n"
"v_mfma_f32_16x16x32_bf16 v[148:151], v[204:207], v[76:79], v[148:151]\n"
"v_mfma_f32_16x16x32_bf16 v[164:167], v[200:203], v[80:83], v[164:167]\n"
"s_add_u32 m0, s63, 34816\n"
"s_nop 0\n"
"buffer_load_dwordx4 v244, s[48:51], s43 offen lds\n"
"v_add_u32 v244, 57344, v244\n"
"v_mfma_f32_16x16x32_bf16 v[164:167], v[204:207], v[84:87], v[164:167]\n"
"ds_read_b64_tr_b16 v[200:201], v246 offset:64\n"
"ds_read_b64_tr_b16 v[202:203], v246 offset:8768\n"
"ds_read_b64_tr_b16 v[204:205], v246 offset:17472\n"
"ds_read_b64_tr_b16 v[206:207], v246 offset:26176\n"
"s_waitcnt lgkmcnt(6)\n"
"v_mfma_f32_16x16x32_bf16 v[136:139], v[192:195], v[64:67], v[136:139]\n"
"ds_read_b128 v[208:211], v240 offset:36864\n"
"s_waitcnt lgkmcnt(5)\n"
"v_mfma_f32_16x16x32_bf16 v[136:139], v[196:199], v[68:71], v[136:139]\n"
"ds_read_b128 v[212:215], v240 offset:37888\n"
"v_mfma_f32_16x16x32_bf16 v[152:155], v[192:195], v[72:75], v[152:155]\n"
"ds_read_b128 v[216:219], v240 offset:45056\n"
"v_mfma_f32_16x16x32_bf16 v[152:155], v[196:199], v[76:79], v[152:155]\n"
"ds_read_b128 v[220:223], v240 offset:46080\n"
"v_mfma_f32_16x16x32_bf16 v[168:171], v[192:195], v[80:83], v[168:171]\n"
"ds_read_b128 v[224:227], v240 offset:53248\n"
"v_mfma_f32_16x16x32_bf16 v[168:171], v[196:199], v[84:87], v[168:171]\n"
"ds_read_b128 v[228:231], v240 offset:54272\n"
"s_waitcnt lgkmcnt(8)\n"
"v_mfma_f32_16x16x32_bf16 v[140:143], v[200:203], v[64:67], v[140:143]\n"
"s_waitcnt lgkmcnt(6)\n"
"v_mfma_f32_16x16x32_bf16 v[140:143], v[204:207], v[68:71], v[140:143]\n"
"v_mfma_f32_16x16x32_bf16 v[156:159], v[200:203], v[72:75], v[156:159]\n"
"v_mfma_f32_16x16x32_bf16 v[156:159], v[204:207], v[76:79], v[156:159]\n"
"s_waitcnt lgkmcnt(0)\n"
"s_waitcnt vmcnt(4) lgkmcnt(0)\n"
"s_barrier\n"
"v_mfma_f32_16x16x32_bf16 v[172:175], v[200:203], v[80:83], v[172:175]\n"
"ds_read_b64_tr_b16 v[192:193], v242 offset:0\n"
"v_mfma_f32_16x16x32_bf16 v[172:175], v[204:207], v[84:87], v[172:175]\n"
"ds_read_b64_tr_b16 v[194:195], v242 offset:8704\n"
"ds_read_b64_tr_b16 v[196:197], v242 offset:17408\n"
"ds_read_b64_tr_b16 v[198:199], v242 offset:26112\n"
"ds_read_b64_tr_b16 v[200:201], v243 offset:0\n"
"ds_read_b64_tr_b16 v[202:203], v243 offset:8704\n"
"ds_read_b64_tr_b16 v[204:205], v243 offset:17408\n"
"ds_read_b64_tr_b16 v[206:207], v243 offset:26112\n"
"s_waitcnt lgkmcnt(6)\n"
"v_mfma_f32_16x16x32_bf16 v[128:131], v[192:195], v[208:211], v[128:131]\n"
"s_waitcnt lgkmcnt(4)\n"
"v_mfma_f32_16x16x32_bf16 v[128:131], v[196:199], v[212:215], v[128:131]\n"
"s_add_u32 m0, s60, 69632\n"
"s_nop 0\n"
"buffer_load_dwordx4 v247, s[48:51], s40 offen lds\n"
"v_mfma_f32_16x16x32_bf16 v[144:147], v[192:195], v[216:219], v[144:147]\n"
"v_mfma_f32_16x16x32_bf16 v[144:147], v[196:199], v[220:223], v[144:147]\n"
"v_mfma_f32_16x16x32_bf16 v[160:163], v[192:195], v[224:227], v[160:163]\n"
"s_add_u32 m0, s61, 69632\n"
"s_nop 0\n"
"buffer_load_dwordx4 v247, s[48:51], s41 offen lds\n"
"v_mfma_f32_16x16x32_bf16 v[160:163], v[196:199], v[228:231], v[160:163]\n"
"ds_read_b64_tr_b16 v[192:193], v242 offset:64\n"
"ds_read_b64_tr_b16 v[194:195], v242 offset:8768\n"
"ds_read_b64_tr_b16 v[196:197], v242 offset:17472\n"
"ds_read_b64_tr_b16 v[198:199], v242 offset:26176\n"
"s_waitcnt lgkmcnt(6)\n"
"v_mfma_f32_16x16x32_bf16 v[132:135], v[200:203], v[208:211], v[132:135]\n"
"s_waitcnt lgkmcnt(4)\n"
"v_mfma_f32_16x16x32_bf16 v[132:135], v[204:207], v[212:215], v[132:135]\n"
"s_add_u32 m0, s62, 69632\n"
"s_nop 0\n"
"buffer_load_dwordx4 v247, s[48:51], s42 offen lds\n"
"v_mfma_f32_16x16x32_bf16 v[148:151], v[200:203], v[216:219], v[148:151]\n"
"v_mfma_f32_16x16x32_bf16 v[148:151], v[204:207], v[220:223], v[148:151]\n"
"v_mfma_f32_16x16x32_bf16 v[164:167], v[200:203], v[224:227], v[164:167]\n"
"s_add_u32 m0, s63, 69632\n"
"s_nop 0\n"
"buffer_load_dwordx4 v247, s[48:51], s43 offen lds\n"
"v_add_u32 v247, 57344, v247\n"
"v_mfma_f32_16x16x32_bf16 v[164:167], v[204:207], v[228:231], v[164:167]\n"
"ds_read_b64_tr_b16 v[200:201], v243 offset:64\n"
"ds_read_b64_tr_b16 v[202:203], v243 offset:8768\n"
"ds_read_b64_tr_b16 v[204:205], v243 offset:17472\n"
"ds_read_b64_tr_b16 v[206:207], v243 offset:26176\n"
"s_waitcnt lgkmcnt(6)\n"
"v_mfma_f32_16x16x32_bf16 v[136:139], v[192:195], v[208:211], v[136:139]\n"
"ds_read_b128 v[64:67], v240 offset:38912\n"
"s_waitcnt lgkmcnt(5)\n"
"v_mfma_f32_16x16x32_bf16 v[136:139], v[196:199], v[212:215], v[136:139]\n"
"ds_read_b128 v[68:71], v240 offset:39936\n"
"v_mfma_f32_16x16x32_bf16 v[152:155], v[192:195], v[216:219], v[152:155]\n"
"ds_read_b128 v[72:75], v240 offset:47104\n"
"v_mfma_f32_16x16x32_bf16 v[152:155], v[196:199], v[220:223], v[152:155]\n"
"ds_read_b128 v[76:79], v240 offset:48128\n"
"v_mfma_f32_16x16x32_bf16 v[168:171], v[192:195], v[224:227], v[168:171]\n"
"ds_read_b128 v[80:83], v240 offset:55296\n"
"v_mfma_f32_16x16x32_bf16 v[168:171], v[196:199], v[228:231], v[168:171]\n"
"ds_read_b128 v[84:87], v240 offset:56320\n"
"s_waitcnt lgkmcnt(8)\n"
"v_mfma_f32_16x16x32_bf16 v[140:143], v[200:203], v[208:211], v[140:143]\n"
"s_waitcnt lgkmcnt(6)\n"
"v_mfma_f32_16x16x32_bf16 v[140:143], v[204:207], v[212:215], v[140:143]\n"
"v_mfma_f32_16x16x32_bf16 v[156:159], v[200:203], v[216:219], v[156:159]\n"
"v_mfma_f32_16x16x32_bf16 v[156:159], v[204:207], v[220:223], v[156:159]\n"
"s_waitcnt lgkmcnt(0)\n"
"s_waitcnt vmcnt(4) lgkmcnt(0)\n"
"s_barrier\n"
"v_mfma_f32_16x16x32_bf16 v[172:175], v[200:203], v[224:227], v[172:175]\n"
"ds_read_b64_tr_b16 v[192:193], v242 offset:34816\n"
"v_mfma_f32_16x16x32_bf16 v[172:175], v[204:207], v[228:231], v[172:175]\n"
"ds_read_b64_tr_b16 v[194:195], v242 offset:43520\n"
"ds_read_b64_tr_b16 v[196:197], v242 offset:52224\n"
"ds_read_b64_tr_b16 v[198:199], v242 offset:60928\n"
"ds_read_b64_tr_b16 v[200:201], v243 offset:34816\n"
"ds_read_b64_tr_b16 v[202:203], v243 offset:43520\n"
"ds_read_b64_tr_b16 v[204:205], v243 offset:52224\n"
"ds_read_b64_tr_b16 v[206:207], v243 offset:60928\n"
"s_waitcnt lgkmcnt(6)\n"
"v_mfma_f32_16x16x32_bf16 v[128:131], v[192:195], v[64:67], v[128:131]\n"
"s_waitcnt lgkmcnt(4)\n"
"v_mfma_f32_16x16x32_bf16 v[128:131], v[196:199], v[68:71], v[128:131]\n"
"s_add_u32 m0, s60, 0\n"
"s_nop 0\n"
"buffer_load_dwordx4 v247, s[48:51], s40 offen lds\n"
"v_mfma_f32_16x16x32_bf16 v[144:147], v[192:195], v[72:75], v[144:147]\n"
"v_mfma_f32_16x16x32_bf16 v[144:147], v[196:199], v[76:79], v[144:147]\n"
"v_mfma_f32_16x16x32_bf16 v[160:163], v[192:195], v[80:83], v[160:163]\n"
"s_add_u32 m0, s61, 0\n"
"s_nop 0\n"
"buffer_load_dwordx4 v247, s[48:51], s41 offen lds\n"
"v_mfma_f32_16x16x32_bf16 v[160:163], v[196:199], v[84:87], v[160:163]\n"
"ds_read_b64_tr_b16 v[192:193], v242 offset:34880\n"
"ds_read_b64_tr_b16 v[194:195], v242 offset:43584\n"
"ds_read_b64_tr_b16 v[196:197], v242 offset:52288\n"
"ds_read_b64_tr_b16 v[198:199], v242 offset:60992\n"
"s_waitcnt lgkmcnt(6)\n"
"v_mfma_f32_16x16x32_bf16 v[132:135], v[200:203], v[64:67], v[132:135]\n"
"s_waitcnt lgkmcnt(4)\n"
"v_mfma_f32_16x16x32_bf16 v[132:135], v[204:207], v[68:71], v[132:135]\n"
"s_add_u32 m0, s62, 0\n"
"s_nop 0\n"
"buffer_load_dwordx4 v247, s[48:51], s42 offen lds\n"
"v_mfma_f32_16x16x32_bf16 v[148:151], v[200:203], v[72:75], v[148:151]\n"
"v_mfma_f32_16x16x32_bf16 v[148:151], v[204:207], v[76:79], v[148:151]\n"
"v_mfma_f32_16x16x32_bf16 v[164:167], v[200:203], v[80:83], v[164:167]\n"
"s_add_u32 m0, s63, 0\n"
"s_nop 0\n"
"buffer_load_dwordx4 v247, s[48:51], s43 offen lds\n"
"v_add_u32 v247, 57344, v247\n"
"v_mfma_f32_16x16x32_bf16 v[164:167], v[204:207], v[84:87], v[164:167]\n"
"ds_read_b64_tr_b16 v[200:201], v243 offset:34880\n"
"ds_read_b64_tr_b16 v[202:203], v243 offset:43584\n"
"ds_read_b64_tr_b16 v[204:205], v243 offset:52288\n"
"ds_read_b64_tr_b16 v[206:207], v243 offset:60992\n"
"s_waitcnt lgkmcnt(6)\n"
"v_mfma_f32_16x16x32_bf16 v[136:139], v[192:195], v[64:67], v[136:139]\n"
"ds_read_b128 v[208:211], v240 offset:32768\n"
"s_waitcnt lgkmcnt(5)\n"
"v_mfma_f32_16x16x32_bf16 v[136:139], v[196:199], v[68:71], v[136:139]\n"
"ds_read_b128 v[212:215], v240 offset:33792\n"
"v_mfma_f32_16x16x32_bf16 v[152:155], v[192:195], v[72:75], v[152:155]\n"
"ds_read_b128 v[216:219], v240 offset:40960\n"
"v_mfma_f32_16x16x32_bf16 v[152:155], v[196:199], v[76:79], v[152:155]\n"
"ds_read_b128 v[220:223], v240 offset:41984\n"
"v_mfma_f32_16x16x32_bf16 v[168:171], v[192:195], v[80:83], v[168:171]\n"
"ds_read_b128 v[224:227], v240 offset:49152\n"
"v_mfma_f32_16x16x32_bf16 v[168:171], v[196:199], v[84:87], v[168:171]\n"
"ds_read_b128 v[228:231], v240 offset:50176\n"
"s_waitcnt lgkmcnt(8)\n"
"v_mfma_f32_16x16x32_bf16 v[140:143], v[200:203], v[64:67], v[140:143]\n"
"s_waitcnt lgkmcnt(6)\n"
"v_mfma_f32_16x16x32_bf16 v[140:143], v[204:207], v[68:71], v[140:143]\n"
"v_mfma_f32_16x16x32_bf16 v[156:159], v[200:203], v[72:75], v[156:159]\n"
"v_mfma_f32_16x16x32_bf16 v[156:159], v[204:207], v[76:79], v[156:159]\n"
"s_waitcnt lgkmcnt(0)\n"
"s_waitcnt vmcnt(4) lgkmcnt(0)\n"
"s_barrier\n"
"v_mfma_f32_16x16x32_bf16 v[172:175], v[200:203], v[80:83], v[172:175]\n"
"ds_read_b64_tr_b16 v[192:193], v245 offset:0\n"
"v_mfma_f32_16x16x32_bf16 v[172:175], v[204:207], v[84:87], v[172:175]\n"
"ds_read_b64_tr_b16 v[194:195], v245 offset:8704\n"
"ds_read_b64_tr_b16 v[196:197], v245 offset:17408\n"
"ds_read_b64_tr_b16 v[198:199], v245 offset:26112\n"
"ds_read_b64_tr_b16 v[200:201], v246 offset:0\n"
"ds_read_b64_tr_b16 v[202:203], v246 offset:8704\n"
"ds_read_b64_tr_b16 v[204:205], v246 offset:17408\n"
"ds_read_b64_tr_b16 v[206:207], v246 offset:26112\n"
"s_waitcnt lgkmcnt(6)\n"
"v_mfma_f32_16x16x32_bf16 v[0:3], v[192:195], v[208:211], v[0:3]\n"
"s_waitcnt lgkmcnt(4)\n"
"v_mfma_f32_16x16x32_bf16 v[0:3], v[196:199], v[212:215], v[0:3]\n"
"s_add_u32 m0, s60, 34816\n"
"s_nop 0\n"
"buffer_load_dwordx4 v247, s[48:51], s40 offen lds\n"
"v_mfma_f32_16x16x32_bf16 v[12:15], v[192:195], v[216:219], v[12:15]\n"
"v_mfma_f32_16x16x32_bf16 v[12:15], v[196:199], v[220:223], v[12:15]\n"
"v_mfma_f32_16x16x32_bf16 v[24:27], v[192:195], v[224:227], v[24:27]\n"
"s_add_u32 m0, s61, 34816\n"
"s_nop 0\n"
"buffer_load_dwordx4 v247, s[48:51], s41 offen lds\n"
"v_mfma_f32_16x16x32_bf16 v[24:27], v[196:199], v[228:231], v[24:27]\n"
"ds_read_b64_tr_b16 v[192:193], v245 offset:64\n"
"ds_read_b64_tr_b16 v[194:195], v245 offset:8768\n"
"ds_read_b64_tr_b16 v[196:197], v245 offset:17472\n"
"ds_read_b64_tr_b16 v[198:199], v245 offset:26176\n"
"s_waitcnt lgkmcnt(6)\n"
"v_mfma_f32_16x16x32_bf16 v[4:7], v[200:203], v[208:211], v[4:7]\n"
"ds_read_b128 v[64:67], v240 offset:34816\n"
"s_waitcnt lgkmcnt(5)\n"
"v_mfma_f32_16x16x32_bf16 v[4:7], v[204:207], v[212:215], v[4:7]\n"
"ds_read_b128 v[68:71], v240 offset:35840\n"
"s_add_u32 m0, s62, 34816\n"
"s_nop 0\n"
"buffer_load_dwordx4 v247, s[48:51], s42 offen lds\n"
"v_mfma_f32_16x16x32_bf16 v[16:19], v[200:203], v[216:219], v[16:19]\n"
"ds_read_b128 v[72:75], v240 offset:43008\n"
"v_mfma_f32_16x16x32_bf16 v[16:19], v[204:207], v[220:223], v[16:19]\n"
"ds_read_b128 v[76:79], v240 offset:44032\n"
"v_mfma_f32_16x16x32_bf16 v[28:31], v[200:203], v[224:227], v[28:31]\n"
"ds_read_b128 v[80:83], v240 offset:51200\n"
"s_add_u32 m0, s63, 34816\n"
"s_nop 0\n"
"buffer_load_dwordx4 v247, s[48:51], s43 offen lds\n"
"v_add_u32 v247, 57344, v247\n"
"v_mfma_f32_16x16x32_bf16 v[28:31], v[204:207], v[228:231], v[28:31]\n"
"ds_read_b128 v[84:87], v240 offset:52224\n"
"s_waitcnt lgkmcnt(8)\n"
"v_mfma_f32_16x16x32_bf16 v[8:11], v[192:195], v[208:211], v[8:11]\n"
"s_waitcnt lgkmcnt(6)\n"
"v_mfma_f32_16x16x32_bf16 v[8:11], v[196:199], v[212:215], v[8:11]\n"
"v_mfma_f32_16x16x32_bf16 v[20:23], v[192:195], v[216:219], v[20:23]\n"
"v_mfma_f32_16x16x32_bf16 v[20:23], v[196:199], v[220:223], v[20:23]\n"
"s_waitcnt lgkmcnt(0)\n"
"s_waitcnt vmcnt(4) lgkmcnt(0)\n"
"s_barrier\n"
"v_mfma_f32_16x16x32_bf16 v[32:35], v[192:195], v[224:227], v[32:35]\n"
"ds_read_b64_tr_b16 v[200:201], v242 offset:0\n"
"v_mfma_f32_16x16x32_bf16 v[32:35], v[196:199], v[228:231], v[32:35]\n"
"ds_read_b64_tr_b16 v[202:203], v242 offset:8704\n"
"ds_read_b64_tr_b16 v[204:205], v242 offset:17408\n"
"ds_read_b64_tr_b16 v[206:207], v242 offset:26112\n"
"ds_read_b64_tr_b16 v[192:193], v243 offset:0\n"
"ds_read_b64_tr_b16 v[194:195], v243 offset:8704\n"
"ds_read_b64_tr_b16 v[196:197], v243 offset:17408\n"
"ds_read_b64_tr_b16 v[198:199], v243 offset:26112\n"
"s_waitcnt lgkmcnt(6)\n"
"v_mfma_f32_16x16x32_bf16 v[0:3], v[200:203], v[64:67], v[0:3]\n"
"s_waitcnt lgkmcnt(4)\n"
"v_mfma_f32_16x16x32_bf16 v[0:3], v[204:207], v[68:71], v[0:3]\n"
"s_add_u32 m0, s60, 69632\n"
"s_nop 0\n"
"buffer_load_dwordx4 v247, s[48:51], s40 offen lds\n"
"v_mfma_f32_16x16x32_bf16 v[12:15], v[200:203], v[72:75], v[12:15]\n"
"v_mfma_f32_16x16x32_bf16 v[12:15], v[204:207], v[76:79], v[12:15]\n"
"v_mfma_f32_16x16x32_bf16 v[24:27], v[200:203], v[80:83], v[24:27]\n"
"s_add_u32 m0, s61, 69632\n"
"s_nop 0\n"
"buffer_load_dwordx4 v247, s[48:51], s41 offen lds\n"
"v_mfma_f32_16x16x32_bf16 v[24:27], v[204:207], v[84:87], v[24:27]\n"
"ds_read_b64_tr_b16 v[200:201], v242 offset:64\n"
"ds_read_b64_tr_b16 v[202:203], v242 offset:8768\n"
"ds_read_b64_tr_b16 v[204:205], v242 offset:17472\n"
"ds_read_b64_tr_b16 v[206:207], v242 offset:26176\n"
"s_waitcnt lgkmcnt(6)\n"
"v_mfma_f32_16x16x32_bf16 v[4:7], v[192:195], v[64:67], v[4:7]\n"
"ds_read_b128 v[208:211], v240 offset:36864\n"
"s_waitcnt lgkmcnt(5)\n"
"v_mfma_f32_16x16x32_bf16 v[4:7], v[196:199], v[68:71], v[4:7]\n"
"ds_read_b128 v[212:215], v240 offset:37888\n"
"s_add_u32 m0, s62, 69632\n"
"s_nop 0\n"
"buffer_load_dwordx4 v247, s[48:51], s42 offen lds\n"
"v_mfma_f32_16x16x32_bf16 v[16:19], v[192:195], v[72:75], v[16:19]\n"
"ds_read_b128 v[216:219], v240 offset:45056\n"
"v_mfma_f32_16x16x32_bf16 v[16:19], v[196:199], v[76:79], v[16:19]\n"
"ds_read_b128 v[220:223], v240 offset:46080\n"
"v_mfma_f32_16x16x32_bf16 v[28:31], v[192:195], v[80:83], v[28:31]\n"
"ds_read_b128 v[224:227], v240 offset:53248\n"
"s_add_u32 m0, s63, 69632\n"
"s_nop 0\n"
"buffer_load_dwordx4 v247, s[48:51], s43 offen lds\n"
"v_add_u32 v247, 57344, v247\n"
"v_mfma_f32_16x16x32_bf16 v[28:31], v[196:199], v[84:87], v[28:31]\n"
"ds_read_b128 v[228:231], v240 offset:54272\n"
"s_waitcnt lgkmcnt(8)\n"
"v_mfma_f32_16x16x32_bf16 v[8:11], v[200:203], v[64:67], v[8:11]\n"
"s_waitcnt lgkmcnt(6)\n"
"v_mfma_f32_16x16x32_bf16 v[8:11], v[204:207], v[68:71], v[8:11]\n"
"v_mfma_f32_16x16x32_bf16 v[20:23], v[200:203], v[72:75], v[20:23]\n"
"v_mfma_f32_16x16x32_bf16 v[20:23], v[204:207], v[76:79], v[20:23]\n"
"s_waitcnt lgkmcnt(0)\n"
"s_waitcnt vmcnt(4) lgkmcnt(0)\n"
"s_barrier\n"
"v_mfma_f32_16x16x32_bf16 v[32:35], v[200:203], v[80:83], v[32:35]\n"
"ds_read_b64_tr_b16 v[192:193], v242 offset:34816\n"
"v_mfma_f32_16x16x32_bf16 v[32:35], v[204:207], v[84:87], v[32:35]\n"
"ds_read_b64_tr_b16 v[194:195], v242 offset:43520\n"
"ds_read_b64_tr_b16 v[196:197], v242 offset:52224\n"
"ds_read_b64_tr_b16 v[198:199], v242 offset:60928\n"
"ds_read_b64_tr_b16 v[200:201], v243 offset:34816\n"
"ds_read_b64_tr_b16 v[202:203], v243 offset:43520\n"
"ds_read_b64_tr_b16 v[204:205], v243 offset:52224\n"
"ds_read_b64_tr_b16 v[206:207], v243 offset:60928\n"
"s_waitcnt lgkmcnt(6)\n"
"v_mfma_f32_16x16x32_bf16 v[0:3], v[192:195], v[208:211], v[0:3]\n"
"s_waitcnt lgkmcnt(4)\n"
"v_mfma_f32_16x16x32_bf16 v[0:3], v[196:199], v[212:215], v[0:3]\n"
"s_bfe_u32 s85, %8, 0x80008\n"
"s_lshl_b32 s85, s85, 10\n"
"s_mul_i32 s89, s87, 98304\n"
"s_add_u32 s89, s89, 524288\n"
"s_add_u32 s89, s89, s85\n"
"v_cvt_pk_bf16_f32 v48, v128, v129\n"
"v_cvt_pk_bf16_f32 v49, v130, v131\n"
"v_cvt_pk_bf16_f32 v50, v144, v145\n"
"v_cvt_pk_bf16_f32 v51, v146, v147\n"
"buffer_store_dwordx4 v[48:51], v248, s[52:55], s89 offen sc1\n"
"s_add_u32 s89, s89, 16384\n"
"v_mfma_f32_16x16x32_bf16 v[12:15], v[192:195], v[216:219], v[12:15]\n"
"v_mfma_f32_16x16x32_bf16 v[12:15], v[196:199], v[220:223], v[12:15]\n"
"v_mfma_f32_16x16x32_bf16 v[24:27], v[192:195], v[224:227], v[24:27]\n"
"v_mfma_f32_16x16x32_bf16 v[24:27], v[196:199], v[228:231], v[24:27]\n"
"ds_read_b64_tr_b16 v[192:193], v242 offset:34880\n"
"ds_read_b64_tr_b16 v[194:195], v242 offset:43584\n"
"ds_read_b64_tr_b16 v[196:197], v242 offset:52288\n"
"ds_read_b64_tr_b16 v[198:199], v242 offset:60992\n"
"v_cvt_pk_bf16_f32 v52, v132, v133\n"
"v_cvt_pk_bf16_f32 v53, v134, v135\n"
"v_cvt_pk_bf16_f32 v54, v148, v149\n"
"v_cvt_pk_bf16_f32 v55, v150, v151\n"
"buffer_store_dwordx4 v[52:55], v248, s[52:55], s89 offen sc1\n"
"s_add_u32 s89, s89, 16384\n"
"s_waitcnt lgkmcnt(6)\n"
"v_mfma_f32_16x16x32_bf16 v[4:7], v[200:203], v[208:211], v[4:7]\n"
"ds_read_b128 v[64:67], v240 offset:38912\n"
"s_waitcnt lgkmcnt(5)\n"
"v_mfma_f32_16x16x32_bf16 v[4:7], v[204:207], v[212:215], v[4:7]\n"
"ds_read_b128 v[68:71], v240 offset:39936\n"
"v_mfma_f32_16x16x32_bf16 v[16:19], v[200:203], v[216:219], v[16:19]\n"
"ds_read_b128 v[72:75], v240 offset:47104\n"
"v_mfma_f32_16x16x32_bf16 v[16:19], v[204:207], v[220:223], v[16:19]\n"
"ds_read_b128 v[76:79], v240 offset:48128\n"
"v_cvt_pk_bf16_f32 v56, v136, v137\n"
"v_cvt_pk_bf16_f32 v57, v138, v139\n"
"v_cvt_pk_bf16_f32 v58, v152, v153\n"
"v_cvt_pk_bf16_f32 v59, v154, v155\n"
"buffer_store_dwordx4 v[56:59], v248, s[52:55], s89 offen sc1\n"
"s_add_u32 s89, s89, 16384\n"
"v_mfma_f32_16x16x32_bf16 v[28:31], v[200:203], v[224:227], v[28:31]\n"
"ds_read_b128 v[80:83], v240 offset:55296\n"
"v_mfma_f32_16x16x32_bf16 v[28:31], v[204:207], v[228:231], v[28:31]\n"
"ds_read_b128 v[84:87], v240 offset:56320\n"
"s_waitcnt lgkmcnt(8)\n"
"v_mfma_f32_16x16x32_bf16 v[8:11], v[192:195], v[208:211], v[8:11]\n"
"s_waitcnt lgkmcnt(6)\n"
"v_mfma_f32_16x16x32_bf16 v[8:11], v[196:199], v[212:215], v[8:11]\n"
"v_mfma_f32_16x16x32_bf16 v[20:23], v[192:195], v[216:219], v[20:23]\n"
"v_mfma_f32_16x16x32_bf16 v[20:23], v[196:199], v[220:223], v[20:23]\n"
"s_waitcnt lgkmcnt(0)\n"
"s_waitcnt vmcnt(3) lgkmcnt(0)\n"
"s_barrier\n"
"v_mfma_f32_16x16x32_bf16 v[32:35], v[192:195], v[224:227], v[32:35]\n"
"ds_read_b64_tr_b16 v[200:201], v245 offset:0\n"
"v_mfma_f32_16x16x32_bf16 v[32:35], v[196:199], v[228:231], v[32:35]\n"
"ds_read_b64_tr_b16 v[202:203], v245 offset:8704\n"
"ds_read_b64_tr_b16 v[204:205], v245 offset:17408\n"
"ds_read_b64_tr_b16 v[206:207], v245 offset:26112\n"
"ds_read_b64_tr_b16 v[192:193], v246 offset:0\n"
"ds_read_b64_tr_b16 v[194:195], v246 offset:8704\n"
"ds_read_b64_tr_b16 v[196:197], v246 offset:17408\n"
"ds_read_b64_tr_b16 v[198:199], v246 offset:26112\n"
"s_waitcnt lgkmcnt(6)\n"
"v_mfma_f32_16x16x32_bf16 v[0:3], v[200:203], v[64:67], v[0:3]\n"
"s_waitcnt lgkmcnt(4)\n"
"v_mfma_f32_16x16x32_bf16 v[0:3], v[204:207], v[68:71], v[0:3]\n"
"v_cvt_pk_bf16_f32 v60, v140, v141\n"
"v_cvt_pk_bf16_f32 v61, v142, v143\n"
"v_cvt_pk_bf16_f32 v62, v156, v157\n"
"v_cvt_pk_bf16_f32 v63, v158, v159\n"
"buffer_store_dwordx4 v[60:63], v248, s[52:55], s89 offen sc1\n"
"s_add_u32 s89, s89, 16384\n"
"v_mfma_f32_16x16x32_bf16 v[12:15], v[200:203], v[72:75], v[12:15]\n"
"v_mfma_f32_16x16x32_bf16 v[12:15], v[204:207], v[76:79], v[12:15]\n"
"v_mfma_f32_16x16x32_bf16 v[24:27], v[200:203], v[80:83], v[24:27]\n"
"v_mfma_f32_16x16x32_bf16 v[24:27], v[204:207], v[84:87], v[24:27]\n"
"ds_read_b64_tr_b16 v[200:201], v245 offset:64\n"
"ds_read_b64_tr_b16 v[202:203], v245 offset:8768\n"
"ds_read_b64_tr_b16 v[204:205], v245 offset:17472\n"
"ds_read_b64_tr_b16 v[206:207], v245 offset:26176\n"
"s_waitcnt lgkmcnt(6)\n"
"v_mfma_f32_16x16x32_bf16 v[4:7], v[192:195], v[64:67], v[4:7]\n"
"v_cvt_pk_bf16_f32 v48, v160, v161\n"
"v_cvt_pk_bf16_f32 v49, v162, v163\n"
"v_cvt_pk_bf16_f32 v50, v164, v165\n"
"v_cvt_pk_bf16_f32 v51, v166, v167\n"
"buffer_store_dwordx4 v[48:51], v248, s[52:55], s89 offen sc1\n"
"s_add_u32 s89, s89, 16384\n"
"s_waitcnt lgkmcnt(4)\n"
"v_mfma_f32_16x16x32_bf16 v[4:7], v[196:199], v[68:71], v[4:7]\n"
"v_mfma_f32_16x16x32_bf16 v[16:19], v[192:195], v[72:75], v[16:19]\n"
"v_mfma_f32_16x16x32_bf16 v[16:19], v[196:199], v[76:79], v[16:19]\n"
"v_mfma_f32_16x16x32_bf16 v[28:31], v[192:195], v[80:83], v[28:31]\n"
"v_mfma_f32_16x16x32_bf16 v[28:31], v[196:199], v[84:87], v[28:31]\n"
"v_cvt_pk_bf16_f32 v52, v168, v169\n"
"v_cvt_pk_bf16_f32 v53, v170, v171\n"
"v_cvt_pk_bf16_f32 v54, v172, v173\n"
"v_cvt_pk_bf16_f32 v55, v174, v175\n"
"buffer_store_dwordx4 v[52:55], v248, s[52:55], s89 offen sc1\n"
"s_add_u32 s89, s89, 16384\n"
"s_waitcnt lgkmcnt(2)\n"
"v_mfma_f32_16x16x32_bf16 v[8:11], v[200:203], v[64:67], v[8:11]\n"
"s_waitcnt lgkmcnt(0)\n"
"v_mfma_f32_16x16x32_bf16 v[8:11], v[204:207], v[68:71], v[8:11]\n"
"v_mfma_f32_16x16x32_bf16 v[20:23], v[200:203], v[72:75], v[20:23]\n"
"v_mfma_f32_16x16x32_bf16 v[20:23], v[204:207], v[76:79], v[20:23]\n"
"v_mfma_f32_16x16x32_bf16 v[32:35], v[200:203], v[80:83], v[32:35]\n"
"v_mfma_f32_16x16x32_bf16 v[32:35], v[204:207], v[84:87], v[32:35]\n"
"s_waitcnt lgkmcnt(0)\n"
"s_waitcnt vmcnt(0)\n"
"s_barrier\n"
"s_bfe_u32 s85, %8, 0x80008\n"
"s_lshl_b32 s85, s85, 10\n"
"s_mul_i32 s89, s87, 81920\n"
"s_add_u32 s89, s89, 1310720\n"
"s_add_u32 s89, s89, s85\n"
"v_cvt_pk_bf16_f32 v56, v0, v1\n"
"v_cvt_pk_bf16_f32 v57, v2, v3\n"
"v_cvt_pk_bf16_f32 v58, v12, v13\n"
"v_cvt_pk_bf16_f32 v59, v14, v15\n"
"buffer_store_dwordx4 v[56:59], v248, s[52:55], s89 offen sc1\n"
"s_add_u32 s89, s89, 16384\n"
"v_cvt_pk_bf16_f32 v60, v4, v5\n"
"v_cvt_pk_bf16_f32 v61, v6, v7\n"
"v_cvt_pk_bf16_f32 v62, v16, v17\n"
"v_cvt_pk_bf16_f32 v63, v18, v19\n"
"buffer_store_dwordx4 v[60:63], v248, s[52:55], s89 offen sc1\n"
"s_add_u32 s89, s89, 16384\n"
"v_cvt_pk_bf16_f32 v48, v8, v9\n"
"v_cvt_pk_bf16_f32 v49, v10, v11\n"
"v_cvt_pk_bf16_f32 v50, v20, v21\n"
"v_cvt_pk_bf16_f32 v51, v22, v23\n"
"buffer_store_dwordx4 v[48:51], v248, s[52:55], s89 offen sc1\n"
"s_add_u32 s89, s89, 16384\n"
"v_cvt_pk_bf16_f32 v52, v24, v25\n"
"v_cvt_pk_bf16_f32 v53, v26, v27\n"
"v_cvt_pk_bf16_f32 v54, v28, v29\n"
"v_cvt_pk_bf16_f32 v55, v30, v31\n"
"buffer_store_dwordx4 v[52:55], v248, s[52:55], s89 offen sc1\n"
"s_add_u32 s89, s89, 16384\n"
"v_cvt_pk_bf16_f32 v56, v32, v33\n"
"v_cvt_pk_bf16_f32 v57, v34, v35\n"
"v_cvt_pk_bf16_f32 v58, v32, v33\n"
"v_cvt_pk_bf16_f32 v59, v34, v35\n"
"buffer_store_dwordx4 v[56:59], v248, s[52:55], s89 offen sc1\n"
"s_add_u32 s89, s89, 16384\n"
"s_bfe_u32 s84, %8, 0x80008\n"
"s_lshl_b32 s66, s86, 4\n"
"s_add_u32 s66, s66, s84\n"
"s_sub_u32 s66, s66, 8\n"
"s_sub_u32 s56, s66, 80\n"
"s_mul_i32 s60, s56, 52\n"
"s_lshr_b32 s60, s60, 8\n"
"s_mul_i32 s67, s60, 5\n"
"s_sub_u32 s61, s56, s67\n"
"s_mul_i32 s60, s60, 7\n"
"s_add_u32 s60, s60, 4\n"
"s_add_u32 s57, s60, s61\n"
"s_mov_b32 s58, s57\n"
"s_mov_b32 s62, 4\n"
"s_mov_b32 s63, 5\n"
"s_add_u32 s59, s60, 1\n"
"s_cmp_eq_u32 s61, 3\n"
"s_cselect_b32 s57, s60, s57\n"
"s_cselect_b32 s58, s59, s58\n"
"s_add_u32 s59, s60, 2\n"
"s_cmp_eq_u32 s61, 4\n"
"s_cselect_b32 s57, s59, s57\n"
"s_cselect_b32 s58, s59, s58\n"
"s_cmp_ge_u32 s61, 3\n"
"s_cselect_b32 s62, 6, s62\n"
"s_cselect_b32 s63, 6, s63\n"
"s_sub_u32 s56, s66, 56\n"
"s_mul_i32 s60, s56, 43\n"
"s_lshr_b32 s60, s60, 8\n"
"s_mul_i32 s67, s60, 6\n"
"s_sub_u32 s61, s56, s67\n"
"s_mul_i32 s60, s60, 7\n"
"s_add_u32 s60, s60, 4\n"
"s_cmp_ge_u32 s61, 3\n"
"s_cselect_b32 s56, 2, 0\n"
"s_cselect_b32 s67, 3, 0\n"
"s_sub_u32 s61, s61, s67\n"
"s_add_u32 s67, s60, s61\n"
"s_add_u32 s59, s56, 1\n"
"s_cmp_lt_u32 s66, 80\n"
"s_cselect_b32 s57, s67, s57\n"
"s_cselect_b32 s58, s67, s58\n"
"s_cselect_b32 s62, s56, s62\n"
"s_cselect_b32 s63, s59, s63\n"
"s_mul_i32 s62, s62, 28672\n"
"s_lshl_b32 s57, s57, 5\n"
"s_add_u32 s57, s57, s62\n"
"s_mul_i32 s63, s63, 28672\n"
"s_lshl_b32 s58, s58, 5\n"
"s_add_u32 s58, s58, s63\n"
"s_cmp_lt_u32 s66, 100\n"
"s_cselect_b32 s57, s57, -1\n"
"s_cmp_eq_u32 s86, 7\n"
"s_cselect_b32 s57, -1, s57\n"
"s_lshl_b32 s66, s66, 14\n"
"s_waitcnt vmcnt(0)\n"
"s_barrier\n"
"s_cmp_eq_u32 s57, -1\n"
"s_cbranch_scc1 L_doneB\n"
"s_mov_b32 s83, 0x10000\n"
"s_mov_b32 s84, 4096\n"
"buffer_load_dword v252, v251, s[72:75], s84 offen sc1\n"
"s_waitcnt vmcnt(0)\n"
"L_pollBp:\n"
"v_cmp_ne_u32 vcc, s88, v252\n"
"s_nop 4\n"
"s_cbranch_vccz L_seenBp\n"
"s_sleep 1\n"
"s_mov_b32 s84, 4096\n"
"buffer_load_dword v252, v251, s[72:75], s84 offen sc1\n"
"s_waitcnt vmcnt(0)\n"
"s_sub_u32 s83, s83, 1\n"
"s_cmp_lg_u32 s83, 0\n"
"s_cbranch_scc1 L_pollBp\n"
"L_seenBp:\n"
"s_bfe_u32 s84, %8, 0x80008\n"
"s_lshl_b32 s84, s84, 4\n"
"s_add_u32 s84, s84, s86\n"
"s_cmp_lg_u32 s84, 4\n"
"s_cbranch_scc1 L_noepB\n"
"v_mov_b32 v250, s88\n"
"v_mov_b32 v252, 0\n"
"s_mov_b32 s84, 8192\n"
"s_mov_b64 exec, 1\n"
"buffer_store_dword v250, v252, s[72:75], s84 offen sc1\n"
"s_mov_b64 exec, -1\n"
"L_noepB:\n"
"buffer_load_dwordx4 v[64:67], v248, s[52:55], s66 offen offset:0 sc1\n"
"buffer_load_dwordx4 v[68:71], v248, s[52:55], s66 offen offset:1024 sc1\n"
"buffer_load_dwordx4 v[72:75], v248, s[52:55], s66 offen offset:2048 sc1\n"
"buffer_load_dwordx4 v[76:79], v248, s[52:55], s66 offen offset:3072 sc1\n"
"s_add_u32 s66, s66, 4096\n"
"buffer_load_dwordx4 v[80:83], v248, s[52:55], s66 offen offset:0 sc1\n"
"buffer_load_dwordx4 v[84:87], v248, s[52:55], s66 offen offset:1024 sc1\n"
"buffer_load_dwordx4 v[88:91], v248, s[52:55], s66 offen offset:2048 sc1\n"
"buffer_load_dwordx4 v[92:95], v248, s[52:55], s66 offen offset:3072 sc1\n"
"s_add_u32 s66, s66, 4096\n"
"buffer_load_dwordx4 v[96:99], v248, s[52:55], s66 offen offset:0 sc1\n"
"buffer_load_dwordx4 v[100:103], v248, s[52:55], s66 offen offset:1024 sc1\n"
"buffer_load_dwordx4 v[104:107], v248, s[52:55], s66 offen offset:2048 sc1\n"
"buffer_load_dwordx4 v[108:111], v248, s[52:55], s66 offen offset:3072 sc1\n"
"s_add_u32 s66, s66, 4096\n"
"buffer_load_dwordx4 v[112:115], v248, s[52:55], s66 offen offset:0 sc1\n"
"buffer_load_dwordx4 v[116:119], v248, s[52:55], s66 offen offset:1024 sc1\n"
"buffer_load_dwordx4 v[120:123], v248, s[52:55], s66 offen offset:2048 sc1\n"
"buffer_load_dwordx4 v[124:127], v248, s[52:55], s66 offen offset:3072 sc1\n"
"s_mov_b64 s[76:77], %7\n"
"s_and_b32 s77, s77, 0xffff\n"
"s_mov_b32 s78, 199808\n"
"s_mov_b32 s79, 0x20000\n"
"v_lshrrev_b32 v136, 4, v248\n"
"v_and_b32 v137, 15, v136\n"
"v_mul_u32_u24 v137, 1792, v137\n"
"v_lshrrev_b32 v136, 4, v136\n"
"v_lshl_add_u32 v137, v136, 3, v137\n"
"s_mov_b32 s84, 0x00003f80\n"
"s_mov_b32 s85, 0x3f800000\n"
"v_mov_b64 v[128:129], 0\n"
"v_mov_b64 v[130:131], 0\n"
"v_mov_b64 v[132:133], 0\n"
"v_mov_b64 v[134:135], 0\n"
"s_waitcnt vmcnt(15)\n"
"v_dot2_f32_bf16 v128, v64, s84, v128\n"
"v_dot2_f32_bf16 v129, v64, s85, v129\n"
"v_dot2_f32_bf16 v130, v65, s84, v130\n"
"v_dot2_f32_bf16 v131, v65, s85, v131\n"
"v_dot2_f32_bf16 v132, v66, s84, v132\n"
"v_dot2_f32_bf16 v133, v66, s85, v133\n"
"v_dot2_f32_bf16 v134, v67, s84, v134\n"
"v_dot2_f32_bf16 v135, v67, s85, v135\n"
"s_waitcnt vmcnt(14)\n"
"v_dot2_f32_bf16 v128, v68, s84, v128\n"
"v_dot2_f32_bf16 v129, v68, s85, v129\n"
"v_dot2_f32_bf16 v130, v69, s84, v130\n"
"v_dot2_f32_bf16 v131, v69, s85, v131\n"
"v_dot2_f32_bf16 v132, v70, s84, v132\n"
"v_dot2_f32_bf16 v133, v70, s85, v133\n"
"v_dot2_f32_bf16 v134, v71, s84, v134\n"
"v_dot2_f32_bf16 v135, v71, s85, v135\n"
"s_waitcnt vmcnt(13)\n"
"v_dot2_f32_bf16 v128, v72, s84, v128\n"
"v_dot2_f32_bf16 v129, v72, s85, v129\n"
"v_dot2_f32_bf16 v130, v73, s84, v130\n"
"v_dot2_f32_bf16 v131, v73, s85, v131\n"
"v_dot2_f32_bf16 v132, v74, s84, v132\n"
"v_dot2_f32_bf16 v133, v74, s85, v133\n"
"v_dot2_f32_bf16 v134, v75, s84, v134\n"
"v_dot2_f32_bf16 v135, v75, s85, v135\n"
"s_waitcnt vmcnt(12)\n"
"v_dot2_f32_bf16 v128, v76, s84, v128\n"
"v_dot2_f32_bf16 v129, v76, s85, v129\n"
"v_dot2_f32_bf16 v130, v77, s84, v130\n"
"v_dot2_f32_bf16 v131, v77, s85, v131\n"
"v_dot2_f32_bf16 v132, v78, s84, v132\n"
"v_dot2_f32_bf16 v133, v78, s85, v133\n"
"v_dot2_f32_bf16 v134, v79, s84, v134\n"
"v_dot2_f32_bf16 v135, v79, s85, v135\n"
"s_waitcnt vmcnt(11)\n"
"v_dot2_f32_bf16 v128, v80, s84, v128\n"
"v_dot2_f32_bf16 v129, v80, s85, v129\n"
"v_dot2_f32_bf16 v130, v81, s84, v130\n"
"v_dot2_f32_bf16 v131, v81, s85, v131\n"
"v_dot2_f32_bf16 v132, v82, s84, v132\n"
"v_dot2_f32_bf16 v133, v82, s85, v133\n"
"v_dot2_f32_bf16 v134, v83, s84, v134\n"
"v_dot2_f32_bf16 v135, v83, s85, v135\n"
"s_waitcnt vmcnt(10)\n"
"v_dot2_f32_bf16 v128, v84, s84, v128\n"
"v_dot2_f32_bf16 v129, v84, s85, v129\n"
"v_dot2_f32_bf16 v130, v85, s84, v130\n"
"v_dot2_f32_bf16 v131, v85, s85, v131\n"
"v_dot2_f32_bf16 v132, v86, s84, v132\n"
"v_dot2_f32_bf16 v133, v86, s85, v133\n"
"v_dot2_f32_bf16 v134, v87, s84, v134\n"
"v_dot2_f32_bf16 v135, v87, s85, v135\n"
"s_waitcnt vmcnt(9)\n"
"v_dot2_f32_bf16 v128, v88, s84, v128\n"
"v_dot2_f32_bf16 v129, v88, s85, v129\n"
"v_dot2_f32_bf16 v130, v89, s84, v130\n"
"v_dot2_f32_bf16 v131, v89, s85, v131\n"
"v_dot2_f32_bf16 v132, v90, s84, v132\n"
"v_dot2_f32_bf16 v133, v90, s85, v133\n"
"v_dot2_f32_bf16 v134, v91, s84, v134\n"
"v_dot2_f32_bf16 v135, v91, s85, v135\n"
"s_waitcnt vmcnt(8)\n"
"v_dot2_f32_bf16 v128, v92, s84, v128\n"
"v_dot2_f32_bf16 v129, v92, s85, v129\n"
"v_dot2_f32_bf16 v130, v93, s84, v130\n"
"v_dot2_f32_bf16 v131, v93, s85, v131\n"
"v_dot2_f32_bf16 v132, v94, s84, v132\n"
"v_dot2_f32_bf16 v133, v94, s85, v133\n"
"v_dot2_f32_bf16 v134, v95, s84, v134\n"
"v_dot2_f32_bf16 v135, v95, s85, v135\n"
"s_waitcnt vmcnt(7)\n"
"v_dot2_f32_bf16 v128, v96, s84, v128\n"
"v_dot2_f32_bf16 v129, v96, s85, v129\n"
"v_dot2_f32_bf16 v130, v97, s84, v130\n"
"v_dot2_f32_bf16 v131, v97, s85, v131\n"
"v_dot2_f32_bf16 v132, v98, s84, v132\n"
"v_dot2_f32_bf16 v133, v98, s85, v133\n"
"v_dot2_f32_bf16 v134, v99, s84, v134\n"
"v_dot2_f32_bf16 v135, v99, s85, v135\n"
"s_waitcnt vmcnt(6)\n"
"v_dot2_f32_bf16 v128, v100, s84, v128\n"
"v_dot2_f32_bf16 v129, v100, s85, v129\n"
"v_dot2_f32_bf16 v130, v101, s84, v130\n"
"v_dot2_f32_bf16 v131, v101, s85, v131\n"
"v_dot2_f32_bf16 v132, v102, s84, v132\n"
"v_dot2_f32_bf16 v133, v102, s85, v133\n"
"v_dot2_f32_bf16 v134, v103, s84, v134\n"
"v_dot2_f32_bf16 v135, v103, s85, v135\n"
"s_waitcnt vmcnt(5)\n"
"v_dot2_f32_bf16 v128, v104, s84, v128\n"
"v_dot2_f32_bf16 v129, v104, s85, v129\n"
"v_dot2_f32_bf16 v130, v105, s84, v130\n"
"v_dot2_f32_bf16 v131, v105, s85, v131\n"
"v_dot2_f32_bf16 v132, v106, s84, v132\n"
"v_dot2_f32_bf16 v133, v106, s85, v133\n"
"v_dot2_f32_bf16 v134, v107, s84, v134\n"
"v_dot2_f32_bf16 v135, v107, s85, v135\n"
"s_waitcnt vmcnt(4)\n"
"v_dot2_f32_bf16 v128, v108, s84, v128\n"
"v_dot2_f32_bf16 v129, v108, s85, v129\n"
"v_dot2_f32_bf16 v130, v109, s84, v130\n"
"v_dot2_f32_bf16 v131, v109, s85, v131\n"
"v_dot2_f32_bf16 v132, v110, s84, v132\n"
"v_dot2_f32_bf16 v133, v110, s85, v133\n"
"v_dot2_f32_bf16 v134, v111, s84, v134\n"
"v_dot2_f32_bf16 v135, v111, s85, v135\n"
"s_waitcnt vmcnt(3)\n"
"v_dot2_f32_bf16 v128, v112, s84, v128\n"
"v_dot2_f32_bf16 v129, v112, s85, v129\n"
"v_dot2_f32_bf16 v130, v113, s84, v130\n"
"v_dot2_f32_bf16 v131, v113, s85, v131\n"
"v_dot2_f32_bf16 v132, v114, s84, v132\n"
"v_dot2_f32_bf16 v133, v114, s85, v133\n"
"v_dot2_f32_bf16 v134, v115, s84, v134\n"
"v_dot2_f32_bf16 v135, v115, s85, v135\n"
"s_waitcnt vmcnt(2)\n"
"v_dot2_f32_bf16 v128, v116, s84, v128\n"
"v_dot2_f32_bf16 v129, v116, s85, v129\n"
"v_dot2_f32_bf16 v130, v117, s84, v130\n"
"v_dot2_f32_bf16 v131, v117, s85, v131\n"
"v_dot2_f32_bf16 v132, v118, s84, v132\n"
"v_dot2_f32_bf16 v133, v118, s85, v133\n"
"v_dot2_f32_bf16 v134, v119, s84, v134\n"
"v_dot2_f32_bf16 v135, v119, s85, v135\n"
"s_waitcnt vmcnt(1)\n"
"v_dot2_f32_bf16 v128, v120, s84, v128\n"
"v_dot2_f32_bf16 v129, v120, s85, v129\n"
"v_dot2_f32_bf16 v130, v121, s84, v130\n"
"v_dot2_f32_bf16 v131, v121, s85, v131\n"
"v_dot2_f32_bf16 v132, v122, s84, v132\n"
"v_dot2_f32_bf16 v133, v122, s85, v133\n"
"v_dot2_f32_bf16 v134, v123, s84, v134\n"
"v_dot2_f32_bf16 v135, v123, s85, v135\n"
"s_waitcnt vmcnt(0)\n"
"v_dot2_f32_bf16 v128, v124, s84, v128\n"
"v_dot2_f32_bf16 v129, v124, s85, v129\n"
"v_dot2_f32_bf16 v130, v125, s84, v130\n"
"v_dot2_f32_bf16 v131, v125, s85, v131\n"
"v_dot2_f32_bf16 v132, v126, s84, v132\n"
"v_dot2_f32_bf16 v133, v126, s85, v133\n"
"v_dot2_f32_bf16 v134, v127, s84, v134\n"
"v_dot2_f32_bf16 v135, v127, s85, v135\n"
"v_cvt_pk_bf16_f32 v140, v128, v129\n"
"v_cvt_pk_bf16_f32 v141, v130, v131\n"
"v_cvt_pk_bf16_f32 v142, v132, v133\n"
"v_cvt_pk_bf16_f32 v143, v134, v135\n"
"buffer_store_dwordx2 v[140:141], v137, s[76:79], s57 offen\n"
"buffer_store_dwordx2 v[142:143], v137, s[76:79], s58 offen\n"
"L_doneB:\n"
"s_waitcnt vmcnt(0)\n"
"s_endpgm\n"
"L_fbB:\n"
"s_mov_b32 s91, 1\n"
"s_branch L_start\n"
"L_safeB:\n"
"v_mov_b32 v252, 0xff800000\n"
"v_mov_b32 v253, 0xff800000\n"
"v_mov_b32 v254, 0xff800000\n"
"s_mov_b32 s83, 16\n"
"L_mxB:\n"
"s_waitcnt vmcnt(8)\n"
"s_barrier\n"
"ds_read_b128 v[208:211], v244 offset:34816\n"
"ds_read_b128 v[212:215], v244 offset:34880\n"
"ds_read_b128 v[216:219], v244 offset:34944\n"
"ds_read_b128 v[220:223], v244 offset:35008\n"
"ds_read_b128 v[224:227], v244 offset:35072\n"
"ds_read_b128 v[228:231], v244 offset:35136\n"
"ds_read_b128 v[232:235], v244 offset:35200\n"
"ds_read_b128 v[236:239], v244 offset:35264\n"
"s_waitcnt lgkmcnt(7)\n"
"v_mfma_f32_16x16x32_bf16 v[192:195], v[208:211], v[0:3], 0\n"
"s_waitcnt lgkmcnt(6)\n"
"v_mfma_f32_16x16x32_bf16 v[192:195], v[212:215], v[4:7], v[192:195]\n"
"s_waitcnt lgkmcnt(5)\n"
"v_mfma_f32_16x16x32_bf16 v[192:195], v[216:219], v[8:11], v[192:195]\n"
"s_waitcnt lgkmcnt(4)\n"
"v_mfma_f32_16x16x32_bf16 v[192:195], v[220:223], v[12:15], v[192:195]\n"
"s_waitcnt lgkmcnt(3)\n"
"v_mfma_f32_16x16x32_bf16 v[192:195], v[224:227], v[16:19], v[192:195]\n"
"s_waitcnt lgkmcnt(2)\n"
"v_mfma_f32_16x16x32_bf16 v[192:195], v[228:231], v[20:23], v[192:195]\n"
"s_waitcnt lgkmcnt(1)\n"
"v_mfma_f32_16x16x32_bf16 v[192:195], v[232:235], v[24:27], v[192:195]\n"
"s_waitcnt lgkmcnt(0)\n"
"v_mfma_f32_16x16x32_bf16 v[192:195], v[236:239], v[28:31], v[192:195]\n"
"v_mfma_f32_16x16x32_bf16 v[196:199], v[208:211], v[32:35], 0\n"
"v_mfma_f32_16x16x32_bf16 v[196:199], v[212:215], v[36:39], v[196:199]\n"
"v_mfma_f32_16x16x32_bf16 v[196:199], v[216:219], v[40:43], v[196:199]\n"
"v_mfma_f32_16x16x32_bf16 v[196:199], v[220:223], v[44:47], v[196:199]\n"
"v_mfma_f32_16x16x32_bf16 v[196:199], v[224:227], v[48:51], v[196:199]\n"
"v_mfma_f32_16x16x32_bf16 v[196:199], v[228:231], v[52:55], v[196:199]\n"
"v_mfma_f32_16x16x32_bf16 v[196:199], v[232:235], v[56:59], v[196:199]\n"
"v_mfma_f32_16x16x32_bf16 v[196:199], v[236:239], v[60:63], v[196:199]\n"
"v_mfma_f32_16x16x32_bf16 v[200:203], v[208:211], v[64:67], 0\n"
"v_mfma_f32_16x16x32_bf16 v[200:203], v[212:215], v[68:71], v[200:203]\n"
"v_mfma_f32_16x16x32_bf16 v[200:203], v[216:219], v[72:75], v[200:203]\n"
"v_mfma_f32_16x16x32_bf16 v[200:203], v[220:223], v[76:79], v[200:203]\n"
"v_mfma_f32_16x16x32_bf16 v[200:203], v[224:227], v[80:83], v[200:203]\n"
"v_mfma_f32_16x16x32_bf16 v[200:203], v[228:231], v[84:87], v[200:203]\n"
"v_mfma_f32_16x16x32_bf16 v[200:203], v[232:235], v[88:91], v[200:203]\n"
"v_mfma_f32_16x16x32_bf16 v[200:203], v[236:239], v[92:95], v[200:203]\n"
"v_max3_f32 v252, v192, v193, v252\n"
"v_max3_f32 v252, v194, v195, v252\n"
"v_max3_f32 v253, v196, v197, v253\n"
"v_max3_f32 v253, v198, v199, v253\n"
"s_nop 3\n"
"v_max3_f32 v254, v200, v201, v254\n"
"v_max3_f32 v254, v202, v203, v254\n"
"s_waitcnt lgkmcnt(0)\n"
"s_barrier\n"
"s_add_u32 m0, s56, 34816\n"
"s_nop 0\n"
"buffer_load_dwordx4 v249, s[44:47], s64 offen nt lds\n"
"s_add_u32 m0, s57, 34816\n"
"s_nop 0\n"
"buffer_load_dwordx4 v249, s[44:47], s65 offen nt lds\n"
"s_add_u32 m0, s58, 34816\n"
"s_nop 0\n"
"buffer_load_dwordx4 v249, s[44:47], s66 offen nt lds\n"
"s_add_u32 m0, s59, 34816\n"
"s_nop 0\n"
"buffer_load_dwordx4 v249, s[44:47], s67 offen nt lds\n"
"s_add_u32 m0, s60, 34816\n"
"s_nop 0\n"
"buffer_load_dwordx4 v249, s[44:47], s68 offen nt lds\n"
"s_add_u32 m0, s61, 34816\n"
"s_nop 0\n"
"buffer_load_dwordx4 v249, s[44:47], s69 offen nt lds\n"
"s_add_u32 m0, s62, 34816\n"
"s_nop 0\n"
"buffer_load_dwordx4 v249, s[44:47], s70 offen nt lds\n"
"s_add_u32 m0, s63, 34816\n"
"s_nop 0\n"
"buffer_load_dwordx4 v249, s[44:47], s71 offen nt lds\n"
"v_add_u32 v249, 32768, v249\n"
"s_waitcnt vmcnt(8)\n"
"s_barrier\n"
"ds_read_b128 v[208:211], v244 offset:0\n"
"ds_read_b128 v[212:215], v244 offset:64\n"
"ds_read_b128 v[216:219], v244 offset:128\n"
"ds_read_b128 v[220:223], v244 offset:192\n"
"ds_read_b128 v[224:227], v244 offset:256\n"
"ds_read_b128 v[228:231], v244 offset:320\n"
"ds_read_b128 v[232:235], v244 offset:384\n"
"ds_read_b128 v[236:239], v244 offset:448\n"
"s_waitcnt lgkmcnt(7)\n"
"v_mfma_f32_16x16x32_bf16 v[192:195], v[208:211], v[0:3], 0\n"
"s_waitcnt lgkmcnt(6)\n"
"v_mfma_f32_16x16x32_bf16 v[192:195], v[212:215], v[4:7], v[192:195]\n"
"s_waitcnt lgkmcnt(5)\n"
"v_mfma_f32_16x16x32_bf16 v[192:195], v[216:219], v[8:11], v[192:195]\n"
"s_waitcnt lgkmcnt(4)\n"
"v_mfma_f32_16x16x32_bf16 v[192:195], v[220:223], v[12:15], v[192:195]\n"
"s_waitcnt lgkmcnt(3)\n"
"v_mfma_f32_16x16x32_bf16 v[192:195], v[224:227], v[16:19], v[192:195]\n"
"s_waitcnt lgkmcnt(2)\n"
"v_mfma_f32_16x16x32_bf16 v[192:195], v[228:231], v[20:23], v[192:195]\n"
"s_waitcnt lgkmcnt(1)\n"
"v_mfma_f32_16x16x32_bf16 v[192:195], v[232:235], v[24:27], v[192:195]\n"
"s_waitcnt lgkmcnt(0)\n"
"v_mfma_f32_16x16x32_bf16 v[192:195], v[236:239], v[28:31], v[192:195]\n"
"v_mfma_f32_16x16x32_bf16 v[196:199], v[208:211], v[32:35], 0\n"
"v_mfma_f32_16x16x32_bf16 v[196:199], v[212:215], v[36:39], v[196:199]\n"
"v_mfma_f32_16x16x32_bf16 v[196:199], v[216:219], v[40:43], v[196:199]\n"
"v_mfma_f32_16x16x32_bf16 v[196:199], v[220:223], v[44:47], v[196:199]\n"
"v_mfma_f32_16x16x32_bf16 v[196:199], v[224:227], v[48:51], v[196:199]\n"
"v_mfma_f32_16x16x32_bf16 v[196:199], v[228:231], v[52:55], v[196:199]\n"
"v_mfma_f32_16x16x32_bf16 v[196:199], v[232:235], v[56:59], v[196:199]\n"
"v_mfma_f32_16x16x32_bf16 v[196:199], v[236:239], v[60:63], v[196:199]\n"
"v_mfma_f32_16x16x32_bf16 v[200:203], v[208:211], v[64:67], 0\n"
"v_mfma_f32_16x16x32_bf16 v[200:203], v[212:215], v[68:71], v[200:203]\n"
"v_mfma_f32_16x16x32_bf16 v[200:203], v[216:219], v[72:75], v[200:203]\n"
"v_mfma_f32_16x16x32_bf16 v[200:203], v[220:223], v[76:79], v[200:203]\n"
"v_mfma_f32_16x16x32_bf16 v[200:203], v[224:227], v[80:83], v[200:203]\n"
"v_mfma_f32_16x16x32_bf16 v[200:203], v[228:231], v[84:87], v[200:203]\n"
"v_mfma_f32_16x16x32_bf16 v[200:203], v[232:235], v[88:91], v[200:203]\n"
"v_mfma_f32_16x16x32_bf16 v[200:203], v[236:239], v[92:95], v[200:203]\n"
"v_max3_f32 v252, v192, v193, v252\n"
"v_max3_f32 v252, v194, v195, v252\n"
"v_max3_f32 v253, v196, v197, v253\n"
"v_max3_f32 v253, v198, v199, v253\n"
"s_nop 3\n"
"v_max3_f32 v254, v200, v201, v254\n"
"v_max3_f32 v254, v202, v203, v254\n"
"s_waitcnt lgkmcnt(0)\n"
"s_barrier\n"
"s_add_u32 m0, s56, 0\n"
"s_nop 0\n"
"buffer_load_dwordx4 v249, s[44:47], s64 offen nt lds\n"
"s_add_u32 m0, s57, 0\n"
"s_nop 0\n"
"buffer_load_dwordx4 v249, s[44:47], s65 offen nt lds\n"
"s_add_u32 m0, s58, 0\n"
"s_nop 0\n"
"buffer_load_dwordx4 v249, s[44:47], s66 offen nt lds\n"
"s_add_u32 m0, s59, 0\n"
"s_nop 0\n"
"buffer_load_dwordx4 v249, s[44:47], s67 offen nt lds\n"
"s_add_u32 m0, s60, 0\n"
"s_nop 0\n"
"buffer_load_dwordx4 v249, s[44:47], s68 offen nt lds\n"
"s_add_u32 m0, s61, 0\n"
"s_nop 0\n"
"buffer_load_dwordx4 v249, s[44:47], s69 offen nt lds\n"
"s_add_u32 m0, s62, 0\n"
"s_nop 0\n"
"buffer_load_dwordx4 v249, s[44:47], s70 offen nt lds\n"
"s_add_u32 m0, s63, 0\n"
"s_nop 0\n"
"buffer_load_dwordx4 v249, s[44:47], s71 offen nt lds\n"
"v_add_u32 v249, 32768, v249\n"
"s_sub_u32 s83, s83, 1\n"
"s_cmp_lg_u32 s83, 0\n"
"s_cbranch_scc1 L_mxB\n"
"v_mov_b32 v208, v252\n"
"s_nop 1\n"
"v_permlane16_swap_b32 v252, v208\n"
"v_max_f32 v252, v252, v208\n"
"v_mov_b32 v208, v252\n"
"s_nop 1\n"
"v_permlane32_swap_b32 v252, v208\n"
"v_max_f32 v252, v252, v208\n"
"v_mov_b32 v209, v253\n"
"s_nop 1\n"
"v_permlane16_swap_b32 v253, v209\n"
"v_max_f32 v253, v253, v209\n"
"v_mov_b32 v209, v253\n"
"s_nop 1\n"
"v_permlane32_swap_b32 v253, v209\n"
"v_max_f32 v253, v253, v209\n"
"v_mov_b32 v210, v254\n"
"s_nop 1\n"
"v_permlane16_swap_b32 v254, v210\n"
"v_max_f32 v254, v254, v210\n"
"v_mov_b32 v210, v254\n"
"s_nop 1\n"
"v_permlane32_swap_b32 v254, v210\n"
"v_max_f32 v254, v254, v210\n"
"v_mbcnt_lo_u32_b32 v216, -1, 0\n"
"v_mbcnt_hi_u32_b32 v216, -1, v216\n"
"v_and_b32 v216, 15, v216\n"
"v_lshlrev_b32 v216, 4, v216\n"
"v_add_u32 v217, 161792, v216\n"
"s_lshl_b32 s84, s87, 2\n"
"v_add_u32 v216, s84, v217\n"
"ds_write_b32 v216, v252 offset:1024\n"
"ds_write_b32 v216, v253 offset:1280\n"
"ds_write_b32 v216, v254 offset:1536\n"
"s_waitcnt vmcnt(0) lgkmcnt(0)\n"
"s_barrier\n"
"ds_read_b128 v[220:223], v217 offset:1024\n"
"ds_read_b128 v[224:227], v217 offset:1280\n"
"ds_read_b128 v[228:231], v217 offset:1536\n"
"s_waitcnt lgkmcnt(2)\n"
"v_max3_f32 v252, v220, v221, v222\n"
"v_max_f32 v252, v252, v223\n"
"s_waitcnt lgkmcnt(1)\n"
"v_max3_f32 v253, v224, v225, v226\n"
"v_max_f32 v253, v253, v227\n"
"s_waitcnt lgkmcnt(0)\n"
"v_max3_f32 v254, v228, v229, v230\n"
"v_max_f32 v254, v254, v231\n"
"v_mbcnt_lo_u32_b32 v208, -1, 0\n"
"v_mbcnt_hi_u32_b32 v208, -1, v208\n"
"v_lshrrev_b32 v209, 5, v208\n"
"v_and_b32 v208, 31, v208\n"
"v_lshlrev_b32 v210, 1, v209\n"
"v_xor_b32 v208, v208, v210\n"
"v_lshlrev_b32 v208, 4, v208\n"
"v_lshl_add_u32 v249, v209, 9, v208\n"
"s_add_u32 m0, s56, 34816\n"
"s_nop 0\n"
"buffer_load_dwordx4 v249, s[44:47], s64 offen nt lds\n"
"s_add_u32 m0, s57, 34816\n"
"s_nop 0\n"
"buffer_load_dwordx4 v249, s[44:47], s65 offen nt lds\n"
"s_add_u32 m0, s58, 34816\n"
"s_nop 0\n"
"buffer_load_dwordx4 v249, s[44:47], s66 offen nt lds\n"
"s_add_u32 m0, s59, 34816\n"
"s_nop 0\n"
"buffer_load_dwordx4 v249, s[44:47], s67 offen nt lds\n"
"s_add_u32 m0, s60, 34816\n"
"s_nop 0\n"
"buffer_load_dwordx4 v249, s[44:47], s68 offen nt lds\n"
"s_add_u32 m0, s61, 34816\n"
"s_nop 0\n"
"buffer_load_dwordx4 v249, s[44:47], s69 offen nt lds\n"
"s_add_u32 m0, s62, 34816\n"
"s_nop 0\n"
"buffer_load_dwordx4 v249, s[44:47], s70 offen nt lds\n"
"s_add_u32 m0, s63, 34816\n"
"s_nop 0\n"
"buffer_load_dwordx4 v249, s[44:47], s71 offen nt lds\n"
"v_add_u32 v249, 32768, v249\n"
"s_add_u32 m0, s56, 0\n"
"s_nop 0\n"
"buffer_load_dwordx4 v249, s[44:47], s64 offen nt lds\n"
"s_add_u32 m0, s57, 0\n"
"s_nop 0\n"
"buffer_load_dwordx4 v249, s[44:47], s65 offen nt lds\n"
"s_add_u32 m0, s58, 0\n"
"s_nop 0\n"
"buffer_load_dwordx4 v249, s[44:47], s66 offen nt lds\n"
"s_add_u32 m0, s59, 0\n"
"s_nop 0\n"
"buffer_load_dwordx4 v249, s[44:47], s67 offen nt lds\n"
"s_add_u32 m0, s60, 0\n"
"s_nop 0\n"
"buffer_load_dwordx4 v249, s[44:47], s68 offen nt lds\n"
"s_add_u32 m0, s61, 0\n"
"s_nop 0\n"
"buffer_load_dwordx4 v249, s[44:47], s69 offen nt lds\n"
"s_add_u32 m0, s62, 0\n"
"s_nop 0\n"
"buffer_load_dwordx4 v249, s[44:47], s70 offen nt lds\n"
"s_add_u32 m0, s63, 0\n"
"s_nop 0\n"
"buffer_load_dwordx4 v249, s[44:47], s71 offen nt lds\n"
"v_add_u32 v249, 32768, v249\n"
"v_mov_b64 v[128:129], 0\n"
"v_mov_b64 v[130:131], 0\n"
"v_mov_b64 v[132:133], 0\n"
"v_mov_b64 v[134:135], 0\n"
"v_mov_b64 v[136:137], 0\n"
"v_mov_b64 v[138:139], 0\n"
"v_mov_b64 v[140:141], 0\n"
"v_mov_b64 v[142:143], 0\n"
"v_mov_b64 v[144:145], 0\n"
"v_mov_b64 v[146:147], 0\n"
"v_mov_b64 v[148:149], 0\n"
"v_mov_b64 v[150:151], 0\n"
"v_mov_b64 v[152:153], 0\n"
"v_mov_b64 v[154:155], 0\n"
"v_mov_b64 v[156:157], 0\n"
"v_mov_b64 v[158:159], 0\n"
"v_mov_b64 v[160:161], 0\n"
"v_mov_b64 v[162:163], 0\n"
"v_mov_b64 v[164:165], 0\n"
"v_mov_b64 v[166:167], 0\n"
"v_mov_b64 v[168:169], 0\n"
"v_mov_b64 v[170:171], 0\n"
"v_mov_b64 v[172:173], 0\n"
"v_mov_b64 v[174:175], 0\n"
"v_mov_b32 v240, 0\n"
"v_mov_b32 v241, 0\n"
"v_mov_b32 v242, 0\n"
"v_mov_b64 v[200:201], 0\n"
"v_mov_b64 v[202:203], 0\n"
"v_mov_b64 v[204:205], 0\n"
"v_mov_b64 v[206:207], 0\n"
"v_mov_b64 v[224:225], 0\n"
"v_mov_b64 v[226:227], 0\n"
"v_mov_b64 v[228:229], 0\n"
"v_mov_b64 v[230:231], 0\n"
"v_mov_b64 v[232:233], 0\n"
"v_mov_b64 v[234:235], 0\n"
"v_mov_b64 v[236:237], 0\n"
"v_mov_b64 v[238:239], 0\n"
"s_waitcnt vmcnt(8)\n"
"s_waitcnt lgkmcnt(0)\n"
"s_barrier\n"
"s_barrier\n"
"s_mov_b32 s83, 15\n"
"L_loopBs:\n"
"s_setprio 1\n"
"v_mfma_f32_16x16x32_bf16 v[172:175], v[200:203], v[224:227], v[172:175]\n"
"ds_read_b128 v[208:211], v244 offset:34816\n"
"v_mfma_f32_16x16x32_bf16 v[172:175], v[204:207], v[228:231], v[172:175]\n"
"ds_read_b128 v[212:215], v244 offset:34880\n"
"ds_read_b128 v[216:219], v244 offset:34944\n"
"ds_read_b128 v[220:223], v244 offset:35008\n"
"ds_read_b128 v[224:227], v244 offset:35072\n"
"ds_read_b128 v[228:231], v244 offset:35136\n"
"ds_read_b128 v[232:235], v244 offset:35200\n"
"ds_read_b128 v[236:239], v244 offset:35264\n"
"s_waitcnt lgkmcnt(7)\n"
"v_mfma_f32_16x16x32_bf16 v[192:195], v[208:211], v[0:3], 0\n"
"s_waitcnt lgkmcnt(6)\n"
"v_mfma_f32_16x16x32_bf16 v[192:195], v[212:215], v[4:7], v[192:195]\n"
"s_waitcnt lgkmcnt(5)\n"
"v_mfma_f32_16x16x32_bf16 v[192:195], v[216:219], v[8:11], v[192:195]\n"
"s_waitcnt lgkmcnt(4)\n"
"v_mfma_f32_16x16x32_bf16 v[192:195], v[220:223], v[12:15], v[192:195]\n"
"v_mfma_f32_16x16x32_bf16 v[196:199], v[208:211], v[32:35], 0\n"
"v_mfma_f32_16x16x32_bf16 v[196:199], v[212:215], v[36:39], v[196:199]\n"
"v_mfma_f32_16x16x32_bf16 v[196:199], v[216:219], v[40:43], v[196:199]\n"
"v_mfma_f32_16x16x32_bf16 v[196:199], v[220:223], v[44:47], v[196:199]\n"
"s_waitcnt lgkmcnt(3)\n"
"v_mfma_f32_16x16x32_bf16 v[192:195], v[224:227], v[16:19], v[192:195]\n"
"s_waitcnt lgkmcnt(2)\n"
"v_mfma_f32_16x16x32_bf16 v[192:195], v[228:231], v[20:23], v[192:195]\n"
"s_waitcnt lgkmcnt(1)\n"
"v_mfma_f32_16x16x32_bf16 v[192:195], v[232:235], v[24:27], v[192:195]\n"
"s_waitcnt lgkmcnt(0)\n"
"v_mfma_f32_16x16x32_bf16 v[192:195], v[236:239], v[28:31], v[192:195]\n"
"s_nop 1\n"
"v_mfma_f32_16x16x32_bf16 v[196:199], v[224:227], v[48:51], v[196:199]\n"
"s_nop 1\n"
"v_mfma_f32_16x16x32_bf16 v[196:199], v[228:231], v[52:55], v[196:199]\n"
"s_nop 1\n"
"v_mfma_f32_16x16x32_bf16 v[196:199], v[232:235], v[56:59], v[196:199]\n"
"v_sub_f32 v192, v192, v252\n"
"v_sub_f32 v193, v193, v252\n"
"v_mfma_f32_16x16x32_bf16 v[196:199], v[236:239], v[60:63], v[196:199]\n"
"v_sub_f32 v194, v194, v252\n"
"v_sub_f32 v195, v195, v252\n"
"v_mfma_f32_16x16x32_bf16 v[200:203], v[208:211], v[64:67], 0\n"
"v_exp_f32 v192, v192\n"
"v_mfma_f32_16x16x32_bf16 v[200:203], v[212:215], v[68:71], v[200:203]\n"
"v_exp_f32 v193, v193\n"
"v_mfma_f32_16x16x32_bf16 v[200:203], v[216:219], v[72:75], v[200:203]\n"
"v_exp_f32 v194, v194\n"
"v_mfma_f32_16x16x32_bf16 v[200:203], v[220:223], v[76:79], v[200:203]\n"
"v_exp_f32 v195, v195\n"
"v_mfma_f32_16x16x32_bf16 v[200:203], v[224:227], v[80:83], v[200:203]\n"
"v_add_f32 v240, v240, v192\n"
"v_cvt_pk_bf16_f32 v250, v192, v193\n"
"v_mfma_f32_16x16x32_bf16 v[200:203], v[228:231], v[84:87], v[200:203]\n"
"v_add_f32 v240, v240, v193\n"
"v_cvt_pk_bf16_f32 v251, v194, v195\n"
"v_mfma_f32_16x16x32_bf16 v[200:203], v[232:235], v[88:91], v[200:203]\n"
"v_add_f32 v240, v240, v194\n"
"ds_write_b64 v247, v[250:251] offset:8192\n"
"v_mfma_f32_16x16x32_bf16 v[200:203], v[236:239], v[92:95], v[200:203]\n"
"v_add_f32 v240, v240, v195\n"
"v_sub_f32 v196, v196, v253\n"
"v_sub_f32 v197, v197, v253\n"
"v_sub_f32 v198, v198, v253\n"
"v_sub_f32 v199, v199, v253\n"
"v_exp_f32 v196, v196\n"
"v_exp_f32 v197, v197\n"
"v_exp_f32 v198, v198\n"
"v_exp_f32 v199, v199\n"
"v_add_f32 v241, v241, v196\n"
"v_cvt_pk_bf16_f32 v250, v196, v197\n"
"v_add_f32 v241, v241, v197\n"
"v_cvt_pk_bf16_f32 v251, v198, v199\n"
"v_add_f32 v241, v241, v198\n"
"ds_write_b64 v247, v[250:251] offset:10240\n"
"v_add_f32 v241, v241, v199\n"
"v_sub_f32 v200, v200, v254\n"
"v_sub_f32 v201, v201, v254\n"
"v_sub_f32 v202, v202, v254\n"
"v_sub_f32 v203, v203, v254\n"
"v_exp_f32 v200, v200\n"
"v_exp_f32 v201, v201\n"
"v_exp_f32 v202, v202\n"
"v_exp_f32 v203, v203\n"
"v_add_f32 v242, v242, v200\n"
"v_cvt_pk_bf16_f32 v250, v200, v201\n"
"v_add_f32 v242, v242, v201\n"
"v_cvt_pk_bf16_f32 v251, v202, v203\n"
"v_add_f32 v242, v242, v202\n"
"ds_write_b64 v247, v[250:251] offset:12288\n"
"v_add_f32 v242, v242, v203\n"
"s_add_u32 m0, s62, 104448\n"
"s_nop 0\n"
"buffer_load_dwordx4 v96, s[52:55], s70 offen nt lds\n"
"s_add_u32 m0, s63, 104448\n"
"s_nop 0\n"
"buffer_load_dwordx4 v96, s[52:55], s71 offen nt lds\n"
"v_add_u32 v96, 32768, v96\n"
"s_setprio 0\n"
"s_waitcnt vmcnt(2) lgkmcnt(0)\n"
"s_barrier\n"
"ds_read_b128 v[208:211], v248 offset:8192\n"
"ds_read_b64_tr_b16 v[192:193], v245 offset:0\n"
"ds_read_b64_tr_b16 v[194:195], v245 offset:8704\n"
"ds_read_b128 v[212:215], v248 offset:9216\n"
"ds_read_b64_tr_b16 v[196:197], v245 offset:17408\n"
"ds_read_b64_tr_b16 v[198:199], v245 offset:26112\n"
"ds_read_b128 v[216:219], v248 offset:10240\n"
"ds_read_b128 v[220:223], v248 offset:11264\n"
"ds_read_b128 v[224:227], v248 offset:12288\n"
"ds_read_b128 v[228:231], v248 offset:13312\n"
"s_add_u32 m0, s56, 34816\n"
"ds_read_b64_tr_b16 v[200:201], v246 offset:0\n"
"buffer_load_dwordx4 v249, s[44:47], s64 offen nt lds\n"
"s_add_u32 m0, s57, 34816\n"
"ds_read_b64_tr_b16 v[202:203], v246 offset:8704\n"
"buffer_load_dwordx4 v249, s[44:47], s65 offen nt lds\n"
"s_add_u32 m0, s58, 34816\n"
"ds_read_b64_tr_b16 v[204:205], v246 offset:17408\n"
"buffer_load_dwordx4 v249, s[44:47], s66 offen nt lds\n"
"s_add_u32 m0, s59, 34816\n"
"ds_read_b64_tr_b16 v[206:207], v246 offset:26112\n"
"buffer_load_dwordx4 v249, s[44:47], s67 offen nt lds\n"
"s_add_u32 m0, s60, 34816\n"
"s_nop 0\n"
"buffer_load_dwordx4 v249, s[44:47], s68 offen nt lds\n"
"s_add_u32 m0, s61, 34816\n"
"s_nop 0\n"
"buffer_load_dwordx4 v249, s[44:47], s69 offen nt lds\n"
"s_add_u32 m0, s62, 34816\n"
"s_nop 0\n"
"buffer_load_dwordx4 v249, s[44:47], s70 offen nt lds\n"
"s_add_u32 m0, s63, 34816\n"
"s_nop 0\n"
"buffer_load_dwordx4 v249, s[44:47], s71 offen nt lds\n"
"v_add_u32 v249, 32768, v249\n"
"s_waitcnt lgkmcnt(11)\n"
"v_mfma_f32_16x16x32_bf16 v[128:131], v[192:195], v[208:211], v[128:131]\n"
"s_waitcnt lgkmcnt(8)\n"
"v_mfma_f32_16x16x32_bf16 v[128:131], v[196:199], v[212:215], v[128:131]\n"
"s_waitcnt lgkmcnt(7)\n"
"v_mfma_f32_16x16x32_bf16 v[144:147], v[192:195], v[216:219], v[144:147]\n"
"s_waitcnt lgkmcnt(6)\n"
"v_mfma_f32_16x16x32_bf16 v[144:147], v[196:199], v[220:223], v[144:147]\n"
"s_waitcnt lgkmcnt(5)\n"
"v_mfma_f32_16x16x32_bf16 v[160:163], v[192:195], v[224:227], v[160:163]\n"
"s_waitcnt lgkmcnt(4)\n"
"v_mfma_f32_16x16x32_bf16 v[160:163], v[196:199], v[228:231], v[160:163]\n"
"ds_read_b64_tr_b16 v[192:193], v245 offset:64\n"
"ds_read_b64_tr_b16 v[194:195], v245 offset:8768\n"
"ds_read_b64_tr_b16 v[196:197], v245 offset:17472\n"
"ds_read_b64_tr_b16 v[198:199], v245 offset:26176\n"
"s_waitcnt lgkmcnt(6)\n"
"v_mfma_f32_16x16x32_bf16 v[132:135], v[200:203], v[208:211], v[132:135]\n"
"s_waitcnt lgkmcnt(4)\n"
"v_mfma_f32_16x16x32_bf16 v[132:135], v[204:207], v[212:215], v[132:135]\n"
"v_mfma_f32_16x16x32_bf16 v[148:151], v[200:203], v[216:219], v[148:151]\n"
"v_mfma_f32_16x16x32_bf16 v[148:151], v[204:207], v[220:223], v[148:151]\n"
"v_mfma_f32_16x16x32_bf16 v[164:167], v[200:203], v[224:227], v[164:167]\n"
"v_mfma_f32_16x16x32_bf16 v[164:167], v[204:207], v[228:231], v[164:167]\n"
"ds_read_b64_tr_b16 v[200:201], v246 offset:64\n"
"ds_read_b64_tr_b16 v[202:203], v246 offset:8768\n"
"ds_read_b64_tr_b16 v[204:205], v246 offset:17472\n"
"ds_read_b64_tr_b16 v[206:207], v246 offset:26176\n"
"s_waitcnt lgkmcnt(6)\n"
"v_mfma_f32_16x16x32_bf16 v[136:139], v[192:195], v[208:211], v[136:139]\n"
"s_waitcnt lgkmcnt(4)\n"
"v_mfma_f32_16x16x32_bf16 v[136:139], v[196:199], v[212:215], v[136:139]\n"
"v_mfma_f32_16x16x32_bf16 v[152:155], v[192:195], v[216:219], v[152:155]\n"
"v_mfma_f32_16x16x32_bf16 v[152:155], v[196:199], v[220:223], v[152:155]\n"
"v_mfma_f32_16x16x32_bf16 v[168:171], v[192:195], v[224:227], v[168:171]\n"
"v_mfma_f32_16x16x32_bf16 v[168:171], v[196:199], v[228:231], v[168:171]\n"
"s_waitcnt lgkmcnt(2)\n"
"v_mfma_f32_16x16x32_bf16 v[140:143], v[200:203], v[208:211], v[140:143]\n"
"s_waitcnt lgkmcnt(0)\n"
"v_mfma_f32_16x16x32_bf16 v[140:143], v[204:207], v[212:215], v[140:143]\n"
"v_mfma_f32_16x16x32_bf16 v[156:159], v[200:203], v[216:219], v[156:159]\n"
"v_mfma_f32_16x16x32_bf16 v[156:159], v[204:207], v[220:223], v[156:159]\n"
"s_waitcnt lgkmcnt(0)\n"
"s_waitcnt vmcnt(8)\n"
"s_barrier\n"
"s_setprio 1\n"
"v_mfma_f32_16x16x32_bf16 v[172:175], v[200:203], v[224:227], v[172:175]\n"
"ds_read_b128 v[208:211], v244 offset:0\n"
"v_mfma_f32_16x16x32_bf16 v[172:175], v[204:207], v[228:231], v[172:175]\n"
"ds_read_b128 v[212:215], v244 offset:64\n"
"ds_read_b128 v[216:219], v244 offset:128\n"
"ds_read_b128 v[220:223], v244 offset:192\n"
"ds_read_b128 v[224:227], v244 offset:256\n"
"ds_read_b128 v[228:231], v244 offset:320\n"
"ds_read_b128 v[232:235], v244 offset:384\n"
"ds_read_b128 v[236:239], v244 offset:448\n"
"s_waitcnt lgkmcnt(7)\n"
"v_mfma_f32_16x16x32_bf16 v[192:195], v[208:211], v[0:3], 0\n"
"s_waitcnt lgkmcnt(6)\n"
"v_mfma_f32_16x16x32_bf16 v[192:195], v[212:215], v[4:7], v[192:195]\n"
"s_waitcnt lgkmcnt(5)\n"
"v_mfma_f32_16x16x32_bf16 v[192:195], v[216:219], v[8:11], v[192:195]\n"
"s_waitcnt lgkmcnt(4)\n"
"v_mfma_f32_16x16x32_bf16 v[192:195], v[220:223], v[12:15], v[192:195]\n"
"v_mfma_f32_16x16x32_bf16 v[196:199], v[208:211], v[32:35], 0\n"
"v_mfma_f32_16x16x32_bf16 v[196:199], v[212:215], v[36:39], v[196:199]\n"
"v_mfma_f32_16x16x32_bf16 v[196:199], v[216:219], v[40:43], v[196:199]\n"
"v_mfma_f32_16x16x32_bf16 v[196:199], v[220:223], v[44:47], v[196:199]\n"
"s_waitcnt lgkmcnt(3)\n"
"v_mfma_f32_16x16x32_bf16 v[192:195], v[224:227], v[16:19], v[192:195]\n"
"s_waitcnt lgkmcnt(2)\n"
"v_mfma_f32_16x16x32_bf16 v[192:195], v[228:231], v[20:23], v[192:195]\n"
"s_waitcnt lgkmcnt(1)\n"
"v_mfma_f32_16x16x32_bf16 v[192:195], v[232:235], v[24:27], v[192:195]\n"
"s_waitcnt lgkmcnt(0)\n"
"v_mfma_f32_16x16x32_bf16 v[192:195], v[236:239], v[28:31], v[192:195]\n"
"s_nop 1\n"
"v_mfma_f32_16x16x32_bf16 v[196:199], v[224:227], v[48:51], v[196:199]\n"
"s_nop 1\n"
"v_mfma_f32_16x16x32_bf16 v[196:199], v[228:231], v[52:55], v[196:199]\n"
"s_nop 1\n"
"v_mfma_f32_16x16x32_bf16 v[196:199], v[232:235], v[56:59], v[196:199]\n"
"v_sub_f32 v192, v192, v252\n"
"v_sub_f32 v193, v193, v252\n"
"v_mfma_f32_16x16x32_bf16 v[196:199], v[236:239], v[60:63], v[196:199]\n"
"v_sub_f32 v194, v194, v252\n"
"v_sub_f32 v195, v195, v252\n"
"v_mfma_f32_16x16x32_bf16 v[200:203], v[208:211], v[64:67], 0\n"
"v_exp_f32 v192, v192\n"
"v_mfma_f32_16x16x32_bf16 v[200:203], v[212:215], v[68:71], v[200:203]\n"
"v_exp_f32 v193, v193\n"
"v_mfma_f32_16x16x32_bf16 v[200:203], v[216:219], v[72:75], v[200:203]\n"
"v_exp_f32 v194, v194\n"
"v_mfma_f32_16x16x32_bf16 v[200:203], v[220:223], v[76:79], v[200:203]\n"
"v_exp_f32 v195, v195\n"
"v_mfma_f32_16x16x32_bf16 v[200:203], v[224:227], v[80:83], v[200:203]\n"
"v_add_f32 v240, v240, v192\n"
"v_cvt_pk_bf16_f32 v250, v192, v193\n"
"v_mfma_f32_16x16x32_bf16 v[200:203], v[228:231], v[84:87], v[200:203]\n"
"v_add_f32 v240, v240, v193\n"
"v_cvt_pk_bf16_f32 v251, v194, v195\n"
"v_mfma_f32_16x16x32_bf16 v[200:203], v[232:235], v[88:91], v[200:203]\n"
"v_add_f32 v240, v240, v194\n"
"ds_write_b64 v247, v[250:251] offset:8192\n"
"v_mfma_f32_16x16x32_bf16 v[200:203], v[236:239], v[92:95], v[200:203]\n"
"v_add_f32 v240, v240, v195\n"
"v_sub_f32 v196, v196, v253\n"
"v_sub_f32 v197, v197, v253\n"
"v_sub_f32 v198, v198, v253\n"
"v_sub_f32 v199, v199, v253\n"
"v_exp_f32 v196, v196\n"
"v_exp_f32 v197, v197\n"
"v_exp_f32 v198, v198\n"
"v_exp_f32 v199, v199\n"
"v_add_f32 v241, v241, v196\n"
"v_cvt_pk_bf16_f32 v250, v196, v197\n"
"v_add_f32 v241, v241, v197\n"
"v_cvt_pk_bf16_f32 v251, v198, v199\n"
"v_add_f32 v241, v241, v198\n"
"ds_write_b64 v247, v[250:251] offset:10240\n"
"v_add_f32 v241, v241, v199\n"
"v_sub_f32 v200, v200, v254\n"
"v_sub_f32 v201, v201, v254\n"
"v_sub_f32 v202, v202, v254\n"
"v_sub_f32 v203, v203, v254\n"
"v_exp_f32 v200, v200\n"
"v_exp_f32 v201, v201\n"
"v_exp_f32 v202, v202\n"
"v_exp_f32 v203, v203\n"
"v_add_f32 v242, v242, v200\n"
"v_cvt_pk_bf16_f32 v250, v200, v201\n"
"v_add_f32 v242, v242, v201\n"
"v_cvt_pk_bf16_f32 v251, v202, v203\n"
"v_add_f32 v242, v242, v202\n"
"ds_write_b64 v247, v[250:251] offset:12288\n"
"v_add_f32 v242, v242, v203\n"
"s_add_u32 m0, s62, 69632\n"
"s_nop 0\n"
"buffer_load_dwordx4 v96, s[52:55], s70 offen nt lds\n"
"s_add_u32 m0, s63, 69632\n"
"s_nop 0\n"
"buffer_load_dwordx4 v96, s[52:55], s71 offen nt lds\n"
"v_add_u32 v96, 32768, v96\n"
"s_setprio 0\n"
"s_waitcnt vmcnt(2) lgkmcnt(0)\n"
"s_barrier\n"
"ds_read_b128 v[208:211], v248 offset:8192\n"
"ds_read_b64_tr_b16 v[192:193], v245 offset:34816\n"
"ds_read_b64_tr_b16 v[194:195], v245 offset:43520\n"
"ds_read_b128 v[212:215], v248 offset:9216\n"
"ds_read_b64_tr_b16 v[196:197], v245 offset:52224\n"
"ds_read_b64_tr_b16 v[198:199], v245 offset:60928\n"
"ds_read_b128 v[216:219], v248 offset:10240\n"
"ds_read_b128 v[220:223], v248 offset:11264\n"
"ds_read_b128 v[224:227], v248 offset:12288\n"
"ds_read_b128 v[228:231], v248 offset:13312\n"
"s_add_u32 m0, s56, 0\n"
"ds_read_b64_tr_b16 v[200:201], v246 offset:34816\n"
"buffer_load_dwordx4 v249, s[44:47], s64 offen nt lds\n"
"s_add_u32 m0, s57, 0\n"
"ds_read_b64_tr_b16 v[202:203], v246 offset:43520\n"
"buffer_load_dwordx4 v249, s[44:47], s65 offen nt lds\n"
"s_add_u32 m0, s58, 0\n"
"ds_read_b64_tr_b16 v[204:205], v246 offset:52224\n"
"buffer_load_dwordx4 v249, s[44:47], s66 offen nt lds\n"
"s_add_u32 m0, s59, 0\n"
"ds_read_b64_tr_b16 v[206:207], v246 offset:60928\n"
"buffer_load_dwordx4 v249, s[44:47], s67 offen nt lds\n"
"s_add_u32 m0, s60, 0\n"
"s_nop 0\n"
"buffer_load_dwordx4 v249, s[44:47], s68 offen nt lds\n"
"s_add_u32 m0, s61, 0\n"
"s_nop 0\n"
"buffer_load_dwordx4 v249, s[44:47], s69 offen nt lds\n"
"s_add_u32 m0, s62, 0\n"
"s_nop 0\n"
"buffer_load_dwordx4 v249, s[44:47], s70 offen nt lds\n"
"s_add_u32 m0, s63, 0\n"
"s_nop 0\n"
"buffer_load_dwordx4 v249, s[44:47], s71 offen nt lds\n"
"v_add_u32 v249, 32768, v249\n"
"s_waitcnt lgkmcnt(11)\n"
"v_mfma_f32_16x16x32_bf16 v[128:131], v[192:195], v[208:211], v[128:131]\n"
"s_waitcnt lgkmcnt(8)\n"
"v_mfma_f32_16x16x32_bf16 v[128:131], v[196:199], v[212:215], v[128:131]\n"
"s_waitcnt lgkmcnt(7)\n"
"v_mfma_f32_16x16x32_bf16 v[144:147], v[192:195], v[216:219], v[144:147]\n"
"s_waitcnt lgkmcnt(6)\n"
"v_mfma_f32_16x16x32_bf16 v[144:147], v[196:199], v[220:223], v[144:147]\n"
"s_waitcnt lgkmcnt(5)\n"
"v_mfma_f32_16x16x32_bf16 v[160:163], v[192:195], v[224:227], v[160:163]\n"
"s_waitcnt lgkmcnt(4)\n"
"v_mfma_f32_16x16x32_bf16 v[160:163], v[196:199], v[228:231], v[160:163]\n"
"ds_read_b64_tr_b16 v[192:193], v245 offset:34880\n"
"ds_read_b64_tr_b16 v[194:195], v245 offset:43584\n"
"ds_read_b64_tr_b16 v[196:197], v245 offset:52288\n"
"ds_read_b64_tr_b16 v[198:199], v245 offset:60992\n"
"s_waitcnt lgkmcnt(6)\n"
"v_mfma_f32_16x16x32_bf16 v[132:135], v[200:203], v[208:211], v[132:135]\n"
"s_waitcnt lgkmcnt(4)\n"
"v_mfma_f32_16x16x32_bf16 v[132:135], v[204:207], v[212:215], v[132:135]\n"
"v_mfma_f32_16x16x32_bf16 v[148:151], v[200:203], v[216:219], v[148:151]\n"
"v_mfma_f32_16x16x32_bf16 v[148:151], v[204:207], v[220:223], v[148:151]\n"
"v_mfma_f32_16x16x32_bf16 v[164:167], v[200:203], v[224:227], v[164:167]\n"
"v_mfma_f32_16x16x32_bf16 v[164:167], v[204:207], v[228:231], v[164:167]\n"
"ds_read_b64_tr_b16 v[200:201], v246 offset:34880\n"
"ds_read_b64_tr_b16 v[202:203], v246 offset:43584\n"
"ds_read_b64_tr_b16 v[204:205], v246 offset:52288\n"
"ds_read_b64_tr_b16 v[206:207], v246 offset:60992\n"
"s_waitcnt lgkmcnt(6)\n"
"v_mfma_f32_16x16x32_bf16 v[136:139], v[192:195], v[208:211], v[136:139]\n"
"s_waitcnt lgkmcnt(4)\n"
"v_mfma_f32_16x16x32_bf16 v[136:139], v[196:199], v[212:215], v[136:139]\n"
"v_mfma_f32_16x16x32_bf16 v[152:155], v[192:195], v[216:219], v[152:155]\n"
"v_mfma_f32_16x16x32_bf16 v[152:155], v[196:199], v[220:223], v[152:155]\n"
"v_mfma_f32_16x16x32_bf16 v[168:171], v[192:195], v[224:227], v[168:171]\n"
"v_mfma_f32_16x16x32_bf16 v[168:171], v[196:199], v[228:231], v[168:171]\n"
"s_waitcnt lgkmcnt(2)\n"
"v_mfma_f32_16x16x32_bf16 v[140:143], v[200:203], v[208:211], v[140:143]\n"
"s_waitcnt lgkmcnt(0)\n"
"v_mfma_f32_16x16x32_bf16 v[140:143], v[204:207], v[212:215], v[140:143]\n"
"v_mfma_f32_16x16x32_bf16 v[156:159], v[200:203], v[216:219], v[156:159]\n"
"v_mfma_f32_16x16x32_bf16 v[156:159], v[204:207], v[220:223], v[156:159]\n"
"s_waitcnt lgkmcnt(0)\n"
"s_waitcnt vmcnt(8)\n"
"s_barrier\n"
"s_sub_u32 s83, s83, 1\n"
"s_cmp_lg_u32 s83, 0\n"
"s_cbranch_scc1 L_loopBs\n"
"s_setprio 1\n"
"v_mfma_f32_16x16x32_bf16 v[172:175], v[200:203], v[224:227], v[172:175]\n"
"ds_read_b128 v[208:211], v244 offset:34816\n"
"v_mfma_f32_16x16x32_bf16 v[172:175], v[204:207], v[228:231], v[172:175]\n"
"ds_read_b128 v[212:215], v244 offset:34880\n"
"ds_read_b128 v[216:219], v244 offset:34944\n"
"ds_read_b128 v[220:223], v244 offset:35008\n"
"ds_read_b128 v[224:227], v244 offset:35072\n"
"ds_read_b128 v[228:231], v244 offset:35136\n"
"ds_read_b128 v[232:235], v244 offset:35200\n"
"ds_read_b128 v[236:239], v244 offset:35264\n"
"s_waitcnt lgkmcnt(7)\n"
"v_mfma_f32_16x16x32_bf16 v[192:195], v[208:211], v[0:3], 0\n"
"s_waitcnt lgkmcnt(6)\n"
"v_mfma_f32_16x16x32_bf16 v[192:195], v[212:215], v[4:7], v[192:195]\n"
"s_waitcnt lgkmcnt(5)\n"
"v_mfma_f32_16x16x32_bf16 v[192:195], v[216:219], v[8:11], v[192:195]\n"
"s_waitcnt lgkmcnt(4)\n"
"v_mfma_f32_16x16x32_bf16 v[192:195], v[220:223], v[12:15], v[192:195]\n"
"v_mfma_f32_16x16x32_bf16 v[196:199], v[208:211], v[32:35], 0\n"
"v_mfma_f32_16x16x32_bf16 v[196:199], v[212:215], v[36:39], v[196:199]\n"
"v_mfma_f32_16x16x32_bf16 v[196:199], v[216:219], v[40:43], v[196:199]\n"
"v_mfma_f32_16x16x32_bf16 v[196:199], v[220:223], v[44:47], v[196:199]\n"
"s_waitcnt lgkmcnt(3)\n"
"v_mfma_f32_16x16x32_bf16 v[192:195], v[224:227], v[16:19], v[192:195]\n"
"s_waitcnt lgkmcnt(2)\n"
"v_mfma_f32_16x16x32_bf16 v[192:195], v[228:231], v[20:23], v[192:195]\n"
"s_waitcnt lgkmcnt(1)\n"
"v_mfma_f32_16x16x32_bf16 v[192:195], v[232:235], v[24:27], v[192:195]\n"
"s_waitcnt lgkmcnt(0)\n"
"v_mfma_f32_16x16x32_bf16 v[192:195], v[236:239], v[28:31], v[192:195]\n"
"s_nop 1\n"
"v_mfma_f32_16x16x32_bf16 v[196:199], v[224:227], v[48:51], v[196:199]\n"
"s_nop 1\n"
"v_mfma_f32_16x16x32_bf16 v[196:199], v[228:231], v[52:55], v[196:199]\n"
"s_nop 1\n"
"v_mfma_f32_16x16x32_bf16 v[196:199], v[232:235], v[56:59], v[196:199]\n"
"v_sub_f32 v192, v192, v252\n"
"v_sub_f32 v193, v193, v252\n"
"v_mfma_f32_16x16x32_bf16 v[196:199], v[236:239], v[60:63], v[196:199]\n"
"v_sub_f32 v194, v194, v252\n"
"v_sub_f32 v195, v195, v252\n"
"v_mfma_f32_16x16x32_bf16 v[200:203], v[208:211], v[64:67], 0\n"
"v_exp_f32 v192, v192\n"
"v_mfma_f32_16x16x32_bf16 v[200:203], v[212:215], v[68:71], v[200:203]\n"
"v_exp_f32 v193, v193\n"
"v_mfma_f32_16x16x32_bf16 v[200:203], v[216:219], v[72:75], v[200:203]\n"
"v_exp_f32 v194, v194\n"
"v_mfma_f32_16x16x32_bf16 v[200:203], v[220:223], v[76:79], v[200:203]\n"
"v_exp_f32 v195, v195\n"
"v_mfma_f32_16x16x32_bf16 v[200:203], v[224:227], v[80:83], v[200:203]\n"
"v_add_f32 v240, v240, v192\n"
"v_cvt_pk_bf16_f32 v250, v192, v193\n"
"v_mfma_f32_16x16x32_bf16 v[200:203], v[228:231], v[84:87], v[200:203]\n"
"v_add_f32 v240, v240, v193\n"
"v_cvt_pk_bf16_f32 v251, v194, v195\n"
"v_mfma_f32_16x16x32_bf16 v[200:203], v[232:235], v[88:91], v[200:203]\n"
"v_add_f32 v240, v240, v194\n"
"ds_write_b64 v247, v[250:251] offset:8192\n"
"v_mfma_f32_16x16x32_bf16 v[200:203], v[236:239], v[92:95], v[200:203]\n"
"v_add_f32 v240, v240, v195\n"
"v_sub_f32 v196, v196, v253\n"
"v_sub_f32 v197, v197, v253\n"
"v_sub_f32 v198, v198, v253\n"
"v_sub_f32 v199, v199, v253\n"
"v_exp_f32 v196, v196\n"
"v_exp_f32 v197, v197\n"
"v_exp_f32 v198, v198\n"
"v_exp_f32 v199, v199\n"
"v_add_f32 v241, v241, v196\n"
"v_cvt_pk_bf16_f32 v250, v196, v197\n"
"v_add_f32 v241, v241, v197\n"
"v_cvt_pk_bf16_f32 v251, v198, v199\n"
"v_add_f32 v241, v241, v198\n"
"ds_write_b64 v247, v[250:251] offset:10240\n"
"v_add_f32 v241, v241, v199\n"
"v_sub_f32 v200, v200, v254\n"
"v_sub_f32 v201, v201, v254\n"
"v_sub_f32 v202, v202, v254\n"
"v_sub_f32 v203, v203, v254\n"
"v_exp_f32 v200, v200\n"
"v_exp_f32 v201, v201\n"
"v_exp_f32 v202, v202\n"
"v_exp_f32 v203, v203\n"
"v_add_f32 v242, v242, v200\n"
"v_cvt_pk_bf16_f32 v250, v200, v201\n"
"v_add_f32 v242, v242, v201\n"
"v_cvt_pk_bf16_f32 v251, v202, v203\n"
"v_add_f32 v242, v242, v202\n"
"ds_write_b64 v247, v[250:251] offset:12288\n"
"v_add_f32 v242, v242, v203\n"
"s_add_u32 m0, s62, 104448\n"
"s_nop 0\n"
"buffer_load_dwordx4 v96, s[52:55], s70 offen nt lds\n"
"s_add_u32 m0, s63, 104448\n"
"s_nop 0\n"
"buffer_load_dwordx4 v96, s[52:55], s71 offen nt lds\n"
"v_add_u32 v96, 32768, v96\n"
"s_setprio 0\n"
"s_waitcnt vmcnt(2) lgkmcnt(0)\n"
"s_barrier\n"
"ds_read_b128 v[208:211], v248 offset:8192\n"
"ds_read_b64_tr_b16 v[192:193], v245 offset:0\n"
"ds_read_b64_tr_b16 v[194:195], v245 offset:8704\n"
"ds_read_b128 v[212:215], v248 offset:9216\n"
"ds_read_b64_tr_b16 v[196:197], v245 offset:17408\n"
"ds_read_b64_tr_b16 v[198:199], v245 offset:26112\n"
"ds_read_b128 v[216:219], v248 offset:10240\n"
"ds_read_b128 v[220:223], v248 offset:11264\n"
"ds_read_b128 v[224:227], v248 offset:12288\n"
"ds_read_b128 v[228:231], v248 offset:13312\n"
"v_mbcnt_lo_u32_b32 v100, -1, 0\n"
"v_mbcnt_hi_u32_b32 v100, -1, v100\n"
"v_lshrrev_b32 v98, 5, v100\n"
"v_and_b32 v100, 31, v100\n"
"v_lshlrev_b32 v99, 1, v98\n"
"v_xor_b32 v100, v100, v99\n"
"v_mul_u32_u24 v98, 896, v98\n"
"v_lshrrev_b32 v99, 3, v100\n"
"v_mul_u32_u24 v99, 224, v99\n"
"v_add_u32 v98, v98, v99\n"
"v_and_b32 v100, 7, v100\n"
"v_lshl_add_u32 v100, v100, 4, v98\n"
"s_add_u32 m0, s56, 34816\n"
"ds_read_b64_tr_b16 v[200:201], v246 offset:0\n"
"buffer_load_dwordx4 v100, s[48:51], s36 offen lds\n"
"s_add_u32 m0, s57, 34816\n"
"ds_read_b64_tr_b16 v[202:203], v246 offset:8704\n"
"buffer_load_dwordx4 v100, s[48:51], s37 offen lds\n"
"s_add_u32 m0, s58, 34816\n"
"ds_read_b64_tr_b16 v[204:205], v246 offset:17408\n"
"buffer_load_dwordx4 v100, s[48:51], s38 offen lds\n"
"s_add_u32 m0, s59, 34816\n"
"ds_read_b64_tr_b16 v[206:207], v246 offset:26112\n"
"buffer_load_dwordx4 v100, s[48:51], s39 offen lds\n"
"s_add_u32 m0, s60, 34816\n"
"s_nop 0\n"
"buffer_load_dwordx4 v100, s[48:51], s40 offen lds\n"
"s_add_u32 m0, s61, 34816\n"
"s_nop 0\n"
"buffer_load_dwordx4 v100, s[48:51], s41 offen lds\n"
"s_add_u32 m0, s62, 34816\n"
"s_nop 0\n"
"buffer_load_dwordx4 v100, s[48:51], s42 offen lds\n"
"s_add_u32 m0, s63, 34816\n"
"s_nop 0\n"
"buffer_load_dwordx4 v100, s[48:51], s43 offen lds\n"
"s_waitcnt lgkmcnt(11)\n"
"v_mfma_f32_16x16x32_bf16 v[128:131], v[192:195], v[208:211], v[128:131]\n"
"s_waitcnt lgkmcnt(8)\n"
"v_mfma_f32_16x16x32_bf16 v[128:131], v[196:199], v[212:215], v[128:131]\n"
"s_waitcnt lgkmcnt(7)\n"
"v_mfma_f32_16x16x32_bf16 v[144:147], v[192:195], v[216:219], v[144:147]\n"
"s_waitcnt lgkmcnt(6)\n"
"v_mfma_f32_16x16x32_bf16 v[144:147], v[196:199], v[220:223], v[144:147]\n"
"s_waitcnt lgkmcnt(5)\n"
"v_mfma_f32_16x16x32_bf16 v[160:163], v[192:195], v[224:227], v[160:163]\n"
"s_waitcnt lgkmcnt(4)\n"
"v_mfma_f32_16x16x32_bf16 v[160:163], v[196:199], v[228:231], v[160:163]\n"
"ds_read_b64_tr_b16 v[192:193], v245 offset:64\n"
"ds_read_b64_tr_b16 v[194:195], v245 offset:8768\n"
"ds_read_b64_tr_b16 v[196:197], v245 offset:17472\n"
"ds_read_b64_tr_b16 v[198:199], v245 offset:26176\n"
"s_waitcnt lgkmcnt(6)\n"
"v_mfma_f32_16x16x32_bf16 v[132:135], v[200:203], v[208:211], v[132:135]\n"
"s_waitcnt lgkmcnt(4)\n"
"v_mfma_f32_16x16x32_bf16 v[132:135], v[204:207], v[212:215], v[132:135]\n"
"v_mfma_f32_16x16x32_bf16 v[148:151], v[200:203], v[216:219], v[148:151]\n"
"v_mfma_f32_16x16x32_bf16 v[148:151], v[204:207], v[220:223], v[148:151]\n"
"v_mfma_f32_16x16x32_bf16 v[164:167], v[200:203], v[224:227], v[164:167]\n"
"v_mfma_f32_16x16x32_bf16 v[164:167], v[204:207], v[228:231], v[164:167]\n"
"ds_read_b64_tr_b16 v[200:201], v246 offset:64\n"
"ds_read_b64_tr_b16 v[202:203], v246 offset:8768\n"
"ds_read_b64_tr_b16 v[204:205], v246 offset:17472\n"
"ds_read_b64_tr_b16 v[206:207], v246 offset:26176\n"
"s_waitcnt lgkmcnt(6)\n"
"v_mfma_f32_16x16x32_bf16 v[136:139], v[192:195], v[208:211], v[136:139]\n"
"s_waitcnt lgkmcnt(4)\n"
"v_mfma_f32_16x16x32_bf16 v[136:139], v[196:199], v[212:215], v[136:139]\n"
"v_mfma_f32_16x16x32_bf16 v[152:155], v[192:195], v[216:219], v[152:155]\n"
"v_mfma_f32_16x16x32_bf16 v[152:155], v[196:199], v[220:223], v[152:155]\n"
"v_mfma_f32_16x16x32_bf16 v[168:171], v[192:195], v[224:227], v[168:171]\n"
"v_mfma_f32_16x16x32_bf16 v[168:171], v[196:199], v[228:231], v[168:171]\n"
"s_waitcnt lgkmcnt(2)\n"
"v_mfma_f32_16x16x32_bf16 v[140:143], v[200:203], v[208:211], v[140:143]\n"
"s_waitcnt lgkmcnt(0)\n"
"v_mfma_f32_16x16x32_bf16 v[140:143], v[204:207], v[212:215], v[140:143]\n"
"v_mfma_f32_16x16x32_bf16 v[156:159], v[200:203], v[216:219], v[156:159]\n"
"v_mfma_f32_16x16x32_bf16 v[156:159], v[204:207], v[220:223], v[156:159]\n"
"s_waitcnt lgkmcnt(0)\n"
"s_waitcnt vmcnt(8)\n"
"s_barrier\n"
"s_setprio 1\n"
"v_mfma_f32_16x16x32_bf16 v[172:175], v[200:203], v[224:227], v[172:175]\n"
"ds_read_b128 v[208:211], v244 offset:0\n"
"v_mfma_f32_16x16x32_bf16 v[172:175], v[204:207], v[228:231], v[172:175]\n"
"ds_read_b128 v[212:215], v244 offset:64\n"
"ds_read_b128 v[216:219], v244 offset:128\n"
"ds_read_b128 v[220:223], v244 offset:192\n"
"ds_read_b128 v[224:227], v244 offset:256\n"
"ds_read_b128 v[228:231], v244 offset:320\n"
"ds_read_b128 v[232:235], v244 offset:384\n"
"ds_read_b128 v[236:239], v244 offset:448\n"
"s_waitcnt lgkmcnt(7)\n"
"v_mfma_f32_16x16x32_bf16 v[192:195], v[208:211], v[0:3], 0\n"
"s_waitcnt lgkmcnt(6)\n"
"v_mfma_f32_16x16x32_bf16 v[192:195], v[212:215], v[4:7], v[192:195]\n"
"s_waitcnt lgkmcnt(5)\n"
"v_mfma_f32_16x16x32_bf16 v[192:195], v[216:219], v[8:11], v[192:195]\n"
"s_waitcnt lgkmcnt(4)\n"
"v_mfma_f32_16x16x32_bf16 v[192:195], v[220:223], v[12:15], v[192:195]\n"
"v_mfma_f32_16x16x32_bf16 v[196:199], v[208:211], v[32:35], 0\n"
"v_mfma_f32_16x16x32_bf16 v[196:199], v[212:215], v[36:39], v[196:199]\n"
"v_mfma_f32_16x16x32_bf16 v[196:199], v[216:219], v[40:43], v[196:199]\n"
"v_mfma_f32_16x16x32_bf16 v[196:199], v[220:223], v[44:47], v[196:199]\n"
"s_waitcnt lgkmcnt(3)\n"
"v_mfma_f32_16x16x32_bf16 v[192:195], v[224:227], v[16:19], v[192:195]\n"
"s_waitcnt lgkmcnt(2)\n"
"v_mfma_f32_16x16x32_bf16 v[192:195], v[228:231], v[20:23], v[192:195]\n"
"s_waitcnt lgkmcnt(1)\n"
"v_mfma_f32_16x16x32_bf16 v[192:195], v[232:235], v[24:27], v[192:195]\n"
"s_waitcnt lgkmcnt(0)\n"
"v_mfma_f32_16x16x32_bf16 v[192:195], v[236:239], v[28:31], v[192:195]\n"
"s_nop 1\n"
"v_mfma_f32_16x16x32_bf16 v[196:199], v[224:227], v[48:51], v[196:199]\n"
"s_nop 1\n"
"v_mfma_f32_16x16x32_bf16 v[196:199], v[228:231], v[52:55], v[196:199]\n"
"s_nop 1\n"
"v_mfma_f32_16x16x32_bf16 v[196:199], v[232:235], v[56:59], v[196:199]\n"
"v_sub_f32 v192, v192, v252\n"
"v_sub_f32 v193, v193, v252\n"
"v_mfma_f32_16x16x32_bf16 v[196:199], v[236:239], v[60:63], v[196:199]\n"
"v_sub_f32 v194, v194, v252\n"
"v_sub_f32 v195, v195, v252\n"
"v_mfma_f32_16x16x32_bf16 v[200:203], v[208:211], v[64:67], 0\n"
"v_exp_f32 v192, v192\n"
"v_mfma_f32_16x16x32_bf16 v[200:203], v[212:215], v[68:71], v[200:203]\n"
"v_exp_f32 v193, v193\n"
"v_mfma_f32_16x16x32_bf16 v[200:203], v[216:219], v[72:75], v[200:203]\n"
"v_exp_f32 v194, v194\n"
"v_mfma_f32_16x16x32_bf16 v[200:203], v[220:223], v[76:79], v[200:203]\n"
"v_exp_f32 v195, v195\n"
"v_mfma_f32_16x16x32_bf16 v[200:203], v[224:227], v[80:83], v[200:203]\n"
"v_add_f32 v240, v240, v192\n"
"v_cvt_pk_bf16_f32 v250, v192, v193\n"
"v_mfma_f32_16x16x32_bf16 v[200:203], v[228:231], v[84:87], v[200:203]\n"
"v_add_f32 v240, v240, v193\n"
"v_cvt_pk_bf16_f32 v251, v194, v195\n"
"v_mfma_f32_16x16x32_bf16 v[200:203], v[232:235], v[88:91], v[200:203]\n"
"v_add_f32 v240, v240, v194\n"
"ds_write_b64 v247, v[250:251] offset:8192\n"
"v_mfma_f32_16x16x32_bf16 v[200:203], v[236:239], v[92:95], v[200:203]\n"
"v_add_f32 v240, v240, v195\n"
"v_sub_f32 v196, v196, v253\n"
"v_sub_f32 v197, v197, v253\n"
"v_sub_f32 v198, v198, v253\n"
"v_sub_f32 v199, v199, v253\n"
"v_exp_f32 v196, v196\n"
"v_exp_f32 v197, v197\n"
"v_exp_f32 v198, v198\n"
"v_exp_f32 v199, v199\n"
"v_add_f32 v241, v241, v196\n"
"v_cvt_pk_bf16_f32 v250, v196, v197\n"
"v_add_f32 v241, v241, v197\n"
"v_cvt_pk_bf16_f32 v251, v198, v199\n"
"v_add_f32 v241, v241, v198\n"
"ds_write_b64 v247, v[250:251] offset:10240\n"
"v_add_f32 v241, v241, v199\n"
"v_sub_f32 v200, v200, v254\n"
"v_sub_f32 v201, v201, v254\n"
"v_sub_f32 v202, v202, v254\n"
"v_sub_f32 v203, v203, v254\n"
"v_exp_f32 v200, v200\n"
"v_exp_f32 v201, v201\n"
"v_exp_f32 v202, v202\n"
"v_exp_f32 v203, v203\n"
"v_add_f32 v242, v242, v200\n"
"v_cvt_pk_bf16_f32 v250, v200, v201\n"
"v_add_f32 v242, v242, v201\n"
"v_cvt_pk_bf16_f32 v251, v202, v203\n"
"v_add_f32 v242, v242, v202\n"
"ds_write_b64 v247, v[250:251] offset:12288\n"
"v_add_f32 v242, v242, v203\n"
"v_mov_b32 v208, v240\n"
"s_nop 1\n"
"v_permlane16_swap_b32 v240, v208\n"
"v_add_f32 v240, v240, v208\n"
"v_mov_b32 v208, v240\n"
"s_nop 1\n"
"v_permlane32_swap_b32 v240, v208\n"
"v_add_f32 v240, v240, v208\n"
"v_mov_b32 v209, v241\n"
"s_nop 1\n"
"v_permlane16_swap_b32 v241, v209\n"
"v_add_f32 v241, v241, v209\n"
"v_mov_b32 v209, v241\n"
"s_nop 1\n"
"v_permlane32_swap_b32 v241, v209\n"
"v_add_f32 v241, v241, v209\n"
"v_mov_b32 v210, v242\n"
"s_nop 1\n"
"v_permlane16_swap_b32 v242, v210\n"
"v_add_f32 v242, v242, v210\n"
"v_mov_b32 v210, v242\n"
"s_nop 1\n"
"v_permlane32_swap_b32 v242, v210\n"
"v_add_f32 v242, v242, v210\n"
"v_mbcnt_lo_u32_b32 v216, -1, 0\n"
"v_mbcnt_hi_u32_b32 v216, -1, v216\n"
"v_and_b32 v216, 15, v216\n"
"v_lshlrev_b32 v216, 4, v216\n"
"s_lshl_b32 s84, s87, 2\n"
"s_add_u32 s84, s84, 161792\n"
"v_add_u32 v216, s84, v216\n"
"ds_write_b32 v216, v240 offset:1024\n"
"ds_write_b32 v216, v241 offset:1280\n"
"ds_write_b32 v216, v242 offset:1536\n"
"s_setprio 0\n"
"s_waitcnt lgkmcnt(0)\n"
"s_barrier\n"
"ds_read_b128 v[208:211], v248 offset:8192\n"
"ds_read_b64_tr_b16 v[192:193], v245 offset:34816\n"
"ds_read_b64_tr_b16 v[194:195], v245 offset:43520\n"
"ds_read_b128 v[212:215], v248 offset:9216\n"
"ds_read_b64_tr_b16 v[196:197], v245 offset:52224\n"
"ds_read_b64_tr_b16 v[198:199], v245 offset:60928\n"
"ds_read_b128 v[216:219], v248 offset:10240\n"
"ds_read_b128 v[220:223], v248 offset:11264\n"
"ds_read_b128 v[224:227], v248 offset:12288\n"
"ds_read_b128 v[228:231], v248 offset:13312\n"
"v_mbcnt_lo_u32_b32 v24, -1, 0\n"
"v_mbcnt_hi_u32_b32 v24, -1, v24\n"
"v_lshrrev_b32 v22, 5, v24\n"
"v_and_b32 v24, 31, v24\n"
"v_lshlrev_b32 v23, 1, v22\n"
"v_xor_b32 v24, v24, v23\n"
"v_mul_u32_u24 v22, 896, v22\n"
"v_lshrrev_b32 v23, 3, v24\n"
"v_mul_u32_u24 v23, 224, v23\n"
"v_add_u32 v22, v22, v23\n"
"v_and_b32 v24, 7, v24\n"
"v_lshl_add_u32 v24, v24, 4, v22\n"
"v_add_u32 v24, 114688, v24\n"
"v_mbcnt_lo_u32_b32 v20, -1, 0\n"
"v_mbcnt_hi_u32_b32 v20, -1, v20\n"
"v_and_b32 v20, 15, v20\n"
"v_lshlrev_b32 v20, 4, v20\n"
"v_add_u32 v20, 161792, v20\n"
"ds_read_b128 v[0:3], v20 offset:1024\n"
"ds_read_b128 v[4:7], v20 offset:1280\n"
"ds_read_b128 v[8:11], v20 offset:1536\n"
"s_add_u32 m0, s56, 0\n"
"ds_read_b64_tr_b16 v[200:201], v246 offset:34816\n"
"buffer_load_dwordx4 v24, s[48:51], s36 offen lds\n"
"s_add_u32 m0, s57, 0\n"
"ds_read_b64_tr_b16 v[202:203], v246 offset:43520\n"
"buffer_load_dwordx4 v24, s[48:51], s37 offen lds\n"
"s_add_u32 m0, s58, 0\n"
"ds_read_b64_tr_b16 v[204:205], v246 offset:52224\n"
"buffer_load_dwordx4 v24, s[48:51], s38 offen lds\n"
"s_add_u32 m0, s59, 0\n"
"ds_read_b64_tr_b16 v[206:207], v246 offset:60928\n"
"buffer_load_dwordx4 v24, s[48:51], s39 offen lds\n"
"s_add_u32 m0, s60, 0\n"
"s_nop 0\n"
"buffer_load_dwordx4 v24, s[48:51], s40 offen lds\n"
"s_add_u32 m0, s61, 0\n"
"s_nop 0\n"
"buffer_load_dwordx4 v24, s[48:51], s41 offen lds\n"
"s_add_u32 m0, s62, 0\n"
"s_nop 0\n"
"buffer_load_dwordx4 v24, s[48:51], s42 offen lds\n"
"s_add_u32 m0, s63, 0\n"
"s_nop 0\n"
"buffer_load_dwordx4 v24, s[48:51], s43 offen lds\n"
"s_waitcnt lgkmcnt(14)\n"
"v_mfma_f32_16x16x32_bf16 v[128:131], v[192:195], v[208:211], v[128:131]\n"
"s_waitcnt lgkmcnt(11)\n"
"v_mfma_f32_16x16x32_bf16 v[128:131], v[196:199], v[212:215], v[128:131]\n"
"s_waitcnt lgkmcnt(10)\n"
"v_mfma_f32_16x16x32_bf16 v[144:147], v[192:195], v[216:219], v[144:147]\n"
"s_waitcnt lgkmcnt(9)\n"
"v_mfma_f32_16x16x32_bf16 v[144:147], v[196:199], v[220:223], v[144:147]\n"
"s_waitcnt lgkmcnt(8)\n"
"v_mfma_f32_16x16x32_bf16 v[160:163], v[192:195], v[224:227], v[160:163]\n"
"s_waitcnt lgkmcnt(7)\n"
"v_mfma_f32_16x16x32_bf16 v[160:163], v[196:199], v[228:231], v[160:163]\n"
"ds_read_b64_tr_b16 v[192:193], v245 offset:34880\n"
"ds_read_b64_tr_b16 v[194:195], v245 offset:43584\n"
"ds_read_b64_tr_b16 v[196:197], v245 offset:52288\n"
"ds_read_b64_tr_b16 v[198:199], v245 offset:60992\n"
"s_waitcnt lgkmcnt(6)\n"
"v_mfma_f32_16x16x32_bf16 v[132:135], v[200:203], v[208:211], v[132:135]\n"
"s_waitcnt lgkmcnt(4)\n"
"v_mfma_f32_16x16x32_bf16 v[132:135], v[204:207], v[212:215], v[132:135]\n"
"v_mfma_f32_16x16x32_bf16 v[148:151], v[200:203], v[216:219], v[148:151]\n"
"v_mfma_f32_16x16x32_bf16 v[148:151], v[204:207], v[220:223], v[148:151]\n"
"v_mfma_f32_16x16x32_bf16 v[164:167], v[200:203], v[224:227], v[164:167]\n"
"v_mfma_f32_16x16x32_bf16 v[164:167], v[204:207], v[228:231], v[164:167]\n"
"ds_read_b64_tr_b16 v[200:201], v246 offset:34880\n"
"ds_read_b64_tr_b16 v[202:203], v246 offset:43584\n"
"ds_read_b64_tr_b16 v[204:205], v246 offset:52288\n"
"ds_read_b64_tr_b16 v[206:207], v246 offset:60992\n"
"s_waitcnt lgkmcnt(6)\n"
"v_mfma_f32_16x16x32_bf16 v[136:139], v[192:195], v[208:211], v[136:139]\n"
"s_waitcnt lgkmcnt(4)\n"
"v_mfma_f32_16x16x32_bf16 v[136:139], v[196:199], v[212:215], v[136:139]\n"
"v_mfma_f32_16x16x32_bf16 v[152:155], v[192:195], v[216:219], v[152:155]\n"
"v_mfma_f32_16x16x32_bf16 v[152:155], v[196:199], v[220:223], v[152:155]\n"
"v_mfma_f32_16x16x32_bf16 v[168:171], v[192:195], v[224:227], v[168:171]\n"
"v_mfma_f32_16x16x32_bf16 v[168:171], v[196:199], v[228:231], v[168:171]\n"
"s_waitcnt lgkmcnt(2)\n"
"v_mfma_f32_16x16x32_bf16 v[140:143], v[200:203], v[208:211], v[140:143]\n"
"s_waitcnt lgkmcnt(0)\n"
"v_mfma_f32_16x16x32_bf16 v[140:143], v[204:207], v[212:215], v[140:143]\n"
"v_mfma_f32_16x16x32_bf16 v[156:159], v[200:203], v[216:219], v[156:159]\n"
"v_mfma_f32_16x16x32_bf16 v[156:159], v[204:207], v[220:223], v[156:159]\n"
"v_mfma_f32_16x16x32_bf16 v[172:175], v[200:203], v[224:227], v[172:175]\n"
"v_mfma_f32_16x16x32_bf16 v[172:175], v[204:207], v[228:231], v[172:175]\n"
"s_branch L_postB\n"
  :
  : "s"(x), "s"(wq), "s"(k), "s"(v), "s"(wo), "s"(gpart), "s"(flags), "s"(outp), "s"(pk)
  : "memory", "vcc", "scc", "v0", "v1", "v2", "v3", "v4", "v5", "v6", "v7", "v8", "v9", "v10", "v11", "v12", "v13", "v14", "v15", "v16", "v17", "v18", "v19", "v20", "v21", "v22", "v23", "v24", "v25", "v26", "v27", "v28", "v29", "v30", "v31", "v32", "v33", "v34", "v35", "v36", "v37", "v38", "v39", "v40", "v41", "v42", "v43", "v44", "v45", "v46", "v47", "v48", "v49", "v50", "v51", "v52", "v53", "v54", "v55", "v56", "v57", "v58", "v59", "v60", "v61", "v62", "v63", "v64", "v65", "v66", "v67", "v68", "v69", "v70", "v71", "v72", "v73", "v74", "v75", "v76", "v77", "v78", "v79", "v80", "v81", "v82", "v83", "v84", "v85", "v86", "v87", "v88", "v89", "v90", "v91", "v92", "v93", "v94", "v95", "v96", "v97", "v98", "v99", "v100", "v101", "v102", "v103", "v104", "v105", "v106", "v107", "v108", "v109", "v110", "v111", "v112", "v113", "v114", "v115", "v116", "v117", "v118", "v119", "v120", "v121", "v122", "v123", "v124", "v125", "v126", "v127", "v128", "v129", "v130", "v131", "v132", "v133", "v134", "v135", "v136", "v137", "v138", "v139", "v140", "v141", "v142", "v143", "v144", "v145", "v146", "v147", "v148", "v149", "v150", "v151", "v152", "v153", "v154", "v155", "v156", "v157", "v158", "v159", "v160", "v161", "v162", "v163", "v164", "v165", "v166", "v167", "v168", "v169", "v170", "v171", "v172", "v173", "v174", "v175", "v176", "v177", "v178", "v179", "v180", "v181", "v182", "v183", "v184", "v185", "v186", "v187", "v188", "v189", "v190", "v191", "v192", "v193", "v194", "v195", "v196", "v197", "v198", "v199", "v200", "v201", "v202", "v203", "v204", "v205", "v206", "v207", "v208", "v209", "v210", "v211", "v212", "v213", "v214", "v215", "v216", "v217", "v218", "v219", "v220", "v221", "v222", "v223", "v224", "v225", "v226", "v227", "v228", "v229", "v230", "v231", "v232", "v233", "v234", "v235", "v236", "v237", "v238", "v239", "v240", "v241", "v242", "v243", "v244", "v245", "v246", "v247", "v248", "v249", "v250", "v251", "v252", "v253", "v254", "v255", "s36", "s37", "s38", "s39", "s40", "s41", "s42", "s43", "s44", "s45", "s46", "s47", "s48", "s49", "s50", "s51", "s52", "s53", "s54", "s55", "s56", "s57", "s58", "s59", "s60", "s61", "s62", "s63", "s64", "s65", "s66", "s67", "s68", "s69", "s70", "s71", "s72", "s73", "s74", "s75", "s76", "s77", "s78", "s79", "s80", "s81", "s82", "s83", "s84", "s85", "s86", "s87", "s88", "s89", "s90", "s91", "s92", "s93", "s94", "s95");
}

extern "C" void kernel_launch(void* const* d_in, const int* in_sizes, int n_in, void* d_out, int out_size, void* d_ws, size_t ws_size,
                              hipStream_t stream) {
  attn_fused<<<256, 512, 0, stream>>>((const char*)d_in[0], (const char*)d_in[1], (const char*)d_in[2], (const char*)d_in[3], (const char*)d_in[4],
                                      (const char*)d_in[5], (const char*)d_in[6], (const char*)d_in[7], (const char*)d_in[8], (const char*)d_in[9], (char*)d_ws, (char*)d_out);
}
```
